```python
import torch

def _softmax(x, axis=-1):
    return torch.softmax(x, dim=axis)
def _gelu(x):
    return torch.nn.functional.gelu(x, approximate="tanh")
def _silu(x):
    return torch.nn.functional.silu(x)
def _sigmoid(x):
    return torch.sigmoid(x)
def _layernorm(x, axis=-1, eps=1e-5):
    m = x.mean(dim=axis, keepdim=True)
    v = x.var(dim=axis, keepdim=True, unbiased=False)
    return (x - m) / torch.sqrt(v + eps)
def _scatter_add(tgt, idx, src, axis=0):
    return tgt.clone().index_add_(axis, idx, src)
def _segment_sum(src, seg, n):
    out = torch.zeros((n,) + src.shape[1:], dtype=src.dtype)
    return out.index_add_(0, seg, src)
def _pad(x, pw):
    flat = tuple(v for pair in reversed(pw) for v in pair)
    return torch.nn.functional.pad(x, flat)

kernel_name = 'sequential_preLN__linear_kernel_hedgehog__glu_gated_glu__prompt'

def setup_inputs():

    _w = (lambda a: (lambda t: t.to(torch.bfloat16) if t.is_floating_point() else t)(torch.as_tensor(a)))
    def _A_setup():
        B, S, D, H = 1, 2048, 448, 16
        Dh = D // H
        return {
            "x": _w((lambda *s: torch.randn(*s, dtype=torch.bfloat16) * 0.05)(B, S, D)),
            "wqkv": _w((lambda *s: torch.randn(*s, dtype=torch.bfloat16) * 0.2)(D, 3 * D)),
            "w_phi": _w((lambda *s: torch.randn(*s, dtype=torch.bfloat16) * 0.2)(H, Dh, Dh) * (Dh ** -0.5)),
            "wo": _w((lambda *s: torch.randn(*s, dtype=torch.bfloat16) * 0.05)(D, D)),
        }
    _A_in = _A_setup()

    def _B_setup():
        B, S, D, F = 1, 2048, 448, 1792
        return {
            "x": _w((lambda *s: torch.randn(*s, dtype=torch.bfloat16) * 0.05)(B, S, D)),
            "w_gate": _w((lambda *s: torch.randn(*s, dtype=torch.bfloat16) * 0.05)(D, F)),
            "w_up": _w((lambda *s: torch.randn(*s, dtype=torch.bfloat16) * 0.05)(D, F)),
            "w_down": _w((lambda *s: torch.randn(*s, dtype=torch.bfloat16) * 0.05)(F, D)),
        }
    _B_in = _B_setup()

    return {
        "A_x": _A_in["x"],
        "A_wqkv": _A_in["wqkv"],
        "A_w_phi": _A_in["w_phi"],
        "A_wo": _A_in["wo"],
        "B_x": _B_in["x"],
        "B_w_gate": _B_in["w_gate"],
        "B_w_up": _B_in["w_up"],
        "B_w_down": _B_in["w_down"],
    }

def reference(A_x, A_wqkv, A_w_phi, A_wo, B_x, B_w_gate, B_w_up, B_w_down):
    def _A_ref(x, wqkv, w_phi, wo):
        B, S, D = x.shape
        H = 16; Dh = D // H
        qkv = (lambda x, *d: x.permute(*d))((x @ wqkv).reshape(B, S, 3, H, Dh), 2, 0, 3, 1, 4)
        q, k, v = qkv[0], qkv[1], qkv[2]
        pq = torch.einsum("bhsd,hdf->bhsf", q, w_phi)
        pk = torch.einsum("bhsd,hdf->bhsf", k, w_phi)
        phi_q = torch.concatenate([_softmax(pq, axis=-1), _softmax(-pq, axis=-1)], axis=-1)
        phi_k = torch.concatenate([_softmax(pk, axis=-1), _softmax(-pk, axis=-1)], axis=-1)
        kv = torch.einsum("bhsf,bhsd->bhfd", phi_k, v)
        z  = torch.einsum("bhsf->bhf", phi_k)
        num = torch.einsum("bhqf,bhfd->bhqd", phi_q, kv)
        den = torch.einsum("bhqf,bhf->bhq", phi_q, z)[..., None] + 1e-6
        o = num / den
        return (lambda x, *d: x.permute(*d))(o, 0, 2, 1, 3).reshape(B, S, D) @ wo

    def _B_ref(x, w_gate, w_up, w_down):
        g = torch.einsum("bsd,df->bsf", x, w_gate)
        u = torch.einsum("bsd,df->bsf", x, w_up)
        h = _sigmoid(g) * u
        return torch.einsum("bsf,fd->bsd", h, w_down)

    x = A_x
    h = x + _A_ref(_layernorm(x, axis=-1), A_wqkv, A_w_phi, A_wo)
    h2 = h + _B_ref(_layernorm(h, axis=-1), B_w_gate, B_w_up, B_w_down)
    return h2
```

```cpp
#include <hip/hip_runtime.h>
#include <hip/hip_bf16.h>
#include <stdint.h>

#define S_ 2048
#define D_ 448
#define H_ 16
#define DH 28
#define F_ 1792

typedef unsigned short u16;
__device__ __forceinline__ float bf2f(u16 v) { return __uint_as_float(((unsigned)v) << 16); }
__device__ __forceinline__ u16 f2bf(float f) { __hip_bfloat16 b = __float2bfloat16(f); return *(u16*)&b; }

__global__ void ln_k(const u16* x, float* out) {
  int row = blockIdx.x; int t = threadIdx.x;
  float v[7]; float s = 0;
  for (int i = 0; i < 7; i++) { v[i] = bf2f(x[row * D_ + t + 64 * i]); s += v[i]; }
  for (int o = 32; o; o >>= 1) s += __shfl_xor(s, o);
  float m = s / D_; float q = 0;
  for (int i = 0; i < 7; i++) { float d = v[i] - m; q += d * d; }
  for (int o = 32; o; o >>= 1) q += __shfl_xor(q, o);
  float r = rsqrtf(q / D_ + 1e-5f);
  for (int i = 0; i < 7; i++) out[row * D_ + t + 64 * i] = bf2f(f2bf((v[i] - m) * r));
}
__global__ void ln_kf(const float* x, float* out) {
  int row = blockIdx.x; int t = threadIdx.x;
  float v[7]; float s = 0;
  for (int i = 0; i < 7; i++) { v[i] = x[row * D_ + t + 64 * i]; s += v[i]; }
  for (int o = 32; o; o >>= 1) s += __shfl_xor(s, o);
  float m = s / D_; float q = 0;
  for (int i = 0; i < 7; i++) { float d = v[i] - m; q += d * d; }
  for (int o = 32; o; o >>= 1) q += __shfl_xor(q, o);
  float r = rsqrtf(q / D_ + 1e-5f);
  for (int i = 0; i < 7; i++) out[row * D_ + t + 64 * i] = bf2f(f2bf((v[i] - m) * r));
}
__global__ void gemm_k(const float* A, const u16* W, float* C, int N, int K) {
  int row = blockIdx.y; int n = blockIdx.x * 64 + threadIdx.x;
  if (n >= N) return;
  float acc = 0;
  for (int k = 0; k < K; k++) acc += A[row * K + k] * bf2f(W[k * N + n]);
  C[row * N + n] = acc;
}
__global__ void phi_k(const float* qkv, const u16* wphi, float* phiq, float* phik) {
  int tok = blockIdx.x; int t = threadIdx.x;
  int h = t & 15, w = t >> 4;
  const float* src = qkv + tok * 3 * D_ + w * D_ + h * DH;
  float p[DH];
  for (int f = 0; f < DH; f++) { float a = 0; for (int d = 0; d < DH; d++) a += bf2f(f2bf(src[d])) * bf2f(wphi[h * DH * DH + d * DH + f]); p[f] = a; }
  float mx = -1e30f, mn = 1e30f;
  for (int f = 0; f < DH; f++) { mx = fmaxf(mx, p[f]); mn = fminf(mn, p[f]); }
  float s1 = 0, s2 = 0;
  for (int f = 0; f < DH; f++) { s1 += __expf(p[f] - mx); s2 += __expf(mn - p[f]); }
  float* dst = (w ? phik : phiq) + (tok * H_ + h) * 56;
  for (int f = 0; f < DH; f++) { dst[f] = __expf(p[f] - mx) / s1; dst[DH + f] = __expf(mn - p[f]) / s2; }
}
__global__ void kv_k(const float* phik, const float* qkv, float* kv) {
  int h = blockIdx.x, f = blockIdx.y; int d = threadIdx.x;
  if (d > DH) return;
  float a = 0;
  for (int s = 0; s < S_; s++) { float pv = phik[(s * H_ + h) * 56 + f]; float v = d < DH ? qkv[s * 3 * D_ + 2 * D_ + h * DH + d] : 1.f; a += pv * v; }
  kv[(h * 56 + f) * 32 + d] = a;
}
__global__ void o_k(const float* phiq, const float* kv, float* o) {
  int tok = blockIdx.x; int t = threadIdx.x;
  int h = t / DH, d = t % DH;
  float num = 0, den = 0;
  for (int f = 0; f < 56; f++) { float p = phiq[(tok * H_ + h) * 56 + f]; num += p * kv[(h * 56 + f) * 32 + d]; den += p * kv[(h * 56 + f) * 32 + DH]; }
  o[tok * D_ + t] = bf2f(f2bf(num / (den + 1e-6f)));
}
__global__ void add_k(const u16* x, const float* a, float* h) { int i = blockIdx.x * 256 + threadIdx.x; h[i] = bf2f(x[i]) + a[i]; }
__global__ void glu_k(const float* g, const float* u, float* hh) { int i = blockIdx.x * 256 + threadIdx.x; hh[i] = bf2f(f2bf(u[i] / (1.f + __expf(-g[i])))); }
__global__ void out_k(const float* h, const float* a, u16* out) { int i = blockIdx.x * 256 + threadIdx.x; out[i] = f2bf(h[i] + a[i]); }

extern "C" void kernel_launch(void* const* d_in, const int* in_sizes, int n_in,
                              void* d_out, int out_size, void* d_ws, size_t ws_size,
                              hipStream_t stream) {
  const u16* x = (const u16*)d_in[0];
  const u16* wqkv = (const u16*)d_in[1];
  const u16* wphi = (const u16*)d_in[2];
  const u16* wo = (const u16*)d_in[3];
  const u16* wg = (const u16*)d_in[5];
  const u16* wu = (const u16*)d_in[6];
  const u16* wd = (const u16*)d_in[7];
  float* ws = (float*)d_ws;
  float* xn = ws; ws += S_ * D_;
  float* qkv = ws; ws += S_ * 3 * D_;
  float* phiq = ws; ws += S_ * H_ * 56;
  float* phik = ws; ws += S_ * H_ * 56;
  float* kv = ws; ws += H_ * 56 * 32;
  float* o = ws; ws += S_ * D_;
  float* att = ws; ws += S_ * D_;
  float* h = ws; ws += S_ * D_;
  float* hn = ws; ws += S_ * D_;
  float* g = ws; ws += S_ * F_;
  float* u = ws; ws += S_ * F_;
  float* hh = ws; ws += S_ * F_;
  float* dn = ws; ws += S_ * D_;
  ln_k<<<S_, 64, 0, stream>>>(x, xn);
  gemm_k<<<dim3(21, S_), 64, 0, stream>>>(xn, wqkv, qkv, 3 * D_, D_);
  phi_k<<<S_, 32, 0, stream>>>(qkv, wphi, phiq, phik);
  kv_k<<<dim3(H_, 56), 32, 0, stream>>>(phik, qkv, kv);
  o_k<<<S_, D_, 0, stream>>>(phiq, kv, o);
  gemm_k<<<dim3(7, S_), 64, 0, stream>>>(o, wo, att, D_, D_);
  add_k<<<S_ * D_ / 256, 256, 0, stream>>>(x, att, h);
  ln_kf<<<S_, 64, 0, stream>>>(h, hn);
  gemm_k<<<dim3(28, S_), 64, 0, stream>>>(hn, wg, g, F_, D_);
  gemm_k<<<dim3(28, S_), 64, 0, stream>>>(hn, wu, u, F_, D_);
  glu_k<<<S_ * F_ / 256, 256, 0, stream>>>(g, u, hh);
  gemm_k<<<dim3(7, S_), 64, 0, stream>>>(hh, wd, dn, D_, F_);
  out_k<<<S_ * D_ / 256, 256, 0, stream>>>(h, dn, (u16*)d_out);
}
```

```cpp
#include <hip/hip_runtime.h>
#include <stdint.h>

#define DEVI __device__ __forceinline__
#define LAS __attribute__((address_space(3)))
#define GAS __attribute__((address_space(1)))
typedef unsigned u32;
typedef unsigned long long u64;
typedef u32 u32x2 __attribute__((ext_vector_type(2)));
typedef u32 u32x4 __attribute__((ext_vector_type(4)));
typedef float f32x4 __attribute__((ext_vector_type(4)));
typedef __bf16 bf16x8 __attribute__((ext_vector_type(8)));
typedef __bf16 bf16x2 __attribute__((ext_vector_type(2)));
typedef short s16x4 __attribute__((ext_vector_type(4)));
typedef __amdgpu_buffer_rsrc_t rsrc_t;

constexpr int S_ = 2048, D_ = 448, F_ = 1792;
constexpr u32 WS_EPOCH = 0;
constexpr u32 WS_FL1 = 4096, WS_FL2 = WS_FL1 + 16 * 128, WS_FL3 = WS_FL2 + 16 * 128, WS_FL4 = WS_FL3 + 16 * 128;
constexpr u32 WS_KVP = 65536;
constexpr u32 WS_O = WS_KVP + 16 * 16 * 8192;
constexpr u32 WS_H = WS_O + 128 * 16 * 1024;
constexpr u32 WS_HH = WS_H + 128 * 14 * 1024;
constexpr u32 WS_END = WS_HH + 128 * 56 * 1024;
constexpr u32 WS_STAMP = WS_END;
constexpr u32 L_PHI = 135168, L_V = L_PHI + 16384, L_KVF = L_V + 8192  ;
constexpr u32 L_CS = 133120;
constexpr u32 LDS_BYTES = 163840;

DEVI f32x4 mfma(u32x4 a, u32x4 b, f32x4 c) {
  return __builtin_amdgcn_mfma_f32_16x16x32_bf16(__builtin_bit_cast(bf16x8, a), __builtin_bit_cast(bf16x8, b), c, 0, 0, 0);
}
DEVI u32 pk(float a, float b) { bf16x2 v = {(__bf16)a, (__bf16)b}; return __builtin_bit_cast(u32, v); }
DEVI float bflo(u32 v) { return __uint_as_float(v << 16); }
DEVI float bfhi(u32 v) { return __uint_as_float(v & 0xffff0000u); }
DEVI u32x2 trr(LAS char* p) { return __builtin_bit_cast(u32x2, __builtin_amdgcn_ds_read_tr16_b64_v4i16((LAS s16x4*)p)); }
DEVI u32x4 tr2(LAS char* p0, LAS char* p1) { u32x2 a = trr(p0), b = trr(p1); u32x4 r = {a[0], a[1], b[0], b[1]}; return r; }
DEVI float dot2(u32 a, u32 b, float c) {
  return __builtin_amdgcn_fdot2_f32_bf16(__builtin_bit_cast(bf16x2, a), __builtin_bit_cast(bf16x2, b), c, false);
}
DEVI float x16(float v, float& o) {
  auto r = __builtin_amdgcn_permlane16_swap(__float_as_uint(v), __float_as_uint(v), false, false);
  o = __uint_as_float(r[1]); return __uint_as_float(r[0]);
}
DEVI float x32(float v, float& o) {
  auto r = __builtin_amdgcn_permlane32_swap(__float_as_uint(v), __float_as_uint(v), false, false);
  o = __uint_as_float(r[1]); return __uint_as_float(r[0]);
}
DEVI float gsum(float v) { float b, a = x16(v, b); v = a + b; a = x32(v, b); return a + b; }
DEVI float gmax(float v) { float b, a = x16(v, b); v = fmaxf(a, b); a = x32(v, b); return fmaxf(a, b); }
DEVI float gmin(float v) { float b, a = x16(v, b); v = fminf(a, b); a = x32(v, b); return fminf(a, b); }
DEVI rsrc_t mkrs(const void* p, u32 bytes) { return __builtin_amdgcn_make_buffer_rsrc((void*)p, 0, bytes, 0x00020000); }
DEVI void dma16(rsrc_t rs, LAS char* dst, u32 voff, u32 soff) {
  __builtin_amdgcn_raw_ptr_buffer_load_lds(rs, (LAS void*)dst, 16, voff, soff, 0, 0);
}
DEVI u32x4 ld16(rsrc_t rs, u32 voff, u32 soff) { return __builtin_amdgcn_raw_buffer_load_b128(rs, voff, soff, 0); }
DEVI u32x4 ld16c(rsrc_t rs, u32 voff, u32 soff) { return __builtin_amdgcn_raw_buffer_load_b128(rs, voff, soff, 16); }
DEVI void st16c(rsrc_t rs, u32x4 v, u32 voff, u32 soff) { __builtin_amdgcn_raw_buffer_store_b128(v, rs, voff, soff, 16); }
#define WAITV0() asm volatile("s_waitcnt vmcnt(0)" ::: "memory")
#define BARRIER() do { asm volatile("s_waitcnt lgkmcnt(0)" ::: "memory"); __builtin_amdgcn_s_barrier(); asm volatile("" ::: "memory"); } while (0)
#define RLX __ATOMIC_RELAXED, __HIP_MEMORY_SCOPE_AGENT

#define STAMP(i)

DEVI void wait_flags(GAS u64* fl, int n, u64 tag, int lane, int& dead) {
  int ln = lane < n ? lane : 0;
  for (u32 spins = 0; !dead; ++spins) {
    u64 v = __hip_atomic_load(fl + ln, RLX);
    if (__all(v == tag)) break;
    if (spins > (1u << 17)) dead = 1;
    __builtin_amdgcn_s_sleep(1);
  }
}

DEVI void dual_softmax(const f32x4& p0, f32x4 p1, bool g3, u32x4& pos, u32x4& neg) {
  const float L2E = 1.4426950408889634f;
  if (g3) p1 = p0;
  float mx = fmaxf(fmaxf(fmaxf(p0[0], p0[1]), fmaxf(p0[2], p0[3])), fmaxf(fmaxf(p1[0], p1[1]), fmaxf(p1[2], p1[3])));
  float mn = fminf(fminf(fminf(p0[0], p0[1]), fminf(p0[2], p0[3])), fminf(fminf(p1[0], p1[1]), fminf(p1[2], p1[3])));
  mx = gmax(mx); mn = gmin(mn);
  float a0[4], a1[4], b0[4], b1[4];
  float sa = 0.f, sb = 0.f;
#pragma unroll
  for (int r = 0; r < 4; ++r) {
    a0[r] = __builtin_amdgcn_exp2f((p0[r] - mx) * L2E);
    b0[r] = __builtin_amdgcn_exp2f((mn - p0[r]) * L2E);
    a1[r] = g3 ? 0.f : __builtin_amdgcn_exp2f((p1[r] - mx) * L2E);
    b1[r] = g3 ? 0.f : __builtin_amdgcn_exp2f((mn - p1[r]) * L2E);
    sa += a0[r] + a1[r]; sb += b0[r] + b1[r];
  }
  sa = __builtin_amdgcn_rcpf(gsum(sa)); sb = __builtin_amdgcn_rcpf(gsum(sb));
  pos[0] = pk(a0[0] * sa, a0[1] * sa); pos[1] = pk(a0[2] * sa, a0[3] * sa);
  pos[2] = pk(a1[0] * sa, a1[1] * sa); pos[3] = pk(a1[2] * sa, a1[3] * sa);
  neg[0] = pk(b0[0] * sb, b0[1] * sb); neg[1] = pk(b0[2] * sb, b0[3] * sb);
  neg[2] = pk(b1[0] * sb, b1[1] * sb); neg[3] = pk(b1[2] * sb, b1[3] * sb);
}

DEVI u32x4 pair16(u32 a0, u32 a1, u32 b0, u32 b1) {
  auto r0 = __builtin_amdgcn_permlane16_swap(a0, b0, false, false);
  auto r1 = __builtin_amdgcn_permlane16_swap(a1, b1, false, false);
  u32x4 o = {r0[0], r1[0], r0[1], r1[1]};
  return o;
}

__global__ __launch_bounds__(512, 2) void fused_block(const uint16_t* __restrict__ x, const uint16_t* __restrict__ wqkv,
                                                      const uint16_t* __restrict__ wphi, const uint16_t* __restrict__ wo,
                                                      const uint16_t* __restrict__ wg, const uint16_t* __restrict__ wu,
                                                      const uint16_t* __restrict__ wd, uint16_t* __restrict__ out,
                                                      char* __restrict__ ws_) {
  extern __shared__ __attribute__((aligned(16))) char smem_[];
  LAS char* smem = (LAS char*)smem_;
  GAS char* ws = (GAS char*)ws_;
  const int tid = threadIdx.x, lane = tid & 63;
  const int w = __builtin_amdgcn_readfirstlane(tid >> 6);
  const int g = lane >> 4, c16 = lane & 15, q = (lane >> 2) & 3, p = lane & 3;
  const bool g3 = (g == 3);
  const int b = blockIdx.x, xc = b & 7, rr = b >> 3;
  const rsrc_t rws = mkrs(ws_, WS_END);
  const rsrc_t rx = mkrs(x, S_ * D_ * 2);
  int dead = 0;
  STAMP(0);
  u64 tag = 0;
  if (w == 0) tag = __hip_atomic_load((GAS u64*)(ws + WS_EPOCH), RLX) + 1;
  const u32x4 ones = {0x3f803f80u, 0x3f803f80u, 0x3f803f80u, 0x3f803f80u};
  const u32 phi_sw = (q >> 1) | ((g & 1) << 1);

  const int h = 2 * xc + (rr >> 4), m = rr & 15;
  u32x4 phq_pos, phq_neg;
  {
    u32x4 xf[14];
    {
      const u32 vo = (u32)((m * 128 + w * 16 + c16) * D_ + 8 * g) * 2;
#pragma unroll
      for (int s = 0; s < 14; ++s) xf[s] = ld16(rx, vo + 64 * s, 0);
    }
    {
      const rsrc_t rw = mkrs(wqkv, D_ * 3 * D_ * 2);
      u32 vo[3];
#pragma unroll
      for (int v = 0; v < 3; ++v) {
        int rl = 4 * v + (lane >> 4), rq = rl / 3, sec = rl % 3;
        int chunk = (lane & 3) ^ (2 * (rq >> 1));
        vo[v] = (u32)((4 * rq + ((lane >> 2) & 3)) * 2688 + sec * 896 + ((56 * h) & ~15) + 16 * chunk);
      }
#pragma unroll
      for (int i = 0; i < 4; ++i) {
        int G = w + 8 * i;
        if (G < 28) {
#pragma unroll
          for (int v = 0; v < 3; ++v) dma16(rw, smem + (G * 3 + v) * 1024, vo[v], (u32)G * 16 * 2688);
        }
      }
    }
    u32x4 wpf[2];
    {
      const GAS uint16_t* wp = (const GAS uint16_t*)wphi + h * 784;
#pragma unroll
      for (int ft = 0; ft < 2; ++ft) {
        int f = 16 * ft + c16;
        u32 e[8];
#pragma unroll
        for (int j = 0; j < 8; ++j) {
          int d = (j < 4) ? 4 * g + j : 12 + 4 * g + j;
          bool ok = (d < 28) && (f < 28);
          u32 v = wp[ok ? d * 28 + f : 0];
          e[j] = ok ? v : 0u;
        }
        wpf[ft][0] = e[0] | (e[1] << 16); wpf[ft][1] = e[2] | (e[3] << 16);
        wpf[ft][2] = e[4] | (e[5] << 16); wpf[ft][3] = e[6] | (e[7] << 16);
      }
    }
    float sm = 0.f, sq = 0.f;
#pragma unroll
    for (int s = 0; s < 14; ++s)
#pragma unroll
      for (int i = 0; i < 4; ++i) { sm = dot2(xf[s][i], 0x3f803f80u, sm); sq = dot2(xf[s][i], xf[s][i], sq); }
    sm = gsum(sm); sq = gsum(sq);
    const float mean = sm * (1.f / D_);
    const float rstd = __builtin_amdgcn_rsqf(fmaxf(sq * (1.f / D_) - mean * mean, 0.f) + 1e-5f);
    WAITV0(); BARRIER();
    STAMP(1);
    u32 wb[2];
#pragma unroll
    for (int dt = 0; dt < 2; ++dt) {
      u32 u = 8 * (h & 1) + 32 * dt + 8 * p; if (u >= 64) u = 56;
      wb[dt] = 1536 * g + 64 * q + (u ^ (32 * (g & 1)));
    }
    if (w < 6) {
      LAS char* bp = smem + wb[w & 1] + 256 * (w >> 1);
      f32x4 cs = {0.f, 0.f, 0.f, 0.f};
#pragma unroll
      for (int s = 0; s < 14; ++s) cs = mfma(tr2(bp + 6144 * s, bp + 6144 * s + 768), ones, cs);
      if (c16 == 0) *(LAS f32x4*)(smem + L_CS + 64 * w + 16 * g) = cs;
    }
    f32x4 acc[6];
#pragma unroll
    for (int i = 0; i < 6; ++i) acc[i] = f32x4{0.f, 0.f, 0.f, 0.f};
#pragma unroll
    for (int s = 0; s < 14; ++s)
#pragma unroll
      for (int nt = 0; nt < 6; ++nt) {
        LAS char* bp = smem + wb[nt & 1] + 6144 * s + 256 * (nt >> 1);
        acc[nt] = mfma(tr2(bp, bp + 768), xf[s], acc[nt]);
      }
    BARRIER();
    STAMP(2);
#pragma unroll
    for (int nt = 0; nt < 6; ++nt) {
      f32x4 cs = *(LAS f32x4*)(smem + L_CS + 64 * nt + 16 * g);
#pragma unroll
      for (int r = 0; r < 4; ++r) acc[nt][r] = rstd * (acc[nt][r] - mean * cs[r]);
    }
    const f32x4 z4 = {0.f, 0.f, 0.f, 0.f};
    const u32 rowsw = ((c16 >> 1) & 1) | (((c16 >> 3) & 1) << 1);
    {
      u32x4 kf = {pk(acc[2][0], acc[2][1]), pk(acc[2][2], acc[2][3]), g3 ? 0u : pk(acc[3][0], acc[3][1]), g3 ? 0u : pk(acc[3][2], acc[3][3])};
      f32x4 p0 = mfma(wpf[0], kf, z4), p1 = mfma(wpf[1], kf, z4);
      u32x4 pos, neg;
      dual_softmax(p0, p1, g3, pos, neg);
      LAS char* pr = smem + L_PHI + 128 * (16 * w + c16) + 8 * g;
      *(LAS u32x2*)(pr + 32 * (0 ^ rowsw)) = u32x2{pos[0], pos[1]};
      *(LAS u32x2*)(pr + 32 * (1 ^ rowsw)) = u32x2{pos[2], pos[3]};
      *(LAS u32x2*)(pr + 32 * (2 ^ rowsw)) = u32x2{neg[0], neg[1]};
      *(LAS u32x2*)(pr + 32 * (3 ^ rowsw)) = u32x2{neg[2], neg[3]};
      LAS char* vr = smem + L_V + 64 * (16 * w + c16) + 8 * g;
      const u32 vs = (c16 >> 3) & 1;
      *(LAS u32x2*)(vr + 32 * (0 ^ vs)) = u32x2{pk(acc[4][0], acc[4][1]), pk(acc[4][2], acc[4][3])};
      *(LAS u32x2*)(vr + 32 * (1 ^ vs)) = g3 ? u32x2{0x00003f80u, 0u} : u32x2{pk(acc[5][0], acc[5][1]), pk(acc[5][2], acc[5][3])};
    }
    BARRIER();
    {
      LAS char* ap = smem + L_PHI + 1024 * g + 128 * q + 32 * ((u32)(w >> 1) ^ phi_sw) + 8 * p;
      LAS char* bp = smem + L_V + 512 * g + 64 * q + 32 * ((w & 1) ^ (g & 1)) + 8 * p;
      f32x4 kv = z4;
#pragma unroll
      for (int s = 0; s < 4; ++s) kv = mfma(tr2(ap + 4096 * s, ap + 4096 * s + 512), tr2(bp + 2048 * s, bp + 2048 * s + 256), kv);
      st16c(rws, __builtin_bit_cast(u32x4, kv), lane * 16, WS_KVP + ((h * 16 + m) * 8 + w) * 1024);
    }
    {
      u32x4 qf = {pk(acc[0][0], acc[0][1]), pk(acc[0][2], acc[0][3]), g3 ? 0u : pk(acc[1][0], acc[1][1]), g3 ? 0u : pk(acc[1][2], acc[1][3])};
      f32x4 p0 = mfma(wpf[0], qf, z4), p1 = mfma(wpf[1], qf, z4);
      WAITV0(); BARRIER();
      if (tid == 0) __hip_atomic_store((GAS u64*)(ws + WS_FL1) + h * 16 + m, tag, RLX);
      STAMP(3);
      dual_softmax(p0, p1, g3, phq_pos, phq_neg);
    }
  }
  {
    if (w == 0) wait_flags((GAS u64*)(ws + WS_FL1) + h * 16, 16, tag, lane, dead);
    BARRIER();
    STAMP(4);
    u32x4 pt[16];
#pragma unroll
    for (int i = 0; i < 16; ++i) pt[i] = ld16c(rws, lane * 16, WS_KVP + ((h * 16 + i) * 8 + w) * 1024);
    f32x4 kv = __builtin_bit_cast(f32x4, pt[0]);
#pragma unroll
    for (int i = 1; i < 16; ++i) kv += __builtin_bit_cast(f32x4, pt[i]);
    *(LAS u32x2*)(smem + L_KVF + (((w >> 2) * 2 + (w & 1)) * 64 + lane) * 16 + 8 * ((w >> 1) & 1)) = u32x2{pk(kv[0], kv[1]), pk(kv[2], kv[3])};
    BARRIER();
    STAMP(5);
    f32x4 o0 = {0.f, 0.f, 0.f, 0.f}, o1 = o0;
    LAS u32x4* kf = (LAS u32x4*)(smem + L_KVF) + lane;
    o0 = mfma(kf[0], phq_pos, o0); o1 = mfma(kf[64], phq_pos, o1);
    o0 = mfma(kf[128], phq_neg, o0); o1 = mfma(kf[192], phq_neg, o1);
    float t0, den = o1[0];
    x32(den, t0); x16(t0, den);
    const float inv = __builtin_amdgcn_rcpf(den + 1e-6f);
    u32x4 of = {pk(o0[0] * inv, o0[1] * inv), pk(o0[2] * inv, o0[3] * inv), g3 ? 0u : pk(o1[0] * inv, o1[1] * inv), g3 ? 0u : pk(o1[2] * inv, o1[3] * inv)};
    st16c(rws, of, lane * 16, WS_O + ((m * 8 + w) * 16 + h) * 1024);
    WAITV0(); BARRIER();
    if (tid == 0) __hip_atomic_store((GAS u64*)(ws + WS_FL2) + m * 16 + h, tag, RLX);
    STAMP(6);
  }
  if (b >= 224) return;
  const int t64 = 4 * xc + rr / 7, cs = rr % 7;
  const int mp = w & 1, nt = w >> 1;
  f32x4 hA, hB;
  {
    const rsrc_t rw = mkrs(wo, D_ * D_ * 2);
    {
      const u32 vo = (lane >> 3) * 896 + 32 * (((lane >> 1) & 3) ^ ((lane >> 4) & 3)) + 16 * (lane & 1);
#pragma unroll
      for (int i = 0; i < 8; ++i) {
        int blk = 8 * w + i;
        dma16(rw, smem + blk * 1024, vo, (u32)(28 * (blk >> 2) + 8 * (blk & 3)) * 896 + 128 * cs);
      }
    }
    u32x2 xa, xb;
    {
      const GAS char* xp = (const GAS char*)x + ((t64 * 64 + mp * 32 + c16) * D_ + 64 * cs + 16 * nt + 4 * g) * 2;
      xa = *(const GAS u32x2*)xp; xb = *(const GAS u32x2*)(xp + 16 * D_ * 2);
    }
    WAITV0(); BARRIER();
    u32x4 wf[16];
    {
      LAS char* bp = smem + 128 * (4 * g + q) + 32 * ((u32)nt ^ phi_sw) + 8 * p;
#pragma unroll
      for (int s = 0; s < 16; ++s) wf[s] = tr2(bp + 4096 * s, bp + 4096 * s + 2048);
    }
    if (w == 0) wait_flags((GAS u64*)(ws + WS_FL2) + (t64 >> 1) * 16, 16, tag, lane, dead);
    BARRIER();
    STAMP(7);
    if (b == 0 && tid == 0) __hip_atomic_store((GAS u64*)(ws + WS_EPOCH), tag, RLX);
    {
      u32x4 st[8];
#pragma unroll
      for (int i = 0; i < 8; ++i) st[i] = ld16c(rws, lane * 16, WS_O + (t64 * 64 + 8 * w + i) * 1024);
#pragma unroll
      for (int i = 0; i < 8; ++i) *(LAS u32x4*)(smem + 65536 + (8 * w + i) * 1024 + lane * 16) = st[i];
    }
    BARRIER();
    STAMP(8);
    hA = f32x4{bflo(xa[0]), bfhi(xa[0]), bflo(xa[1]), bfhi(xa[1])};
    hB = f32x4{bflo(xb[0]), bfhi(xb[0]), bflo(xb[1]), bfhi(xb[1])};
    LAS char* ap = smem + 65536 + mp * 32768 + lane * 16;
#pragma unroll
    for (int s = 0; s < 16; ++s) {
      hA = mfma(wf[s], *(LAS u32x4*)(ap + 1024 * s), hA);
      hB = mfma(wf[s], *(LAS u32x4*)(ap + 16384 + 1024 * s), hB);
    }
    u32x4 hv = pair16(pk(hA[0], hA[1]), pk(hA[2], hA[3]), pk(hB[0], hB[1]), pk(hB[2], hB[3]));
    st16c(rws, hv, (g & 1) * 14336 + (2 * (nt & 1) + (g >> 1)) * 256 + c16 * 16,
          WS_H + ((t64 * 4 + 2 * mp) * 14 + 2 * cs + (nt >> 1)) * 1024);
    WAITV0(); BARRIER();
    if (tid == 0) __hip_atomic_store((GAS u64*)(ws + WS_FL3) + (t64 >> 1) * 16 + (t64 & 1) * 7 + cs, tag, RLX);
    STAMP(9);
  }
  const int m4 = 4 * (xc >> 1) + rr / 7, fs = 7 * (xc & 1) + rr % 7;
  {
    u32x4 wgf[14], wuf[14];
    u32 vo[2];
#pragma unroll
    for (int v = 0; v < 2; ++v) vo[v] = (lane >> 4) * 3584 + 32 * (((lane >> 1) & 7) ^ ((lane >> 4) | (v << 2))) + 16 * (lane & 1);
    LAS char* bp = smem + 2048 * g + 256 * q + 32 * ((u32)w ^ (q | ((g & 1) << 2))) + 8 * p;
    {
      const rsrc_t rw = mkrs(wg, D_ * F_ * 2);
#pragma unroll
      for (int i = 0; i < 14; ++i) { int blk = 14 * w + i; dma16(rw, smem + blk * 1024, vo[(blk >> 1) & 1], (u32)blk * 4 * 3584 + 256 * fs); }
      WAITV0(); BARRIER();
#pragma unroll
      for (int s = 0; s < 14; ++s) wgf[s] = tr2(bp + 8192 * s, bp + 8192 * s + 1024);
      BARRIER();
    }
    {
      const rsrc_t rw = mkrs(wu, D_ * F_ * 2);
#pragma unroll
      for (int i = 0; i < 14; ++i) { int blk = 14 * w + i; dma16(rw, smem + blk * 1024, vo[(blk >> 1) & 1], (u32)blk * 4 * 3584 + 256 * fs); }
      WAITV0(); BARRIER();
#pragma unroll
      for (int s = 0; s < 14; ++s) wuf[s] = tr2(bp + 8192 * s, bp + 8192 * s + 1024);
    }
    f32x4 csg = {0.f, 0.f, 0.f, 0.f}, csu = csg;
#pragma unroll
    for (int s = 0; s < 14; ++s) { csg = mfma(wgf[s], ones, csg); csu = mfma(wuf[s], ones, csu); }
    if (w == 0) wait_flags((GAS u64*)(ws + WS_FL3) + m4 * 16, 14, tag, lane, dead);
    BARRIER();
    STAMP(10);
    {
      u32x4 st[14];
#pragma unroll
      for (int s = 0; s < 14; ++s) st[s] = ld16c(rws, lane * 16, WS_H + ((m4 * 8 + w) * 14 + s) * 1024);
#pragma unroll
      for (int s = 0; s < 14; ++s) *(LAS u32x4*)(smem + (w * 14 + s) * 1024 + lane * 16) = st[s];
      float sm = 0.f, sq = 0.f;
#pragma unroll
      for (int s = 0; s < 14; ++s)
#pragma unroll
        for (int i = 0; i < 4; ++i) { sm = dot2(st[s][i], 0x3f803f80u, sm); sq = dot2(st[s][i], st[s][i], sq); }
      sm = gsum(sm); sq = gsum(sq);
      const float mean = sm * (1.f / D_);
      const float rstd = __builtin_amdgcn_rsqf(fmaxf(sq * (1.f / D_) - mean * mean, 0.f) + 1e-5f);
      if (g == 0) *(LAS u32x2*)(smem + L_CS + (w * 16 + c16) * 8) = u32x2{__float_as_uint(mean), __float_as_uint(rstd)};
    }
    BARRIER();
    STAMP(11);
    const int ftile = 8 * fs + w;
    const u32 svo = (g & 1) * 57344 + (2 * (ftile & 1) + (g >> 1)) * 256 + c16 * 16;
#pragma unroll 1
    for (int mt = 0; mt < 8; mt += 2) {
      u32 o[4];
#pragma unroll
      for (int e = 0; e < 2; ++e) {
        LAS char* ap = smem + (mt + e) * 14336 + lane * 16;
        f32x4 ag = {0.f, 0.f, 0.f, 0.f}, au = ag;
#pragma unroll
        for (int s = 0; s < 14; ++s) {
          u32x4 xf = *(LAS u32x4*)(ap + 1024 * s);
          ag = mfma(wgf[s], xf, ag); au = mfma(wuf[s], xf, au);
        }
        u32x2 stt = *(LAS u32x2*)(smem + L_CS + ((mt + e) * 16 + c16) * 8);
        const float mean = __uint_as_float(stt[0]), rstd = __uint_as_float(stt[1]);
        float hv[4];
#pragma unroll
        for (int r = 0; r < 4; ++r) {
          float gg = rstd * (ag[r] - mean * csg[r]), uu = rstd * (au[r] - mean * csu[r]);
          hv[r] = uu * __builtin_amdgcn_rcpf(1.f + __builtin_amdgcn_exp2f(gg * -1.4426950408889634f));
        }
        o[2 * e] = pk(hv[0], hv[1]); o[2 * e + 1] = pk(hv[2], hv[3]);
      }
      st16c(rws, pair16(o[0], o[1], o[2], o[3]), svo, WS_HH + ((m4 * 8 + mt) * 56 + (ftile >> 1)) * 1024);
    }
    WAITV0(); BARRIER();
    if (tid == 0) __hip_atomic_store((GAS u64*)(ws + WS_FL4) + m4 * 16 + fs, tag, RLX);
    STAMP(12);
  }
  {
    u32x4 wf[7][4];
    const rsrc_t rw = mkrs(wd, F_ * D_ * 2);
    u32 vo[2];
#pragma unroll
    for (int v = 0; v < 2; ++v) vo[v] = (lane >> 3) * 896 + 32 * (((lane >> 1) & 3) ^ (((lane >> 4) & 1) | (v << 1))) + 16 * (lane & 1);
    LAS char* reg = smem + 16384 * w;
    LAS char* bp = reg + 1024 * g + 128 * q + 8 * p;
#pragma unroll
    for (int i = 0; i < 16; ++i) dma16(rw, reg + i * 1024, vo[i & 1], (u32)(224 * w + 8 * i) * 896 + 128 * cs);
    WAITV0();
#pragma unroll
    for (int s = 0; s < 4; ++s)
#pragma unroll
      for (int n = 0; n < 4; ++n) { LAS char* a = bp + 32 * ((u32)n ^ phi_sw) + 4096 * s; wf[s][n] = tr2(a, a + 512); }
    asm volatile("s_waitcnt lgkmcnt(0)" ::: "memory");
#pragma unroll
    for (int i = 0; i < 12; ++i) dma16(rw, reg + i * 1024, vo[i & 1], (u32)(224 * w + 128 + 8 * i) * 896 + 128 * cs);
    WAITV0();
#pragma unroll
    for (int s = 0; s < 3; ++s)
#pragma unroll
      for (int n = 0; n < 4; ++n) { LAS char* a = bp + 32 * ((u32)n ^ phi_sw) + 4096 * s; wf[4 + s][n] = tr2(a, a + 512); }
    if (w == 0) wait_flags((GAS u64*)(ws + WS_FL4) + (t64 >> 1) * 16, 14, tag, lane, dead);
    BARRIER();
    STAMP(13);
#pragma unroll
    for (int hf = 0; hf < 2; ++hf) {
      u32x4 xf[2][7];
#pragma unroll
      for (int e = 0; e < 2; ++e)
#pragma unroll
        for (int s = 0; s < 7; ++s) xf[e][s] = ld16c(rws, lane * 16, WS_HH + ((t64 * 4 + 2 * hf + e) * 56 + 7 * w + s) * 1024);
#pragma unroll
      for (int n = 0; n < 4; ++n)
#pragma unroll
        for (int e = 0; e < 2; ++e) {
          f32x4 a = {0.f, 0.f, 0.f, 0.f};
#pragma unroll
          for (int s = 0; s < 7; ++s) a = mfma(wf[s][n], xf[e][s], a);
          *(LAS f32x4*)(smem + ((((n * 2 + hf) * 2 + e) * 8 + w) * 64 + lane) * 16) = a;
        }
    }
    BARRIER();
    STAMP(14);
    LAS f32x4* rp = (LAS f32x4*)(smem + (w * 2 * 8 * 64 + lane) * 16);
#pragma unroll
    for (int i = 0; i < 8; ++i) { hA += rp[i * 64]; hB += rp[512 + i * 64]; }
    u32x4 ov = pair16(pk(hA[0], hA[1]), pk(hA[2], hA[3]), pk(hB[0], hB[1]), pk(hB[2], hB[3]));
    GAS char* op = (GAS char*)out + ((t64 * 64 + mp * 32 + (g & 1) * 16 + c16) * D_ + 64 * cs + 16 * nt + 8 * (g >> 1)) * 2;
    *(GAS u32x4*)op = ov;
    STAMP(15);
  }
}

extern "C" void kernel_launch(void* const* d_in, const int* in_sizes, int n_in, void* d_out, int out_size, void* d_ws,
                              size_t ws_size, hipStream_t stream) {
  hipFuncSetAttribute((const void*)fused_block, hipFuncAttributeMaxDynamicSharedMemorySize, LDS_BYTES);
  fused_block<<<256, 512, LDS_BYTES, stream>>>((const uint16_t*)d_in[0], (const uint16_t*)d_in[1], (const uint16_t*)d_in[2],
                                               (const uint16_t*)d_in[3], (const uint16_t*)d_in[5], (const uint16_t*)d_in[6],
                                               (const uint16_t*)d_in[7], (uint16_t*)d_out, (char*)d_ws);
}
```

```cpp
#include <hip/hip_runtime.h>
#include <stdint.h>

#define DEVI __device__ __forceinline__
#define LAS __attribute__((address_space(3)))
#define GAS __attribute__((address_space(1)))
typedef unsigned u32;
typedef unsigned long long u64;
typedef u32 u32x2 __attribute__((ext_vector_type(2)));
typedef u32 u32x4 __attribute__((ext_vector_type(4)));
typedef float f32x4 __attribute__((ext_vector_type(4)));
typedef __bf16 bf16x8 __attribute__((ext_vector_type(8)));
typedef __bf16 bf16x2 __attribute__((ext_vector_type(2)));
typedef short s16x4 __attribute__((ext_vector_type(4)));
typedef __amdgpu_buffer_rsrc_t rsrc_t;

constexpr int S_ = 2048, D_ = 448, F_ = 1792;
constexpr u32 WS_EPOCH = 0;
constexpr u32 WS_FL1 = 4096, WS_FL2 = WS_FL1 + 16 * 128, WS_FL3 = WS_FL2 + 16 * 128, WS_FL4 = WS_FL3 + 16 * 128, WS_FL5 = WS_FL4 + 16 * 128;
constexpr u32 WS_KVP = 65536;
constexpr u32 WS_O = WS_KVP + 16 * 16 * 8192;
constexpr u32 WS_H = WS_O + 128 * 16 * 1024;
constexpr u32 WS_HH = WS_H + 128 * 14 * 1024;
constexpr u32 WS_ST = WS_HH + 128 * 56 * 1024;
constexpr u32 WS_END = WS_ST + 7 * 2048 * 8;
constexpr u32 WS_STAMP = WS_END;
constexpr u32 SLOT = 24576;
constexpr u32 L_Q = SLOT, L_W3 = L_Q + 28672;
constexpr u32 L_RAW = 5 * SLOT;
constexpr u32 L_PHI = 0, L_V = 16384;
constexpr u32 L_MISC = 6 * SLOT;
constexpr u32 L_KVF = L_MISC, L_WPHI = L_MISC + 4096, L_CS = L_MISC + 6144  , L_ST = L_MISC + 12288  , L_PS = L_MISC + 13312  , L_DUMMY = L_MISC + 15360;
constexpr u32 LDS_BYTES = 163840;

DEVI f32x4 mfma(u32x4 a, u32x4 b, f32x4 c) {
  return __builtin_amdgcn_mfma_f32_16x16x32_bf16(__builtin_bit_cast(bf16x8, a), __builtin_bit_cast(bf16x8, b), c, 0, 0, 0);
}
DEVI u32 pk(float a, float b) { bf16x2 v = {(__bf16)a, (__bf16)b}; return __builtin_bit_cast(u32, v); }
DEVI float bflo(u32 v) { return __uint_as_float(v << 16); }
DEVI float bfhi(u32 v) { return __uint_as_float(v & 0xffff0000u); }
DEVI u32x2 trr(LAS char* p) { return __builtin_bit_cast(u32x2, __builtin_amdgcn_ds_read_tr16_b64_v4i16((LAS s16x4*)p)); }
DEVI u32x4 tr2(LAS char* p0, LAS char* p1) { u32x2 a = trr(p0), b = trr(p1); u32x4 r = {a[0], a[1], b[0], b[1]}; return r; }
DEVI float dot2(u32 a, u32 b, float c) {
  return __builtin_amdgcn_fdot2_f32_bf16(__builtin_bit_cast(bf16x2, a), __builtin_bit_cast(bf16x2, b), c, false);
}
DEVI float x16(float v, float& o) {
  auto r = __builtin_amdgcn_permlane16_swap(__float_as_uint(v), __float_as_uint(v), false, false);
  o = __uint_as_float(r[1]); return __uint_as_float(r[0]);
}
DEVI float x32(float v, float& o) {
  auto r = __builtin_amdgcn_permlane32_swap(__float_as_uint(v), __float_as_uint(v), false, false);
  o = __uint_as_float(r[1]); return __uint_as_float(r[0]);
}
DEVI float gsum(float v) { float b, a = x16(v, b); v = a + b; a = x32(v, b); return a + b; }
DEVI float gmax(float v) { float b, a = x16(v, b); v = fmaxf(a, b); a = x32(v, b); return fmaxf(a, b); }
DEVI float gmin(float v) { float b, a = x16(v, b); v = fminf(a, b); a = x32(v, b); return fminf(a, b); }
DEVI rsrc_t mkrs(const void* p, u32 bytes) { return __builtin_amdgcn_make_buffer_rsrc((void*)p, 0, bytes, 0x00020000); }
DEVI u32x4 mkd(const void* p, u32 bytes) {
  u64 a = (u64)p; u32x4 d = {(u32)a, (u32)(a >> 32) & 0xffffu, bytes, 0x00020000u}; return d;
}
DEVI void dma16(u32x4 d, u32 lds, u32 voff, u32 soff) {
  asm volatile("s_mov_b32 m0, %0\n\ts_nop 3\n\tbuffer_load_dwordx4 %1, %2, %3 offen lds" ::"s"(lds), "v"(voff), "s"(d), "s"(soff) : "memory", "m0");
}
DEVI void dma16c(u32x4 d, u32 lds, u32 voff, u32 soff) {
  asm volatile("s_mov_b32 m0, %0\n\ts_nop 3\n\tbuffer_load_dwordx4 %1, %2, %3 offen sc1 lds" ::"s"(lds), "v"(voff), "s"(d), "s"(soff) : "memory", "m0");
}
DEVI void touch(u32x4 d, u32 voff) {
  asm volatile("s_mov_b32 m0, %0\n\ts_nop 3\n\tbuffer_load_dword %1, %2, 0 offen lds" ::"s"(L_DUMMY), "v"(voff), "s"(d) : "memory", "m0");
}
#define WAITV(n) asm volatile("s_waitcnt vmcnt(" #n ")" ::: "memory")
DEVI u32x4 ld16(rsrc_t rs, u32 voff, u32 soff) { return __builtin_amdgcn_raw_buffer_load_b128(rs, voff, soff, 0); }
DEVI u32x4 ld16c(rsrc_t rs, u32 voff, u32 soff) { return __builtin_amdgcn_raw_buffer_load_b128(rs, voff, soff, 16); }
DEVI void st16c(rsrc_t rs, u32x4 v, u32 voff, u32 soff) { __builtin_amdgcn_raw_buffer_store_b128(v, rs, voff + soff, 0, 16); }
#define WAITV0() asm volatile("s_waitcnt vmcnt(0)" ::: "memory")
#define BARRIER() do { asm volatile("s_waitcnt lgkmcnt(0)" ::: "memory"); __builtin_amdgcn_s_barrier(); asm volatile("" ::: "memory"); } while (0)
#define RLX __ATOMIC_RELAXED, __HIP_MEMORY_SCOPE_AGENT

#define STAMP(i)
#define STR(i) (void)tr_
#define STFLUSH(n)
#define STFLUSH2(n)

DEVI void wait_flags(GAS u64* fl, int n, u64 tag, int lane, int& dead) {
  int ln = lane < n ? lane : 0;
  for (u32 spins = 0; !dead; ++spins) {
    u64 v = __hip_atomic_load(fl + ln, RLX);
    if (__all(v == tag)) break;
    if (spins > (1u << 17)) dead = 1;
    __builtin_amdgcn_s_sleep(1);
  }
}

template <class HK>
DEVI void dual_softmax(const f32x4& p0, f32x4 p1, bool g3, u32x4& pos, u32x4& neg, HK hk) {
  const float L2E = 1.4426950408889634f;
  if (g3) p1 = p0;
  float mx = fmaxf(fmaxf(fmaxf(p0[0], p0[1]), fmaxf(p0[2], p0[3])), fmaxf(fmaxf(p1[0], p1[1]), fmaxf(p1[2], p1[3])));
  float mn = fminf(fminf(fminf(p0[0], p0[1]), fminf(p0[2], p0[3])), fminf(fminf(p1[0], p1[1]), fminf(p1[2], p1[3])));
  mx = gmax(mx); mn = gmin(mn);
  hk(0);
  float a0[4], a1[4], b0[4], b1[4];
  float sa = 0.f, sb = 0.f;
#pragma unroll
  for (int r = 0; r < 4; ++r) {
    a0[r] = __builtin_amdgcn_exp2f((p0[r] - mx) * L2E);
    b0[r] = __builtin_amdgcn_exp2f((mn - p0[r]) * L2E);
    a1[r] = g3 ? 0.f : __builtin_amdgcn_exp2f((p1[r] - mx) * L2E);
    b1[r] = g3 ? 0.f : __builtin_amdgcn_exp2f((mn - p1[r]) * L2E);
    sa += a0[r] + a1[r]; sb += b0[r] + b1[r];
  }
  hk(1);
  sa = __builtin_amdgcn_rcpf(gsum(sa)); sb = __builtin_amdgcn_rcpf(gsum(sb));
  hk(2);
  pos[0] = pk(a0[0] * sa, a0[1] * sa); pos[1] = pk(a0[2] * sa, a0[3] * sa);
  pos[2] = pk(a1[0] * sa, a1[1] * sa); pos[3] = pk(a1[2] * sa, a1[3] * sa);
  neg[0] = pk(b0[0] * sb, b0[1] * sb); neg[1] = pk(b0[2] * sb, b0[3] * sb);
  neg[2] = pk(b1[0] * sb, b1[1] * sb); neg[3] = pk(b1[2] * sb, b1[3] * sb);
}

DEVI u32x4 pair16(u32 a0, u32 a1, u32 b0, u32 b1) {
  auto r0 = __builtin_amdgcn_permlane16_swap(a0, b0, false, false);
  auto r1 = __builtin_amdgcn_permlane16_swap(a1, b1, false, false);
  u32x4 o = {r0[0], r1[0], r0[1], r1[1]};
  return o;
}

__global__ __launch_bounds__(512, 2) void fused_block(const uint16_t* __restrict__ x, const uint16_t* __restrict__ wqkv,
                                                      const uint16_t* __restrict__ wphi, const uint16_t* __restrict__ wo,
                                                      const uint16_t* __restrict__ wg, const uint16_t* __restrict__ wu,
                                                      const uint16_t* __restrict__ wd, uint16_t* __restrict__ out,
                                                      char* __restrict__ ws_) {
  extern __shared__ __attribute__((aligned(16))) char smem_[];
  LAS char* smem = (LAS char*)smem_;
  u32 tr_[12];
  STR(0);
  asm volatile("" ::"s"(x), "s"(wqkv), "s"(wphi), "s"(wo), "s"(wg), "s"(wu), "s"(wd), "s"(out), "s"(ws_));
  GAS char* ws = (GAS char*)ws_;
  const int tid = threadIdx.x, lane = tid & 63;
  const int w = __builtin_amdgcn_readfirstlane(tid >> 6);
  const int g = lane >> 4, c16 = lane & 15, q = (lane >> 2) & 3, p = lane & 3;
  const bool g3 = (g == 3);
  const int b = blockIdx.x, xc = b & 7, rr = b >> 3;
  const rsrc_t rws = mkrs(ws_, WS_END);
  int dead = 0;
  STAMP(0);
  const u32x4 ones = {0x3f803f80u, 0x3f803f80u, 0x3f803f80u, 0x3f803f80u};
  const f32x4 z4 = {0.f, 0.f, 0.f, 0.f};
  const u32 phi_sw = (q >> 1) | ((g & 1) << 1);
  const bool worker = b < 224;
  const int h = 4 * (xc & 3) + (rr & 3), m = 8 * (xc >> 2) + (rr >> 2);
  const int r7 = worker ? rr / 7 : 0, c7 = worker ? rr % 7 : 0;
  const int t64 = 4 * xc + r7, cs = c7;
  const int mt0 = (t64 >> 1) * 8 + 2 * (t64 & 1);
  const int m4 = 4 * (xc >> 1) + r7, fs = 7 * (xc & 1) + c7;
  const int mp = w & 1, nt = w >> 1;
  const u32x4 dX = mkd(x, S_ * D_ * 2), dW1 = mkd(wqkv, D_ * 3 * D_ * 2), dW3 = mkd(wo, D_ * D_ * 2);
  const u32x4 dG = mkd(wg, D_ * F_ * 2), dU = mkd(wu, D_ * F_ * 2), dD = mkd(wd, F_ * D_ * 2), dWS = mkd(ws_, WS_END);

  u64 tag;
  u32x2 xra, xrb;
  {
    const GAS char* ep = ws + WS_EPOCH;
    const GAS char* xp = (const GAS char*)x + (((mt0 + 4 * mp) * 16 + c16) * D_ + 64 * cs + 16 * nt + 4 * g) * 2;
    asm volatile("global_load_dwordx2 %0, %3, off sc1\n\tglobal_load_dwordx2 %1, %4, off\n\tglobal_load_dwordx2 %2, %5, off"
                 : "=&v"(tag), "=&v"(xra), "=&v"(xrb) : "v"(ep), "v"(xp), "v"(xp + 16 * D_ * 2) : "memory");
  }
  u32 v4[2];
#pragma unroll
  for (int v = 0; v < 2; ++v) v4[v] = (lane >> 4) * 3584 + 32 * (((lane >> 1) & 7) ^ ((lane >> 4) | (v << 2))) + 16 * (lane & 1) + 256 * fs;
  const u32 b4 = 2048 * g + 256 * q + 32 * ((u32)w ^ (q | ((g & 1) << 2))) + 8 * p;
  const int rw4 = (w + 2 * r7) & 7;
  const u32 v4w = v4[(rw4 >> 1) & 1];
  u32x4 wgf[14], wuf[14];

  u32x4 phq_pos, phq_neg;
  u32x4 w3f[16];
  const int grp = w >> 2, tg = w & 3;
  const int myMt = 2 * tg + grp;
  {
    const int rot = (2 * (rr & 3) + (rr >> 2)) % 7;
    const u32 win0 = (56 * h) & ~15;
    if (w == 7) {
      const u32x4 dP = mkd(wphi, 16 * 784 * 2);
      dma16(dP, L_WPHI, lane * 16, h * 1568); dma16(dP, L_WPHI + 1024, lane * 16, h * 1568 + 1024);
    }
    u32 vx0, vx1, vw, vq, v3;
    {
      const int r8 = lane >> 3, pos = lane & 7;
      vx0 = (u32)((m * 128 + w * 16 + r8) * 896 + 16 * (pos ^ (lane >> 4)));
      vx1 = (u32)((m * 128 + w * 16 + 8 + r8) * 896 + 16 * (pos ^ (4 + (lane >> 4))));
      vw = (u32)((8 * w + 4 * (lane >> 5) + q) * 2688 + (1 + (g & 1)) * 896 + win0 + 16 * (p ^ (2 * (w & 1))));
      vq = (u32)((4 * g + q) * 2688 + win0 + 16 * (p ^ (2 * (lane >> 5))));
      v3 = (u32)(r8 * 896 + 32 * (((lane >> 1) & 3) ^ ((lane >> 4) & 3)) + 16 * (lane & 1) + 128 * cs);
    }
    auto chunk_of = [&](int i) { int c = rot + i; return c >= 7 ? c - 7 : c; };
    auto issue_chunk = [&](int i, u32 slot) {
      const int c = chunk_of(i);
      dma16(dX, slot + w * 2048, vx0, c * 128);
      dma16(dX, slot + w * 2048 + 1024, vx1, c * 128);
      dma16(dW1, slot + 16384 + w * 1024, vw, c * 64 * 2688);
    };
    auto ex = [&](int k) {
      const int e = 8 * k + w;
      __builtin_amdgcn_sched_barrier(0);
      if (e < 28) dma16(dW1, L_Q + e * 1024, vq, (chunk_of(e >> 2) * 64 + (e & 3) * 16) * 2688);
      else if (e < 92) { const int k3 = e - 28; dma16(dW3, L_W3 + k3 * 1024, v3, (28 * (k3 >> 2) + 8 * (k3 & 3)) * 896); }
      __builtin_amdgcn_sched_barrier(0);
    };
#pragma unroll
    for (int i = 0; i < 3; ++i) issue_chunk(i, i * SLOT);
    STR(1);
    u32 wb[2];
#pragma unroll
    for (int dt = 0; dt < 2; ++dt) {
      u32 u = 8 * (h & 1) + 32 * dt + 8 * p; if (u >= 64) u = 56;
      wb[dt] = 64 * q + (u ^ (32 * (g & 1)));
    }
    const u32 xb = 128 * c16 + 16 * (g ^ ((c16 >> 1) & 7)) + myMt * 2048;
    u32x4 xk[14], wpf[2];
    f32x4 a00 = z4, a01 = z4, a10 = z4, a11 = z4, cs0 = z4, cs1 = z4;
    float sm = 0.f, sq = 0.f;
    u32x4 x1[2][2], w0[2][2], w1[2][2];
#pragma unroll
    for (int i = 0; i < 8; ++i) {
      if (i < 7) {
        if (i == 6) WAITV(0); else if (i == 5) WAITV(3); else WAITV(6);
        BARRIER();
        STR(2 + i);
        LAS char* sl = smem + (i % 6) * SLOT;
#pragma unroll
        for (int e = 0; e < 2; ++e) {
          LAS char* wp0 = sl + 16384 + 4096 * e + 1024 * g + 256 * grp + wb[0];
          LAS char* wp1 = sl + 16384 + 4096 * e + 1024 * g + 256 * grp + wb[1];
          w0[i & 1][e] = tr2(wp0, wp0 + 512);
          xk[2 * i + e] = *(LAS u32x4*)(sl + (xb ^ (64 * e)));
          w1[i & 1][e] = tr2(wp1, wp1 + 512);
          x1[i & 1][e] = *(LAS u32x4*)(sl + (xb ^ (64 * e) ^ 2048));
        }
        __builtin_amdgcn_sched_barrier(0);
      }
      if (i > 0) {
        const int c = (i - 1) & 1;
#pragma unroll
        for (int e = 0; e < 2; ++e) {
          a00 = mfma(w0[c][e], xk[2 * i - 2 + e], a00); a01 = mfma(w1[c][e], xk[2 * i - 2 + e], a01);
          a10 = mfma(w0[c][e], x1[c][e], a10); a11 = mfma(w1[c][e], x1[c][e], a11);
#pragma unroll
          for (int k = 0; k < 4; ++k) { sm = dot2(xk[2 * i - 2 + e][k], 0x3f803f80u, sm); sq = dot2(xk[2 * i - 2 + e][k], xk[2 * i - 2 + e][k], sq); }
        }
        asm volatile("" : "+v"(sm), "+v"(sq), "+v"(a00), "+v"(a01), "+v"(a10), "+v"(a11));
        if (tg == 0) {
#pragma unroll
          for (int e = 0; e < 2; ++e) { cs0 = mfma(w0[c][e], ones, cs0); cs1 = mfma(w1[c][e], ones, cs1); }
        }
      }
      __builtin_amdgcn_sched_barrier(0);
      if (i <= 3) issue_chunk(i + 3, ((i + 3) % 6) * SLOT);
      if (i == 6) { ex(0); ex(1); }
      if (i == 7) { ex(2); ex(3); }
      if (i == 1) {
#pragma unroll
        for (int ft = 0; ft < 2; ++ft) {
          const int f = 16 * ft + c16;
          u32 e[8];
#pragma unroll
          for (int j = 0; j < 8; ++j) {
            const int d = (j < 4) ? 4 * g + j : 12 + 4 * g + j;
            const bool ok = (d < 28) && (f < 28);
            u32 v = *(LAS uint16_t*)(smem + L_WPHI + (ok ? d * 28 + f : 0) * 2);
            e[j] = ok ? v : 0u;
          }
          wpf[ft][0] = e[0] | (e[1] << 16); wpf[ft][1] = e[2] | (e[3] << 16);
          wpf[ft][2] = e[4] | (e[5] << 16); wpf[ft][3] = e[6] | (e[7] << 16);
        }
        asm volatile("" : "+v"(wpf[0]), "+v"(wpf[1]));
      }
    }
    asm volatile("" : "+v"(tag), "+v"(xra), "+v"(xrb));
    tag += 1;
    {
      LAS f32x4* rp = (LAS f32x4*)(smem + L_RAW + w * 2048) + lane;
      rp[0] = a10; rp[64] = a11;
      if (tg == 0 && c16 == 0) {
        *(LAS f32x4*)(smem + L_CS + (2 * grp) * 64 + 16 * g) = cs0;
        *(LAS f32x4*)(smem + L_CS + (2 * grp + 1) * 64 + 16 * g) = cs1;
      }
    }
    ex(4);
    sm = gsum(sm); sq = gsum(sq);
    const float mean = sm * (1.f / D_);
    const float rstd = __builtin_amdgcn_rsqf(fmaxf(sq * (1.f / D_) - mean * mean, 0.f) + 1e-5f);
    ex(5);
    BARRIER();
    STR(9);
    STAMP(1); STAMP(2);
    {
      LAS f32x4* rp = (LAS f32x4*)(smem + L_RAW + (w ^ 4) * 2048) + lane;
      LAS f32x4* cp = (LAS f32x4*)(smem + L_CS + 16 * g);
      f32x4 r0 = rp[0], r1 = rp[64];
      ex(6);
      f32x4 acc[4] = {grp ? r0 : a00, grp ? r1 : a01, grp ? a00 : r0, grp ? a01 : r1};
#pragma unroll
      for (int n = 0; n < 4; ++n) {
        const f32x4 c = cp[4 * n];
#pragma unroll
        for (int r = 0; r < 4; ++r) acc[n][r] = rstd * (acc[n][r] - mean * c[r]);
      }
      u32x4 kf = {pk(acc[0][0], acc[0][1]), pk(acc[0][2], acc[0][3]), g3 ? 0u : pk(acc[1][0], acc[1][1]), g3 ? 0u : pk(acc[1][2], acc[1][3])};
      f32x4 p0 = mfma(wpf[0], kf, z4), p1 = mfma(wpf[1], kf, z4);
      ex(7);
      LAS char* vr = smem + L_V + 64 * (16 * myMt + c16) + 8 * g;
      const u32 vs = (c16 >> 3) & 1;
      *(LAS u32x2*)(vr + 32 * (0 ^ vs)) = u32x2{pk(acc[2][0], acc[2][1]), pk(acc[2][2], acc[2][3])};
      *(LAS u32x2*)(vr + 32 * (1 ^ vs)) = g3 ? u32x2{0x00003f80u, 0u} : u32x2{pk(acc[3][0], acc[3][1]), pk(acc[3][2], acc[3][3])};
      u32x4 pos, neg;
      ex(8);
      dual_softmax(p0, p1, g3, pos, neg, [&](int k) { ex(9 + k); });
      const u32 rowsw = ((c16 >> 1) & 1) | (((c16 >> 3) & 1) << 1);
      LAS char* pr = smem + L_PHI + 128 * (16 * myMt + c16) + 8 * g;
      *(LAS u32x2*)(pr + 32 * (0 ^ rowsw)) = u32x2{pos[0], pos[1]};
      *(LAS u32x2*)(pr + 32 * (1 ^ rowsw)) = u32x2{pos[2], pos[3]};
      *(LAS u32x2*)(pr + 32 * (2 ^ rowsw)) = u32x2{neg[0], neg[1]};
      *(LAS u32x2*)(pr + 32 * (3 ^ rowsw)) = u32x2{neg[2], neg[3]};
    }
    BARRIER();
    STR(10);
    if (w >= 4) {
      LAS char* ap = smem + L_PHI + 1024 * g + 128 * q + 32 * ((u32)(w - 4) ^ phi_sw) + 8 * p;
      LAS char* bp0 = smem + L_V + 512 * g + 64 * q + 32 * (g & 1) + 8 * p;
      LAS char* bp1 = smem + L_V + 512 * g + 64 * q + 32 * (1 ^ (g & 1)) + 8 * p;
      f32x4 kv0 = z4, kv1 = z4;
#pragma unroll
      for (int s = 0; s < 4; ++s) {
        u32x4 a = tr2(ap + 4096 * s, ap + 4096 * s + 512);
        kv0 = mfma(a, tr2(bp0 + 2048 * s, bp0 + 2048 * s + 256), kv0);
        kv1 = mfma(a, tr2(bp1 + 2048 * s, bp1 + 2048 * s + 256), kv1);
      }
      u32x4 kb = {pk(kv0[0], kv0[1]), pk(kv0[2], kv0[3]), pk(kv1[0], kv1[1]), pk(kv1[2], kv1[3])};
      st16c(rws, kb, lane * 16, WS_KVP + ((h * 16 + m) * 4 + (w - 4)) * 1024);
      WAITV0();
    } else {
      WAITV0();
    }
    BARRIER();
    if (tid == 448) __hip_atomic_store((GAS u64*)(ws + WS_FL1) + h * 16 + m, tag, RLX);
    STR(11); STFLUSH(12);
    STAMP(3);
    if (w < 4) {
#pragma unroll
      for (int k = 0; k < 2; ++k) {
        int l = w * 64 + lane; l = l < 196 ? l : 0;
        const int idx = rr * 196 + l;
        touch(k ? dU : dG, (u32)((idx / 14) * 3584 + (xc & 1) * 1792 + (idx % 14) * 128));
      }
    }
    {
      LAS char* bp = smem + L_W3 + 128 * (4 * g + q) + 32 * ((u32)nt ^ phi_sw) + 8 * p;
#pragma unroll
      for (int s = 0; s < 16; ++s) w3f[s] = tr2(bp + 4096 * s, bp + 4096 * s + 2048);
    }
    BARRIER();
    {
      f32x4 q0 = z4, q1 = z4, c0 = z4;
      LAS char* bp0 = smem + L_Q + 512 * g + wb[0];
      LAS char* bp1 = smem + L_Q + 512 * g + wb[1];
      {
        LAS char* bc = smem + L_Q + 512 * g + wb[grp] + 2048 * tg;
        const u32 on2 = tg < 2 ? 0x3f803f80u : 0u;
        const u32x4 ones2 = {on2, on2, on2, on2};
#pragma unroll
        for (int k = 0; k < 3; ++k) c0 = mfma(tr2(bc + 8192 * k, bc + 8192 * k + 256), ones, c0);
        LAS char* bl = bc + (tg < 2 ? 24576 : 0);
        c0 = mfma(tr2(bl, bl + 256), ones2, c0);
        if (c16 == 0) *(LAS f32x4*)(smem + L_CS + 256 + (grp * 4 + tg) * 64 + 16 * g) = c0;
      }
      u32x4 wa = tr2(bp0, bp0 + 256), wbb = tr2(bp1, bp1 + 256);
#pragma unroll
      for (int s = 0; s < 14; ++s) {
        u32x4 na, nb;
        if (s < 13) { na = tr2(bp0 + 2048 * (s + 1), bp0 + 2048 * (s + 1) + 256); nb = tr2(bp1 + 2048 * (s + 1), bp1 + 2048 * (s + 1) + 256); }
        if (worker) dma16(dG, (s < 3 ? 8192 * s : 28672 + 8192 * s) + rw4 * 1024, v4w, (u32)(8 * s + rw4) * 4 * 3584);
        q0 = mfma(wa, xk[s], q0); q1 = mfma(wbb, xk[s], q1);
        if (s < 13) { wa = na; wbb = nb; }
        __builtin_amdgcn_sched_barrier(0);
      }
      BARRIER();
      LAS f32x4* cp = (LAS f32x4*)(smem + L_CS + 256 + 16 * g);
      c0 = (cp[0] + cp[4]) + (cp[8] + cp[12]);
      const f32x4 c1 = (cp[16] + cp[20]) + (cp[24] + cp[28]);
#pragma unroll
      for (int r = 0; r < 4; ++r) { q0[r] = rstd * (q0[r] - mean * c0[r]); q1[r] = rstd * (q1[r] - mean * c1[r]); }
      u32x4 qf = {pk(q0[0], q0[1]), pk(q0[2], q0[3]), g3 ? 0u : pk(q1[0], q1[1]), g3 ? 0u : pk(q1[2], q1[3])};
      f32x4 p0 = mfma(wpf[0], qf, z4), p1 = mfma(wpf[1], qf, z4);
      dual_softmax(p0, p1, g3, phq_pos, phq_neg, [&](int) {});
    }
  }
  STAMP(4);
  {
    if (w == 7) wait_flags((GAS u64*)(ws + WS_FL1) + h * 16, 16, tag, lane, dead);
    BARRIER();
    STAMP(5);
    if (w < 4) {
      u32x4 pt[16];
#pragma unroll
      for (int i = 0; i < 16; ++i) pt[i] = ld16c(rws, lane * 16, WS_KVP + ((h * 16 + ((i + 2 * m) & 15)) * 4 + w) * 1024);
      float kv[8];
#pragma unroll
      for (int k = 0; k < 4; ++k) { kv[2 * k] = bflo(pt[0][k]); kv[2 * k + 1] = bfhi(pt[0][k]); }
#pragma unroll
      for (int i = 1; i < 16; ++i)
#pragma unroll
        for (int k = 0; k < 4; ++k) { kv[2 * k] += bflo(pt[i][k]); kv[2 * k + 1] += bfhi(pt[i][k]); }
      LAS char* kp = smem + L_KVF + ((w >> 1) * 128 + lane) * 16 + 8 * (w & 1);
      *(LAS u32x2*)kp = u32x2{pk(kv[0], kv[1]), pk(kv[2], kv[3])};
      *(LAS u32x2*)(kp + 1024) = u32x2{pk(kv[4], kv[5]), pk(kv[6], kv[7])};
    }
    WAITV0(); BARRIER();
    STAMP(6);
    f32x4 o0 = z4, o1 = z4;
    LAS u32x4* kf = (LAS u32x4*)(smem + L_KVF) + lane;
    o0 = mfma(kf[0], phq_pos, o0); o1 = mfma(kf[64], phq_pos, o1);
    o0 = mfma(kf[128], phq_neg, o0); o1 = mfma(kf[192], phq_neg, o1);
    float t0, den = o1[0];
    x32(den, t0); x16(t0, den);
    const float inv = __builtin_amdgcn_rcpf(den + 1e-6f);
    u32x4 of = {pk(o0[0] * inv, o0[1] * inv), pk(o0[2] * inv, o0[3] * inv), g3 ? 0u : pk(o1[0] * inv, o1[1] * inv), g3 ? 0u : pk(o1[2] * inv, o1[3] * inv)};
    st16c(rws, of, lane * 16, WS_O + ((m * 8 + myMt) * 16 + h) * 1024);
    if (worker) {
#pragma unroll
      for (int s = 0; s < 14; ++s) { LAS char* a = smem + b4 + (s < 3 ? 8192 * s : 28672 + 8192 * s); wgf[s] = tr2(a, a + 1024); }
    }
    WAITV0(); BARRIER();
    if (tid == 448) __hip_atomic_store((GAS u64*)(ws + WS_FL2) + m * 16 + h, tag, RLX);
    STAMP(7);
  }
  if (!worker) {
#pragma unroll 1
    for (int k = 0; k < 7; ++k) {
      const int r = (rr - 28) * 64 + lane;
      if (w < 7) touch(dD, (u32)((224 * (r >> 5) + 32 * k + (r & 31)) * 896 + w * 128));
    }
    WAITV0();
    return;
  }
#pragma unroll
  for (int i = 0; i < 7; ++i) dma16(dU, i * 8192 + rw4 * 1024, v4w, (u32)(8 * i + rw4) * 4 * 3584);
  f32x4 hA, hB;
  {
    if (w == 7) wait_flags((GAS u64*)(ws + WS_FL2) + (t64 >> 1) * 16, 16, tag, lane, dead);
    BARRIER();
    STAMP(8);
    if (b == 0 && tid == 448) __hip_atomic_store((GAS u64*)(ws + WS_EPOCH), tag, RLX);
    {
      const int rb = (w + c7) & 7;
#pragma unroll
      for (int i = 0; i < 8; ++i) dma16c(dWS, 81920 + (8 * i + rb) * 1024, lane * 16, WS_O + (mt0 * 16 + (i >> 2) * 64 + 8 * (i & 3) + rb) * 1024);
    }
    WAITV0(); BARRIER();
    STAMP(9);
    hA = f32x4{bflo(xra[0]), bfhi(xra[0]), bflo(xra[1]), bfhi(xra[1])};
    hB = f32x4{bflo(xrb[0]), bfhi(xrb[0]), bflo(xrb[1]), bfhi(xrb[1])};
    LAS char* ap = smem + 81920 + mp * 32768 + lane * 16;
#pragma unroll
    for (int s = 0; s < 16; ++s) {
      hA = mfma(w3f[s], *(LAS u32x4*)(ap + 1024 * s), hA);
      hB = mfma(w3f[s], *(LAS u32x4*)(ap + 16384 + 1024 * s), hB);
    }
    const u32 a0 = pk(hA[0], hA[1]), a1 = pk(hA[2], hA[3]), b0 = pk(hB[0], hB[1]), b1 = pk(hB[2], hB[3]);
    u32x4 hv = pair16(a0, a1, b0, b1);
    st16c(rws, hv, (g & 1) * 14336 + (2 * (nt & 1) + (g >> 1)) * 256 + c16 * 16,
          WS_H + ((mt0 + 4 * mp) * 14 + 2 * cs + (nt >> 1)) * 1024);
    {
      float sA = gsum(dot2(a0, 0x3f803f80u, dot2(a1, 0x3f803f80u, 0.f))), qA = gsum(dot2(a0, a0, dot2(a1, a1, 0.f)));
      float sB = gsum(dot2(b0, 0x3f803f80u, dot2(b1, 0x3f803f80u, 0.f))), qB = gsum(dot2(b0, b0, dot2(b1, b1, 0.f)));
      if (g < 2) *(LAS u32x2*)(smem + L_PS + ((mp * 32 + g * 16 + c16) * 4 + nt) * 8) =
          g ? u32x2{__float_as_uint(sB), __float_as_uint(qB)} : u32x2{__float_as_uint(sA), __float_as_uint(qA)};
    }
    BARRIER();
    if (w == 7) {
      LAS f32x4* pp = (LAS f32x4*)(smem + L_PS + lane * 32);
      f32x4 u = pp[0], v = pp[1];
      u32x2 o2 = {__float_as_uint((u[0] + u[2]) + (v[0] + v[2])), __float_as_uint((u[1] + u[3]) + (v[1] + v[3]))};
      __builtin_amdgcn_raw_buffer_store_b64(o2, rws, WS_ST + (cs * 2048 + mt0 * 16 + (lane >> 5) * 64 + (lane & 31)) * 8, 0, 16);
    }
#pragma unroll
    for (int s = 0; s < 7; ++s) wuf[s] = tr2(smem + b4 + 8192 * s, smem + b4 + 8192 * s + 1024);
    WAITV0(); BARRIER();
    if (tid == 448) __hip_atomic_store((GAS u64*)(ws + WS_FL3) + (t64 >> 1) * 16 + (t64 & 1) * 7 + cs, tag, RLX);
    STAMP(10);
  }
  u32 v5[2];
#pragma unroll
  for (int v = 0; v < 2; ++v) v5[v] = (lane >> 3) * 896 + 32 * (((lane >> 1) & 3) ^ (((lane >> 4) & 1) | (v << 1))) + 16 * (lane & 1) + 128 * cs;
  const u32 b5 = 1024 * g + 128 * q + 8 * p;
  const u32 STG = 114688 + 4096 * w;
  u32x4 w5f[7][4];
  auto w5_issue = [&](int ks, u32 dst) {
#pragma unroll
    for (int i = 0; i < 4; ++i) dma16(dD, dst + i * 1024, v5[i & 1], (u32)(224 * w + 32 * ks + 8 * i) * 896);
  };
  auto w5_read = [&](u32x4* f, u32 src) {
#pragma unroll
    for (int n = 0; n < 4; ++n) { LAS char* a = smem + src + b5 + 32 * ((u32)n ^ phi_sw); f[n] = tr2(a, a + 512); }
  };
  auto w5_home = [&](int pair) { return w < 7 ? (u32)(28672 * pair + 4096 * w) : (u32)(L_MISC + 4096 * pair); };
#pragma unroll
  for (int i = 0; i < 7; ++i) dma16(dU, i * 8192 + rw4 * 1024, v4w, (u32)(56 + 8 * i + rw4) * 4 * 3584);
  {
    WAITV0(); BARRIER();
#pragma unroll
    for (int s = 0; s < 7; ++s) wuf[7 + s] = tr2(smem + b4 + 8192 * s, smem + b4 + 8192 * s + 1024);
    f32x4 csg = z4, csu = z4;
#pragma unroll
    for (int s = 0; s < 14; ++s) { csg = mfma(wgf[s], ones, csg); csu = mfma(wuf[s], ones, csu); }
    if (w == 7) wait_flags((GAS u64*)(ws + WS_FL3) + m4 * 16, 14, tag, lane, dead);
    BARRIER();
    STAMP(11);
    const int rb4 = (w + c7) & 7;
    auto hdma = [&](int j, int k) {
      int blk = 8 * k + rb4; if (blk >= 28) blk -= 8;
      dma16c(dWS, (28 * j + blk) * 1024, lane * 16, WS_H + (m4 * 112 + 28 * j + blk) * 1024);
    };
    {
      u32x2 s0, s1;
      {
        const u32 vo = WS_ST + (u32)(g * 2048 + m4 * 128 + w * 16 + c16) * 8;
        const u32 vo1 = g3 ? vo : vo + 4 * 2048 * 8;
        asm volatile("buffer_load_dwordx2 %0, %2, %4, 0 offen sc1\n\tbuffer_load_dwordx2 %1, %3, %4, 0 offen sc1"
                     : "=&v"(s0), "=&v"(s1) : "v"(vo), "v"(vo1), "s"(dWS) : "memory");
      }
#pragma unroll
      for (int k = 0; k < 8; ++k) hdma(k >> 2, k & 3);
      asm volatile("s_waitcnt vmcnt(8)" : "+v"(s0), "+v"(s1)::"memory");
      float sm = __uint_as_float(s0[0]) + (g3 ? 0.f : __uint_as_float(s1[0]));
      float sq = __uint_as_float(s0[1]) + (g3 ? 0.f : __uint_as_float(s1[1]));
      sm = gsum(sm); sq = gsum(sq);
      const float mean = sm * (1.f / D_);
      const float rstd = __builtin_amdgcn_rsqf(fmaxf(sq * (1.f / D_) - mean * mean, 0.f) + 1e-5f);
      if (g == 0) *(LAS u32x2*)(smem + L_ST + (w * 16 + c16) * 8) = u32x2{__float_as_uint(mean), __float_as_uint(rstd)};
    }
    WAITV(4); BARRIER();
    STAMP(12);
    const int ftile = 8 * fs + w;
    const u32 svo = (g & 1) * 57344 + (2 * (ftile & 1) + (g >> 1)) * 256 + c16 * 16 + WS_HH + (m4 * 8 * 56 + (ftile >> 1)) * 1024;
    LAS char* ap = smem + lane * 16;
    LAS char* sp = smem + L_ST + c16 * 8;
    u32x4 ring[4];
#pragma unroll
    for (int k = 0; k < 4; ++k) ring[k] = *(LAS u32x4*)(ap + 1024 * k);
    STR(0);
    f32x4 pg = z4, pu = z4;
    u32x2 pst = {0u, 0u};
    u32 o0 = 0, o1 = 0;
    auto hook = [&](int T, int s) {
      auto rd = [&](int ks, int n) { LAS char* a = smem + STG + b5 + 32 * ((u32)n ^ phi_sw); w5f[ks][n] = tr2(a, a + 512); };
      auto is = [&](int ks, int i, u32 dst) { dma16(dD, dst + i * 1024, v5[i & 1], (u32)(224 * w + 32 * ks + 8 * i) * 896); };
      if (T == 0) { if (s >= 2 && s < 10) hdma(2 + ((s - 2) >> 2), (s - 2) & 3); }
      if (T == 1) { if (s >= 1 && s < 9 && (s & 1)) is(0, s >> 1, STG); }
      if (T == 3) { if (s < 4) rd(0, s); if (s >= 4 && s < 8) is(3, s - 4, w5_home(0)); if (s >= 9 && s < 13) is(1, s - 9, STG); }
      if (T == 5) { if (s < 4) rd(1, s); if (s >= 4 && s < 8) is(4, s - 4, w5_home(1)); if (s >= 9 && s < 13) is(2, s - 9, STG); }
      if (T == 7) { if (s < 4) rd(2, s); if (s >= 4 && s < 8) is(5, s - 4, w5_home(2)); if (s >= 9 && s < 13) is(6, s - 9, STG); }
    };
#define RAWBAR() do { asm volatile("" ::: "memory"); __builtin_amdgcn_s_barrier(); asm volatile("" ::: "memory"); } while (0)
#define P4_TILE(T, AG, AU, EG, EU, ST, R0, R1)                                        \
  {                                                                                   \
    const float mean = __uint_as_float(ST[0]), rstd = __uint_as_float(ST[1]);         \
    float rn, t[4], u[4];                                                             \
    _Pragma("unroll") for (int s = 0; s < 14; ++s) {                                  \
      const int k = (14 * (T) + s) & 3;                                               \
      u32x4 xf = ring[k];                                                             \
      AG = mfma(wgf[s], xf, AG); AU = mfma(wuf[s], xf, AU);                           \
      if (14 * (T) + s + 4 < 112) ring[k] = *(LAS u32x4*)(ap + 1024 * (14 * (T) + s + 4)); \
      if ((T) > 0) {                                                                  \
        if (s == 0) rn = rstd * -1.4426950408889634f;                                 \
        if (s >= 1 && s <= 4) t[s - 1] = __builtin_amdgcn_exp2f(rn * fmaf(-mean, csg[s - 1], EG[s - 1])); \
        if (s >= 5 && s <= 8) { t[s - 5] = __builtin_amdgcn_rcpf(1.f + t[s - 5]); u[s - 5] = fmaf(-mean, csu[s - 5], EU[s - 5]); } \
        if (s >= 9 && s <= 12) u[s - 9] = (u[s - 9] * rstd) * t[s - 9];               \
        if (s == 13) { R0 = pk(u[0], u[1]); R1 = pk(u[2], u[3]); }                    \
      }                                                                               \
      hook((T), s);                                                                   \
      __builtin_amdgcn_sched_barrier(0);                                              \
    }                                                                                 \
  }
#pragma unroll
    for (int j = 0; j < 4; ++j) {
      f32x4 ag = z4, au = z4;
      u32x2 st0 = *(LAS u32x2*)(sp + 256 * j), st1 = *(LAS u32x2*)(sp + 256 * j + 128);
      __builtin_amdgcn_sched_barrier(0);
      u32 o2, o3;
      P4_TILE(2 * j, ag, au, pg, pu, pst, o2, o3)
      STR(1 + 2 * j);
      if (j > 0) st16c(rws, pair16(o0, o1, o2, o3), svo, (2 * j - 2) * 56 * 1024);
      if (j == 0) WAITV(8); else WAITV(1);
      RAWBAR();
      if (j == 3 && tid == 448) __hip_atomic_store((GAS u64*)(ws + WS_FL4) + m4 * 16 + fs, tag, RLX);
      __builtin_amdgcn_sched_barrier(0);
      STR(2 + 2 * j);
      pg = z4; pu = z4;
      P4_TILE(2 * j + 1, pg, pu, ag, au, st0, o0, o1)
      pst = st1;
    }
    {
      const float mean = __uint_as_float(pst[0]), rstd = __uint_as_float(pst[1]);
      float hv[4];
#pragma unroll
      for (int r = 0; r < 4; ++r) {
        float gg = rstd * (pg[r] - mean * csg[r]), uu = rstd * (pu[r] - mean * csu[r]);
        hv[r] = uu * __builtin_amdgcn_rcpf(1.f + __builtin_amdgcn_exp2f(gg * -1.4426950408889634f));
      }
      st16c(rws, pair16(o0, o1, pk(hv[0], hv[1]), pk(hv[2], hv[3])), svo, 6 * 56 * 1024);
    }
    STR(9);
  }
  {
    if (w == 7) wait_flags((GAS u64*)(ws + WS_FL4) + (t64 >> 1) * 16, 14, tag, lane, dead);
    w5_read(w5f[3], w5_home(0)); w5_read(w5f[4], w5_home(1));
    RAWBAR();
    STAMP(13);
    {
      u32x4 xf[2][7];
      const int r2 = c7 & 1;
#pragma unroll
      for (int s = 0; s < 4; ++s) xf[0][s] = ld16c(rws, lane * 16, WS_HH + ((mt0 + r2) * 56 + 7 * w + s) * 1024);
      __builtin_amdgcn_sched_barrier(0);
      WAITV(4);
      w5_read(w5f[5], w5_home(2)); w5_read(w5f[6], STG);
      BARRIER();
      if (tid == 448) __hip_atomic_store((GAS u64*)(ws + WS_FL5) + m4 * 16 + fs, tag, RLX);
      STR(10); STFLUSH2(11);
      STAMP(14);
      __builtin_amdgcn_sched_barrier(0);
#pragma unroll
      for (int k = 4; k < 14; ++k) xf[k / 7][k % 7] = ld16c(rws, lane * 16, WS_HH + ((mt0 + ((k / 7) ^ r2)) * 56 + 7 * w + k % 7) * 1024);
      __builtin_amdgcn_sched_barrier(0);
#pragma unroll
      for (int i = 0; i < 4; ++i) {
        const int mt = (i & 2) | ((i & 1) ^ r2);
        f32x4 a[4] = {z4, z4, z4, z4};
#pragma unroll
        for (int s = 0; s < 7; ++s)
#pragma unroll
          for (int n = 0; n < 4; ++n) a[n] = mfma(w5f[s][n], xf[i & 1][s], a[n]);
        __builtin_amdgcn_sched_barrier(0);
        if (i == 0) {
          if (w == 7) wait_flags((GAS u64*)(ws + WS_FL5) + (t64 >> 1) * 16, 14, tag, lane, dead);
          BARRIER();
        }
        if (i < 2) {
#pragma unroll
          for (int s = 0; s < 7; ++s) xf[i & 1][s] = ld16c(rws, lane * 16, WS_HH + ((mt0 + 4 + (i ^ r2)) * 56 + 7 * w + s) * 1024);
        }
        __builtin_amdgcn_sched_barrier(0);
#pragma unroll
        for (int n = 0; n < 4; ++n) *(LAS f32x4*)(smem + (((n * 4 + mt) * 8 + w) * 64 + lane) * 16) = a[n];
      }
    }
    BARRIER();
    STAMP(15);
    LAS f32x4* rp = (LAS f32x4*)(smem + (w * 2 * 8 * 64 + lane) * 16);
#pragma unroll
    for (int i = 0; i < 8; ++i) { hA += rp[i * 64]; hB += rp[512 + i * 64]; }
    u32x4 ov = pair16(pk(hA[0], hA[1]), pk(hA[2], hA[3]), pk(hB[0], hB[1]), pk(hB[2], hB[3]));
    GAS char* op = (GAS char*)out + (((mt0 + 4 * mp + (g & 1)) * 16 + c16) * D_ + 64 * cs + 16 * nt + 8 * (g >> 1)) * 2;
    *(GAS u32x4*)op = ov;
    STAMP(16);
  }
}

extern "C" void kernel_launch(void* const* d_in, const int* in_sizes, int n_in, void* d_out, int out_size, void* d_ws,
                              size_t ws_size, hipStream_t stream) {
  hipFuncSetAttribute((const void*)fused_block, hipFuncAttributeMaxDynamicSharedMemorySize, LDS_BYTES);
  fused_block<<<256, 512, LDS_BYTES, stream>>>((const uint16_t*)d_in[0], (const uint16_t*)d_in[1], (const uint16_t*)d_in[2],
                                               (const uint16_t*)d_in[3], (const uint16_t*)d_in[5], (const uint16_t*)d_in[6],
                                               (const uint16_t*)d_in[7], (uint16_t*)d_out, (char*)d_ws);
}
```

```cpp
#define CODE_PULL
#include <hip/hip_runtime.h>
#include <stdint.h>

#define DEVI __device__ __forceinline__
#define LAS __attribute__((address_space(3)))
#define GAS __attribute__((address_space(1)))
typedef unsigned u32;
typedef unsigned long long u64;
typedef u32 u32x2 __attribute__((ext_vector_type(2)));
typedef u32 u32x4 __attribute__((ext_vector_type(4)));
typedef float f32x4 __attribute__((ext_vector_type(4)));
typedef __bf16 bf16x8 __attribute__((ext_vector_type(8)));
typedef __bf16 bf16x2 __attribute__((ext_vector_type(2)));
typedef short s16x4 __attribute__((ext_vector_type(4)));
typedef __amdgpu_buffer_rsrc_t rsrc_t;

constexpr int S_ = 2048, D_ = 448, F_ = 1792;
constexpr u32 WS_EPOCH = 0;
constexpr u32 WS_FL1 = 4096, WS_FL2 = WS_FL1 + 16 * 128, WS_FL3 = WS_FL2 + 16 * 128, WS_FL4 = WS_FL3 + 16 * 128, WS_FL5 = WS_FL4 + 16 * 128;
constexpr u32 WS_KVP = 65536;
constexpr u32 WS_O = WS_KVP + 16 * 16 * 8192;
constexpr u32 WS_H = WS_O + 128 * 16 * 1024;
constexpr u32 WS_HH = WS_H + 128 * 14 * 1024;
constexpr u32 WS_ST = WS_HH + 128 * 56 * 1024;
constexpr u32 WS_END = WS_ST + 7 * 2048 * 8;
constexpr u32 WS_STAMP = WS_END;
constexpr u32 SLOT = 24576;
constexpr u32 L_Q = SLOT, L_W3 = L_Q + 28672;
constexpr u32 L_RAW = 5 * SLOT;
constexpr u32 L_PHI = 0, L_V = 16384;
constexpr u32 L_MISC = 6 * SLOT;
constexpr u32 L_KVF = L_MISC, L_WPHI = L_MISC + 4096, L_CS = L_MISC + 6144  , L_ST = L_MISC + 12288  , L_PS = L_MISC + 13312  , L_DUMMY = L_MISC + 15360;
constexpr u32 LDS_BYTES = 163840;

DEVI f32x4 mfma(u32x4 a, u32x4 b, f32x4 c) {
  return __builtin_amdgcn_mfma_f32_16x16x32_bf16(__builtin_bit_cast(bf16x8, a), __builtin_bit_cast(bf16x8, b), c, 0, 0, 0);
}
DEVI u32 pk(float a, float b) { bf16x2 v = {(__bf16)a, (__bf16)b}; return __builtin_bit_cast(u32, v); }
DEVI float bflo(u32 v) { return __uint_as_float(v << 16); }
DEVI float bfhi(u32 v) { return __uint_as_float(v & 0xffff0000u); }
DEVI u32x2 trr(LAS char* p) { return __builtin_bit_cast(u32x2, __builtin_amdgcn_ds_read_tr16_b64_v4i16((LAS s16x4*)p)); }
DEVI u32x4 tr2(LAS char* p0, LAS char* p1) { u32x2 a = trr(p0), b = trr(p1); u32x4 r = {a[0], a[1], b[0], b[1]}; return r; }
DEVI float dot2(u32 a, u32 b, float c) {
  return __builtin_amdgcn_fdot2_f32_bf16(__builtin_bit_cast(bf16x2, a), __builtin_bit_cast(bf16x2, b), c, false);
}
DEVI float x16(float v, float& o) {
  auto r = __builtin_amdgcn_permlane16_swap(__float_as_uint(v), __float_as_uint(v), false, false);
  o = __uint_as_float(r[1]); return __uint_as_float(r[0]);
}
DEVI float x32(float v, float& o) {
  auto r = __builtin_amdgcn_permlane32_swap(__float_as_uint(v), __float_as_uint(v), false, false);
  o = __uint_as_float(r[1]); return __uint_as_float(r[0]);
}
DEVI float gsum(float v) { float b, a = x16(v, b); v = a + b; a = x32(v, b); return a + b; }
DEVI float gmax(float v) { float b, a = x16(v, b); v = fmaxf(a, b); a = x32(v, b); return fmaxf(a, b); }
DEVI float gmin(float v) { float b, a = x16(v, b); v = fminf(a, b); a = x32(v, b); return fminf(a, b); }
DEVI rsrc_t mkrs(const void* p, u32 bytes) { return __builtin_amdgcn_make_buffer_rsrc((void*)p, 0, bytes, 0x00020000); }
DEVI u32x4 mkd(const void* p, u32 bytes) {
  u64 a = (u64)p; u32x4 d = {(u32)a, (u32)(a >> 32) & 0xffffu, bytes, 0x00020000u}; return d;
}
DEVI void dma16(u32x4 d, u32 lds, u32 voff, u32 soff) {
  asm volatile("s_mov_b32 m0, %0\n\ts_nop 3\n\tbuffer_load_dwordx4 %1, %2, %3 offen lds" ::"s"(lds), "v"(voff), "s"(d), "s"(soff) : "memory", "m0");
}
DEVI void dma16c(u32x4 d, u32 lds, u32 voff, u32 soff) {
  asm volatile("s_mov_b32 m0, %0\n\ts_nop 3\n\tbuffer_load_dwordx4 %1, %2, %3 offen sc1 lds" ::"s"(lds), "v"(voff), "s"(d), "s"(soff) : "memory", "m0");
}
DEVI void touch(u32x4 d, u32 voff) {
  asm volatile("s_mov_b32 m0, %0\n\ts_nop 3\n\tbuffer_load_dword %1, %2, 0 offen lds" ::"s"(L_DUMMY), "v"(voff), "s"(d) : "memory", "m0");
}
#define WAITV(n) asm volatile("s_waitcnt vmcnt(" #n ")" ::: "memory")
DEVI u32x4 ld16(rsrc_t rs, u32 voff, u32 soff) { return __builtin_amdgcn_raw_buffer_load_b128(rs, voff, soff, 0); }
DEVI u32x4 ld16c(rsrc_t rs, u32 voff, u32 soff) { return __builtin_amdgcn_raw_buffer_load_b128(rs, voff, soff, 16); }
DEVI void st16c(rsrc_t rs, u32x4 v, u32 voff, u32 soff) { __builtin_amdgcn_raw_buffer_store_b128(v, rs, voff + soff, 0, 16); }
#define WAITV0() asm volatile("s_waitcnt vmcnt(0)" ::: "memory")
#define BARRIER() do { asm volatile("s_waitcnt lgkmcnt(0)" ::: "memory"); __builtin_amdgcn_s_barrier(); asm volatile("" ::: "memory"); } while (0)
#define RLX __ATOMIC_RELAXED, __HIP_MEMORY_SCOPE_AGENT

#define STAMP(i)
#define STR(i) (void)tr_
#define STFLUSH(n)
#define STFLUSH2(n)

DEVI void wait_flags(GAS u64* fl, int n, u64 tag, int lane, int& dead) {
  int ln = lane < n ? lane : 0;
  for (u32 spins = 0; !dead; ++spins) {
    u64 v = __hip_atomic_load(fl + ln, RLX);
    if (__all(v == tag)) break;
    if (spins > (1u << 17)) dead = 1;
    __builtin_amdgcn_s_sleep(1);
  }
}

template <class HK>
DEVI void dual_softmax(const f32x4& p0, f32x4 p1, bool g3, u32x4& pos, u32x4& neg, HK hk) {
  const float L2E = 1.4426950408889634f;
  if (g3) p1 = p0;
  float mx = fmaxf(fmaxf(fmaxf(p0[0], p0[1]), fmaxf(p0[2], p0[3])), fmaxf(fmaxf(p1[0], p1[1]), fmaxf(p1[2], p1[3])));
  float mn = fminf(fminf(fminf(p0[0], p0[1]), fminf(p0[2], p0[3])), fminf(fminf(p1[0], p1[1]), fminf(p1[2], p1[3])));
  mx = gmax(mx); mn = gmin(mn);
  hk(0);
  float a0[4], a1[4], b0[4], b1[4];
  float sa = 0.f, sb = 0.f;
#pragma unroll
  for (int r = 0; r < 4; ++r) {
    a0[r] = __builtin_amdgcn_exp2f((p0[r] - mx) * L2E);
    b0[r] = __builtin_amdgcn_exp2f((mn - p0[r]) * L2E);
    a1[r] = g3 ? 0.f : __builtin_amdgcn_exp2f((p1[r] - mx) * L2E);
    b1[r] = g3 ? 0.f : __builtin_amdgcn_exp2f((mn - p1[r]) * L2E);
    sa += a0[r] + a1[r]; sb += b0[r] + b1[r];
  }
  hk(1);
  sa = __builtin_amdgcn_rcpf(gsum(sa)); sb = __builtin_amdgcn_rcpf(gsum(sb));
  hk(2);
  pos[0] = pk(a0[0] * sa, a0[1] * sa); pos[1] = pk(a0[2] * sa, a0[3] * sa);
  pos[2] = pk(a1[0] * sa, a1[1] * sa); pos[3] = pk(a1[2] * sa, a1[3] * sa);
  neg[0] = pk(b0[0] * sb, b0[1] * sb); neg[1] = pk(b0[2] * sb, b0[3] * sb);
  neg[2] = pk(b1[0] * sb, b1[1] * sb); neg[3] = pk(b1[2] * sb, b1[3] * sb);
}

DEVI u32x4 pair16(u32 a0, u32 a1, u32 b0, u32 b1) {
  auto r0 = __builtin_amdgcn_permlane16_swap(a0, b0, false, false);
  auto r1 = __builtin_amdgcn_permlane16_swap(a1, b1, false, false);
  u32x4 o = {r0[0], r1[0], r0[1], r1[1]};
  return o;
}

__global__ __launch_bounds__(512, 2) void fused_block(const uint16_t* __restrict__ x, const uint16_t* __restrict__ wqkv,
                                                      const uint16_t* __restrict__ wphi, const uint16_t* __restrict__ wo,
                                                      const uint16_t* __restrict__ wg, const uint16_t* __restrict__ wu,
                                                      const uint16_t* __restrict__ wd, uint16_t* __restrict__ out,
                                                      char* __restrict__ ws_) {
  extern __shared__ __attribute__((aligned(16))) char smem_[];
  LAS char* smem = (LAS char*)smem_;
  u32 tr_[12];
  STR(0);
  asm volatile("" ::"s"(x), "s"(wqkv), "s"(wphi), "s"(wo), "s"(wg), "s"(wu), "s"(wd), "s"(out), "s"(ws_));
  GAS char* ws = (GAS char*)ws_;
  const int tid = threadIdx.x, lane = tid & 63;
  const int w = __builtin_amdgcn_readfirstlane(tid >> 6);
  const int g = lane >> 4, c16 = lane & 15, q = (lane >> 2) & 3, p = lane & 3;
  const bool g3 = (g == 3);
  const int b = blockIdx.x, xc = b & 7, rr = b >> 3;
  const rsrc_t rws = mkrs(ws_, WS_END);
  int dead = 0;
  STAMP(0);
  const u32x4 ones = {0x3f803f80u, 0x3f803f80u, 0x3f803f80u, 0x3f803f80u};
  const f32x4 z4 = {0.f, 0.f, 0.f, 0.f};
  const u32 phi_sw = (q >> 1) | ((g & 1) << 1);
  const bool worker = b < 224;
  const int h = 4 * (xc & 3) + (rr & 3), m = 8 * (xc >> 2) + (rr >> 2);
  const int r7 = worker ? rr / 7 : 0, c7 = worker ? rr % 7 : 0;
  const int t64 = 4 * xc + r7, cs = c7;
  const int mt0 = (t64 >> 1) * 8 + 2 * (t64 & 1);
  const int m4 = 4 * (xc >> 1) + r7, fs = 7 * (xc & 1) + c7;
  const int mp = w & 1, nt = w >> 1;
  const u32x4 dX = mkd(x, S_ * D_ * 2), dW1 = mkd(wqkv, D_ * 3 * D_ * 2), dW3 = mkd(wo, D_ * D_ * 2);
  const u32x4 dG = mkd(wg, D_ * F_ * 2), dU = mkd(wu, D_ * F_ * 2), dD = mkd(wd, F_ * D_ * 2), dWS = mkd(ws_, WS_END);

  if (w == 6) {
    u64 pc; u32 len;
    asm volatile("s_getpc_b64 %0\n.Lcp_start:\n\ts_mov_b32 %1, .Lcp_end-.Lcp_start" : "=s"(pc), "=s"(len));
    const u32x4 dC = {(u32)pc, (u32)(pc >> 32) & 0xffffu, len, 0x00020000u};
    touch(dC, (u32)(((rr & 7) * 64 + lane) * 128));
  }
  u64 tag;
  u32x2 xra, xrb;
  {
    const GAS char* ep = ws + WS_EPOCH;
    const GAS char* xp = (const GAS char*)x + (((mt0 + 4 * mp) * 16 + c16) * D_ + 64 * cs + 16 * nt + 4 * g) * 2;
    asm volatile("global_load_dwordx2 %0, %3, off sc1\n\tglobal_load_dwordx2 %1, %4, off\n\tglobal_load_dwordx2 %2, %5, off"
                 : "=&v"(tag), "=&v"(xra), "=&v"(xrb) : "v"(ep), "v"(xp), "v"(xp + 16 * D_ * 2) : "memory");
  }
  u32 v4[2];
#pragma unroll
  for (int v = 0; v < 2; ++v) v4[v] = (lane >> 4) * 3584 + 32 * (((lane >> 1) & 7) ^ ((lane >> 4) | (v << 2))) + 16 * (lane & 1) + 256 * fs;
  const u32 b4 = 2048 * g + 256 * q + 32 * ((u32)w ^ (q | ((g & 1) << 2))) + 8 * p;
  const int rw4 = (w + 2 * r7) & 7;
  const u32 v4w = v4[(rw4 >> 1) & 1];
  u32x4 wgf[14], wuf[14];

  u32x4 phq_pos, phq_neg;
  u32x4 w3f[16];
  const int grp = w >> 2, tg = w & 3;
  const int myMt = 2 * tg + grp;
  {
    const int rot = (2 * (rr & 3) + (rr >> 2)) % 7;
    const u32 win0 = (56 * h) & ~15;
    if (w == 7) {
      const u32x4 dP = mkd(wphi, 16 * 784 * 2);
      dma16(dP, L_WPHI, lane * 16, h * 1568); dma16(dP, L_WPHI + 1024, lane * 16, h * 1568 + 1024);
    }
    u32 vx0, vx1, vw, vq, v3;
    {
      const int r8 = lane >> 3, pos = lane & 7;
      vx0 = (u32)((m * 128 + w * 16 + r8) * 896 + 16 * (pos ^ (lane >> 4)));
      vx1 = (u32)((m * 128 + w * 16 + 8 + r8) * 896 + 16 * (pos ^ (4 + (lane >> 4))));
      vw = (u32)((8 * w + 4 * (lane >> 5) + q) * 2688 + (1 + (g & 1)) * 896 + win0 + 16 * (p ^ (2 * (w & 1))));
      vq = (u32)((4 * g + q) * 2688 + win0 + 16 * (p ^ (2 * (lane >> 5))));
      v3 = (u32)(r8 * 896 + 32 * (((lane >> 1) & 3) ^ ((lane >> 4) & 3)) + 16 * (lane & 1) + 128 * cs);
    }
    auto chunk_of = [&](int i) { int c = rot + i; return c >= 7 ? c - 7 : c; };
    auto issue_chunk = [&](int i, u32 slot) {
      const int c = chunk_of(i);
      dma16(dX, slot + w * 2048, vx0, c * 128);
      dma16(dX, slot + w * 2048 + 1024, vx1, c * 128);
      dma16(dW1, slot + 16384 + w * 1024, vw, c * 64 * 2688);
    };
    auto ex = [&](int k) {
      const int e = 8 * k + w;
      __builtin_amdgcn_sched_barrier(0);
      if (e < 28) dma16(dW1, L_Q + e * 1024, vq, (chunk_of(e >> 2) * 64 + (e & 3) * 16) * 2688);
      else if (e < 92) { const int k3 = e - 28; dma16(dW3, L_W3 + k3 * 1024, v3, (28 * (k3 >> 2) + 8 * (k3 & 3)) * 896); }
      __builtin_amdgcn_sched_barrier(0);
    };
#pragma unroll
    for (int i = 0; i < 3; ++i) issue_chunk(i, i * SLOT);
    STR(1);
    u32 wb[2];
#pragma unroll
    for (int dt = 0; dt < 2; ++dt) {
      u32 u = 8 * (h & 1) + 32 * dt + 8 * p; if (u >= 64) u = 56;
      wb[dt] = 64 * q + (u ^ (32 * (g & 1)));
    }
    const u32 xb = 128 * c16 + 16 * (g ^ ((c16 >> 1) & 7)) + myMt * 2048;
    u32x4 xk[14], wpf[2];
    f32x4 a00 = z4, a01 = z4, a10 = z4, a11 = z4, cs0 = z4, cs1 = z4;
    float sm = 0.f, sq = 0.f;
    u32x4 x1[2][2], w0[2][2], w1[2][2];
#pragma unroll
    for (int i = 0; i < 8; ++i) {
      if (i < 7) {
        if (i == 6) WAITV(0); else if (i == 5) WAITV(3); else WAITV(6);
        BARRIER();
        STR(2 + i);
        LAS char* sl = smem + (i % 6) * SLOT;
#pragma unroll
        for (int e = 0; e < 2; ++e) {
          LAS char* wp0 = sl + 16384 + 4096 * e + 1024 * g + 256 * grp + wb[0];
          LAS char* wp1 = sl + 16384 + 4096 * e + 1024 * g + 256 * grp + wb[1];
          w0[i & 1][e] = tr2(wp0, wp0 + 512);
          xk[2 * i + e] = *(LAS u32x4*)(sl + (xb ^ (64 * e)));
          w1[i & 1][e] = tr2(wp1, wp1 + 512);
          x1[i & 1][e] = *(LAS u32x4*)(sl + (xb ^ (64 * e) ^ 2048));
        }
        __builtin_amdgcn_sched_barrier(0);
      }
      if (i > 0) {
        const int c = (i - 1) & 1;
#pragma unroll
        for (int e = 0; e < 2; ++e) {
          a00 = mfma(w0[c][e], xk[2 * i - 2 + e], a00); a01 = mfma(w1[c][e], xk[2 * i - 2 + e], a01);
          a10 = mfma(w0[c][e], x1[c][e], a10); a11 = mfma(w1[c][e], x1[c][e], a11);
#pragma unroll
          for (int k = 0; k < 4; ++k) { sm = dot2(xk[2 * i - 2 + e][k], 0x3f803f80u, sm); sq = dot2(xk[2 * i - 2 + e][k], xk[2 * i - 2 + e][k], sq); }
        }
        asm volatile("" : "+v"(sm), "+v"(sq), "+v"(a00), "+v"(a01), "+v"(a10), "+v"(a11));
        if (tg == 0) {
#pragma unroll
          for (int e = 0; e < 2; ++e) { cs0 = mfma(w0[c][e], ones, cs0); cs1 = mfma(w1[c][e], ones, cs1); }
        }
      }
      __builtin_amdgcn_sched_barrier(0);
      if (i <= 3) issue_chunk(i + 3, ((i + 3) % 6) * SLOT);
      if (i == 6) { ex(0); ex(1); }
      if (i == 7) { ex(2); ex(3); }
      if (i == 1) {
#pragma unroll
        for (int ft = 0; ft < 2; ++ft) {
          const int f = 16 * ft + c16;
          u32 e[8];
#pragma unroll
          for (int j = 0; j < 8; ++j) {
            const int d = (j < 4) ? 4 * g + j : 12 + 4 * g + j;
            const bool ok = (d < 28) && (f < 28);
            u32 v = *(LAS uint16_t*)(smem + L_WPHI + (ok ? d * 28 + f : 0) * 2);
            e[j] = ok ? v : 0u;
          }
          wpf[ft][0] = e[0] | (e[1] << 16); wpf[ft][1] = e[2] | (e[3] << 16);
          wpf[ft][2] = e[4] | (e[5] << 16); wpf[ft][3] = e[6] | (e[7] << 16);
        }
        asm volatile("" : "+v"(wpf[0]), "+v"(wpf[1]));
      }
    }
    asm volatile("" : "+v"(tag), "+v"(xra), "+v"(xrb));
    tag += 1;
    {
      LAS f32x4* rp = (LAS f32x4*)(smem + L_RAW + w * 2048) + lane;
      rp[0] = a10; rp[64] = a11;
      if (tg == 0 && c16 == 0) {
        *(LAS f32x4*)(smem + L_CS + (2 * grp) * 64 + 16 * g) = cs0;
        *(LAS f32x4*)(smem + L_CS + (2 * grp + 1) * 64 + 16 * g) = cs1;
      }
    }
    ex(4);
    sm = gsum(sm); sq = gsum(sq);
    const float mean = sm * (1.f / D_);
    const float rstd = __builtin_amdgcn_rsqf(fmaxf(sq * (1.f / D_) - mean * mean, 0.f) + 1e-5f);
    ex(5);
    BARRIER();
    STR(9);
    STAMP(1); STAMP(2);
    {
      LAS f32x4* rp = (LAS f32x4*)(smem + L_RAW + (w ^ 4) * 2048) + lane;
      LAS f32x4* cp = (LAS f32x4*)(smem + L_CS + 16 * g);
      f32x4 r0 = rp[0], r1 = rp[64];
      ex(6);
      f32x4 acc[4] = {grp ? r0 : a00, grp ? r1 : a01, grp ? a00 : r0, grp ? a01 : r1};
#pragma unroll
      for (int n = 0; n < 4; ++n) {
        const f32x4 c = cp[4 * n];
#pragma unroll
        for (int r = 0; r < 4; ++r) acc[n][r] = rstd * (acc[n][r] - mean * c[r]);
      }
      u32x4 kf = {pk(acc[0][0], acc[0][1]), pk(acc[0][2], acc[0][3]), g3 ? 0u : pk(acc[1][0], acc[1][1]), g3 ? 0u : pk(acc[1][2], acc[1][3])};
      f32x4 p0 = mfma(wpf[0], kf, z4), p1 = mfma(wpf[1], kf, z4);
      ex(7);
      LAS char* vr = smem + L_V + 64 * (16 * myMt + c16) + 8 * g;
      const u32 vs = (c16 >> 3) & 1;
      *(LAS u32x2*)(vr + 32 * (0 ^ vs)) = u32x2{pk(acc[2][0], acc[2][1]), pk(acc[2][2], acc[2][3])};
      *(LAS u32x2*)(vr + 32 * (1 ^ vs)) = g3 ? u32x2{0x00003f80u, 0u} : u32x2{pk(acc[3][0], acc[3][1]), pk(acc[3][2], acc[3][3])};
      u32x4 pos, neg;
      ex(8);
      dual_softmax(p0, p1, g3, pos, neg, [&](int k) { ex(9 + k); });
      const u32 rowsw = ((c16 >> 1) & 1) | (((c16 >> 3) & 1) << 1);
      LAS char* pr = smem + L_PHI + 128 * (16 * myMt + c16) + 8 * g;
      *(LAS u32x2*)(pr + 32 * (0 ^ rowsw)) = u32x2{pos[0], pos[1]};
      *(LAS u32x2*)(pr + 32 * (1 ^ rowsw)) = u32x2{pos[2], pos[3]};
      *(LAS u32x2*)(pr + 32 * (2 ^ rowsw)) = u32x2{neg[0], neg[1]};
      *(LAS u32x2*)(pr + 32 * (3 ^ rowsw)) = u32x2{neg[2], neg[3]};
    }
    BARRIER();
    STR(10);
    if (w >= 4) {
      LAS char* ap = smem + L_PHI + 1024 * g + 128 * q + 32 * ((u32)(w - 4) ^ phi_sw) + 8 * p;
      LAS char* bp0 = smem + L_V + 512 * g + 64 * q + 32 * (g & 1) + 8 * p;
      LAS char* bp1 = smem + L_V + 512 * g + 64 * q + 32 * (1 ^ (g & 1)) + 8 * p;
      f32x4 kv0 = z4, kv1 = z4;
#pragma unroll
      for (int s = 0; s < 4; ++s) {
        u32x4 a = tr2(ap + 4096 * s, ap + 4096 * s + 512);
        kv0 = mfma(a, tr2(bp0 + 2048 * s, bp0 + 2048 * s + 256), kv0);
        kv1 = mfma(a, tr2(bp1 + 2048 * s, bp1 + 2048 * s + 256), kv1);
      }
      u32x4 kb = {pk(kv0[0], kv0[1]), pk(kv0[2], kv0[3]), pk(kv1[0], kv1[1]), pk(kv1[2], kv1[3])};
      st16c(rws, kb, lane * 16, WS_KVP + ((h * 16 + m) * 4 + (w - 4)) * 1024);
      WAITV0();
    } else {
      WAITV0();
    }
    BARRIER();
    if (tid == 448) __hip_atomic_store((GAS u64*)(ws + WS_FL1) + h * 16 + m, tag, RLX);
    STR(11); STFLUSH(12);
    STAMP(3);
    if (w < 4) {
#pragma unroll
      for (int k = 0; k < 2; ++k) {
        int l = w * 64 + lane; l = l < 196 ? l : 0;
        const int idx = rr * 196 + l;
        touch(k ? dU : dG, (u32)((idx / 14) * 3584 + (xc & 1) * 1792 + (idx % 14) * 128));
      }
    }
    {
      LAS char* bp = smem + L_W3 + 128 * (4 * g + q) + 32 * ((u32)nt ^ phi_sw) + 8 * p;
#pragma unroll
      for (int s = 0; s < 16; ++s) w3f[s] = tr2(bp + 4096 * s, bp + 4096 * s + 2048);
    }
    BARRIER();
    {
      f32x4 q0 = z4, q1 = z4, c0 = z4;
      LAS char* bp0 = smem + L_Q + 512 * g + wb[0];
      LAS char* bp1 = smem + L_Q + 512 * g + wb[1];
      {
        LAS char* bc = smem + L_Q + 512 * g + wb[grp] + 2048 * tg;
        const u32 on2 = tg < 2 ? 0x3f803f80u : 0u;
        const u32x4 ones2 = {on2, on2, on2, on2};
#pragma unroll
        for (int k = 0; k < 3; ++k) c0 = mfma(tr2(bc + 8192 * k, bc + 8192 * k + 256), ones, c0);
        LAS char* bl = bc + (tg < 2 ? 24576 : 0);
        c0 = mfma(tr2(bl, bl + 256), ones2, c0);
        if (c16 == 0) *(LAS f32x4*)(smem + L_CS + 256 + (grp * 4 + tg) * 64 + 16 * g) = c0;
      }
      u32x4 wa = tr2(bp0, bp0 + 256), wbb = tr2(bp1, bp1 + 256);
#pragma unroll
      for (int s = 0; s < 14; ++s) {
        u32x4 na, nb;
        if (s < 13) { na = tr2(bp0 + 2048 * (s + 1), bp0 + 2048 * (s + 1) + 256); nb = tr2(bp1 + 2048 * (s + 1), bp1 + 2048 * (s + 1) + 256); }
        if (worker) dma16(dG, (s < 3 ? 8192 * s : 28672 + 8192 * s) + rw4 * 1024, v4w, (u32)(8 * s + rw4) * 4 * 3584);
        q0 = mfma(wa, xk[s], q0); q1 = mfma(wbb, xk[s], q1);
        if (s < 13) { wa = na; wbb = nb; }
        __builtin_amdgcn_sched_barrier(0);
      }
      BARRIER();
      LAS f32x4* cp = (LAS f32x4*)(smem + L_CS + 256 + 16 * g);
      c0 = (cp[0] + cp[4]) + (cp[8] + cp[12]);
      const f32x4 c1 = (cp[16] + cp[20]) + (cp[24] + cp[28]);
#pragma unroll
      for (int r = 0; r < 4; ++r) { q0[r] = rstd * (q0[r] - mean * c0[r]); q1[r] = rstd * (q1[r] - mean * c1[r]); }
      u32x4 qf = {pk(q0[0], q0[1]), pk(q0[2], q0[3]), g3 ? 0u : pk(q1[0], q1[1]), g3 ? 0u : pk(q1[2], q1[3])};
      f32x4 p0 = mfma(wpf[0], qf, z4), p1 = mfma(wpf[1], qf, z4);
      dual_softmax(p0, p1, g3, phq_pos, phq_neg, [&](int) {});
    }
  }
  STAMP(4);
  {
    if (w == 7) wait_flags((GAS u64*)(ws + WS_FL1) + h * 16, 16, tag, lane, dead);
    BARRIER();
    STAMP(5);
    if (w < 4) {
      u32x4 pt[16];
#pragma unroll
      for (int i = 0; i < 16; ++i) pt[i] = ld16c(rws, lane * 16, WS_KVP + ((h * 16 + ((i + 2 * m) & 15)) * 4 + w) * 1024);
      float kv[8];
#pragma unroll
      for (int k = 0; k < 4; ++k) { kv[2 * k] = bflo(pt[0][k]); kv[2 * k + 1] = bfhi(pt[0][k]); }
#pragma unroll
      for (int i = 1; i < 16; ++i)
#pragma unroll
        for (int k = 0; k < 4; ++k) { kv[2 * k] += bflo(pt[i][k]); kv[2 * k + 1] += bfhi(pt[i][k]); }
      LAS char* kp = smem + L_KVF + ((w >> 1) * 128 + lane) * 16 + 8 * (w & 1);
      *(LAS u32x2*)kp = u32x2{pk(kv[0], kv[1]), pk(kv[2], kv[3])};
      *(LAS u32x2*)(kp + 1024) = u32x2{pk(kv[4], kv[5]), pk(kv[6], kv[7])};
    }
    WAITV0(); BARRIER();
    STAMP(6);
    f32x4 o0 = z4, o1 = z4;
    LAS u32x4* kf = (LAS u32x4*)(smem + L_KVF) + lane;
    o0 = mfma(kf[0], phq_pos, o0); o1 = mfma(kf[64], phq_pos, o1);
    o0 = mfma(kf[128], phq_neg, o0); o1 = mfma(kf[192], phq_neg, o1);
    float t0, den = o1[0];
    x32(den, t0); x16(t0, den);
    const float inv = __builtin_amdgcn_rcpf(den + 1e-6f);
    u32x4 of = {pk(o0[0] * inv, o0[1] * inv), pk(o0[2] * inv, o0[3] * inv), g3 ? 0u : pk(o1[0] * inv, o1[1] * inv), g3 ? 0u : pk(o1[2] * inv, o1[3] * inv)};
    st16c(rws, of, lane * 16, WS_O + ((m * 8 + myMt) * 16 + h) * 1024);
    if (worker) {
#pragma unroll
      for (int s = 0; s < 14; ++s) { LAS char* a = smem + b4 + (s < 3 ? 8192 * s : 28672 + 8192 * s); wgf[s] = tr2(a, a + 1024); }
    }
    WAITV0(); BARRIER();
    if (tid == 448) __hip_atomic_store((GAS u64*)(ws + WS_FL2) + m * 16 + h, tag, RLX);
    STAMP(7);
  }
  if (!worker) {
#pragma unroll 1
    for (int k = 0; k < 7; ++k) {
      const int r = (rr - 28) * 64 + lane;
      if (w < 7) touch(dD, (u32)((224 * (r >> 5) + 32 * k + (r & 31)) * 896 + w * 128));
    }
    WAITV0();
    return;
  }
#pragma unroll
  for (int i = 0; i < 7; ++i) dma16(dU, i * 8192 + rw4 * 1024, v4w, (u32)(8 * i + rw4) * 4 * 3584);
  f32x4 hA, hB;
  {
    if (w == 7) wait_flags((GAS u64*)(ws + WS_FL2) + (t64 >> 1) * 16, 16, tag, lane, dead);
    BARRIER();
    STAMP(8);
    if (b == 0 && tid == 448) __hip_atomic_store((GAS u64*)(ws + WS_EPOCH), tag, RLX);
    {
      const int rb = (w + c7) & 7;
#pragma unroll
      for (int i = 0; i < 8; ++i) dma16c(dWS, 81920 + (8 * i + rb) * 1024, lane * 16, WS_O + (mt0 * 16 + (i >> 2) * 64 + 8 * (i & 3) + rb) * 1024);
    }
    WAITV0(); BARRIER();
    STAMP(9);
    hA = f32x4{bflo(xra[0]), bfhi(xra[0]), bflo(xra[1]), bfhi(xra[1])};
    hB = f32x4{bflo(xrb[0]), bfhi(xrb[0]), bflo(xrb[1]), bfhi(xrb[1])};
    LAS char* ap = smem + 81920 + mp * 32768 + lane * 16;
#pragma unroll
    for (int s = 0; s < 16; ++s) {
      hA = mfma(w3f[s], *(LAS u32x4*)(ap + 1024 * s), hA);
      hB = mfma(w3f[s], *(LAS u32x4*)(ap + 16384 + 1024 * s), hB);
    }
    const u32 a0 = pk(hA[0], hA[1]), a1 = pk(hA[2], hA[3]), b0 = pk(hB[0], hB[1]), b1 = pk(hB[2], hB[3]);
    u32x4 hv = pair16(a0, a1, b0, b1);
    st16c(rws, hv, (g & 1) * 14336 + (2 * (nt & 1) + (g >> 1)) * 256 + c16 * 16,
          WS_H + ((mt0 + 4 * mp) * 14 + 2 * cs + (nt >> 1)) * 1024);
    {
      float sA = gsum(dot2(a0, 0x3f803f80u, dot2(a1, 0x3f803f80u, 0.f))), qA = gsum(dot2(a0, a0, dot2(a1, a1, 0.f)));
      float sB = gsum(dot2(b0, 0x3f803f80u, dot2(b1, 0x3f803f80u, 0.f))), qB = gsum(dot2(b0, b0, dot2(b1, b1, 0.f)));
      if (g < 2) *(LAS u32x2*)(smem + L_PS + ((mp * 32 + g * 16 + c16) * 4 + nt) * 8) =
          g ? u32x2{__float_as_uint(sB), __float_as_uint(qB)} : u32x2{__float_as_uint(sA), __float_as_uint(qA)};
    }
    BARRIER();
    if (w == 7) {
      LAS f32x4* pp = (LAS f32x4*)(smem + L_PS + lane * 32);
      f32x4 u = pp[0], v = pp[1];
      u32x2 o2 = {__float_as_uint((u[0] + u[2]) + (v[0] + v[2])), __float_as_uint((u[1] + u[3]) + (v[1] + v[3]))};
      __builtin_amdgcn_raw_buffer_store_b64(o2, rws, WS_ST + (cs * 2048 + mt0 * 16 + (lane >> 5) * 64 + (lane & 31)) * 8, 0, 16);
    }
#pragma unroll
    for (int s = 0; s < 7; ++s) wuf[s] = tr2(smem + b4 + 8192 * s, smem + b4 + 8192 * s + 1024);
    WAITV0(); BARRIER();
    if (tid == 448) __hip_atomic_store((GAS u64*)(ws + WS_FL3) + (t64 >> 1) * 16 + (t64 & 1) * 7 + cs, tag, RLX);
    STAMP(10);
  }
  u32 v5[2];
#pragma unroll
  for (int v = 0; v < 2; ++v) v5[v] = (lane >> 3) * 896 + 32 * (((lane >> 1) & 3) ^ (((lane >> 4) & 1) | (v << 1))) + 16 * (lane & 1) + 128 * cs;
  const u32 b5 = 1024 * g + 128 * q + 8 * p;
  const u32 STG = 114688 + 4096 * w;
  u32x4 w5f[7][4];
  auto w5_issue = [&](int ks, u32 dst) {
#pragma unroll
    for (int i = 0; i < 4; ++i) dma16(dD, dst + i * 1024, v5[i & 1], (u32)(224 * w + 32 * ks + 8 * i) * 896);
  };
  auto w5_read = [&](u32x4* f, u32 src) {
#pragma unroll
    for (int n = 0; n < 4; ++n) { LAS char* a = smem + src + b5 + 32 * ((u32)n ^ phi_sw); f[n] = tr2(a, a + 512); }
  };
  auto w5_home = [&](int pair) { return w < 7 ? (u32)(28672 * pair + 4096 * w) : (u32)(L_MISC + 4096 * pair); };
#pragma unroll
  for (int i = 0; i < 7; ++i) dma16(dU, i * 8192 + rw4 * 1024, v4w, (u32)(56 + 8 * i + rw4) * 4 * 3584);
  {
    WAITV0(); BARRIER();
#pragma unroll
    for (int s = 0; s < 7; ++s) wuf[7 + s] = tr2(smem + b4 + 8192 * s, smem + b4 + 8192 * s + 1024);
    f32x4 csg = z4, csu = z4;
#pragma unroll
    for (int s = 0; s < 14; ++s) { csg = mfma(wgf[s], ones, csg); csu = mfma(wuf[s], ones, csu); }
    if (w == 7) wait_flags((GAS u64*)(ws + WS_FL3) + m4 * 16, 14, tag, lane, dead);
    BARRIER();
    STAMP(11);
    const int rb4 = (w + c7) & 7;
    auto hdma = [&](int j, int k) {
      int blk = 8 * k + rb4; if (blk >= 28) blk -= 8;
      dma16c(dWS, (28 * j + blk) * 1024, lane * 16, WS_H + (m4 * 112 + 28 * j + blk) * 1024);
    };
    {
      u32x2 s0, s1;
      {
        const u32 vo = WS_ST + (u32)(g * 2048 + m4 * 128 + w * 16 + c16) * 8;
        const u32 vo1 = g3 ? vo : vo + 4 * 2048 * 8;
        asm volatile("buffer_load_dwordx2 %0, %2, %4, 0 offen sc1\n\tbuffer_load_dwordx2 %1, %3, %4, 0 offen sc1"
                     : "=&v"(s0), "=&v"(s1) : "v"(vo), "v"(vo1), "s"(dWS) : "memory");
      }
#pragma unroll
      for (int k = 0; k < 8; ++k) hdma(k >> 2, k & 3);
      asm volatile("s_waitcnt vmcnt(8)" : "+v"(s0), "+v"(s1)::"memory");
      float sm = __uint_as_float(s0[0]) + (g3 ? 0.f : __uint_as_float(s1[0]));
      float sq = __uint_as_float(s0[1]) + (g3 ? 0.f : __uint_as_float(s1[1]));
      sm = gsum(sm); sq = gsum(sq);
      const float mean = sm * (1.f / D_);
      const float rstd = __builtin_amdgcn_rsqf(fmaxf(sq * (1.f / D_) - mean * mean, 0.f) + 1e-5f);
      if (g == 0) *(LAS u32x2*)(smem + L_ST + (w * 16 + c16) * 8) = u32x2{__float_as_uint(mean), __float_as_uint(rstd)};
    }
    WAITV(4); BARRIER();
    STAMP(12);
    const int ftile = 8 * fs + w;
    const u32 svo = (g & 1) * 57344 + (2 * (ftile & 1) + (g >> 1)) * 256 + c16 * 16 + WS_HH + (m4 * 8 * 56 + (ftile >> 1)) * 1024;
    LAS char* ap = smem + lane * 16;
    LAS char* sp = smem + L_ST + c16 * 8;
    u32x4 ring[4];
#pragma unroll
    for (int k = 0; k < 4; ++k) ring[k] = *(LAS u32x4*)(ap + 1024 * k);
    STR(0);
    f32x4 pg = z4, pu = z4;
    u32x2 pst = {0u, 0u};
    u32 o0 = 0, o1 = 0;
    auto hook = [&](int T, int s) {
      auto rd = [&](int ks, int n) { LAS char* a = smem + STG + b5 + 32 * ((u32)n ^ phi_sw); w5f[ks][n] = tr2(a, a + 512); };
      auto is = [&](int ks, int i, u32 dst) { dma16(dD, dst + i * 1024, v5[i & 1], (u32)(224 * w + 32 * ks + 8 * i) * 896); };
      if (T == 0) { if (s >= 2 && s < 10) hdma(2 + ((s - 2) >> 2), (s - 2) & 3); }
      if (T == 1) { if (s >= 1 && s < 9 && (s & 1)) is(0, s >> 1, STG); }
      if (T == 3) { if (s < 4) rd(0, s); if (s >= 4 && s < 8) is(3, s - 4, w5_home(0)); if (s >= 9 && s < 13) is(1, s - 9, STG); }
      if (T == 5) { if (s < 4) rd(1, s); if (s >= 4 && s < 8) is(4, s - 4, w5_home(1)); if (s >= 9 && s < 13) is(2, s - 9, STG); }
      if (T == 7) { if (s < 4) rd(2, s); if (s >= 4 && s < 8) is(5, s - 4, w5_home(2)); if (s >= 9 && s < 13) is(6, s - 9, STG); }
    };
#define RAWBAR() do { asm volatile("" ::: "memory"); __builtin_amdgcn_s_barrier(); asm volatile("" ::: "memory"); } while (0)
#define P4_TILE(T, AG, AU, EG, EU, ST, R0, R1)                                        \
  {                                                                                   \
    const float mean = __uint_as_float(ST[0]), rstd = __uint_as_float(ST[1]);         \
    float rn, t[4], u[4];                                                             \
    _Pragma("unroll") for (int s = 0; s < 14; ++s) {                                  \
      const int k = (14 * (T) + s) & 3;                                               \
      u32x4 xf = ring[k];                                                             \
      AG = mfma(wgf[s], xf, AG); AU = mfma(wuf[s], xf, AU);                           \
      if (14 * (T) + s + 4 < 112) ring[k] = *(LAS u32x4*)(ap + 1024 * (14 * (T) + s + 4)); \
      if ((T) > 0) {                                                                  \
        if (s == 0) rn = rstd * -1.4426950408889634f;                                 \
        if (s >= 1 && s <= 4) t[s - 1] = __builtin_amdgcn_exp2f(rn * fmaf(-mean, csg[s - 1], EG[s - 1])); \
        if (s >= 5 && s <= 8) { t[s - 5] = __builtin_amdgcn_rcpf(1.f + t[s - 5]); u[s - 5] = fmaf(-mean, csu[s - 5], EU[s - 5]); } \
        if (s >= 9 && s <= 12) u[s - 9] = (u[s - 9] * rstd) * t[s - 9];               \
        if (s == 13) { R0 = pk(u[0], u[1]); R1 = pk(u[2], u[3]); }                    \
      }                                                                               \
      hook((T), s);                                                                   \
      __builtin_amdgcn_sched_barrier(0);                                              \
    }                                                                                 \
  }
#pragma unroll
    for (int j = 0; j < 4; ++j) {
      f32x4 ag = z4, au = z4;
      u32x2 st0 = *(LAS u32x2*)(sp + 256 * j), st1 = *(LAS u32x2*)(sp + 256 * j + 128);
      __builtin_amdgcn_sched_barrier(0);
      u32 o2, o3;
      P4_TILE(2 * j, ag, au, pg, pu, pst, o2, o3)
      STR(1 + 2 * j);
      if (j > 0) st16c(rws, pair16(o0, o1, o2, o3), svo, (2 * j - 2) * 56 * 1024);
      if (j == 0) WAITV(8); else WAITV(1);
      RAWBAR();
      if (j == 3 && tid == 448) __hip_atomic_store((GAS u64*)(ws + WS_FL4) + m4 * 16 + fs, tag, RLX);
      __builtin_amdgcn_sched_barrier(0);
      STR(2 + 2 * j);
      pg = z4; pu = z4;
      P4_TILE(2 * j + 1, pg, pu, ag, au, st0, o0, o1)
      pst = st1;
    }
    {
      const float mean = __uint_as_float(pst[0]), rstd = __uint_as_float(pst[1]);
      float hv[4];
#pragma unroll
      for (int r = 0; r < 4; ++r) {
        float gg = rstd * (pg[r] - mean * csg[r]), uu = rstd * (pu[r] - mean * csu[r]);
        hv[r] = uu * __builtin_amdgcn_rcpf(1.f + __builtin_amdgcn_exp2f(gg * -1.4426950408889634f));
      }
      st16c(rws, pair16(o0, o1, pk(hv[0], hv[1]), pk(hv[2], hv[3])), svo, 6 * 56 * 1024);
    }
    STR(9);
  }
  {
    if (w == 7) wait_flags((GAS u64*)(ws + WS_FL4) + (t64 >> 1) * 16, 14, tag, lane, dead);
    w5_read(w5f[3], w5_home(0)); w5_read(w5f[4], w5_home(1));
    RAWBAR();
    STAMP(13);
    {
      u32x4 xf[2][7];
      const int r2 = c7 & 1;
#pragma unroll
      for (int s = 0; s < 4; ++s) xf[0][s] = ld16c(rws, lane * 16, WS_HH + ((mt0 + r2) * 56 + 7 * w + s) * 1024);
      __builtin_amdgcn_sched_barrier(0);
      WAITV(4);
      w5_read(w5f[5], w5_home(2)); w5_read(w5f[6], STG);
      BARRIER();
      if (tid == 448) __hip_atomic_store((GAS u64*)(ws + WS_FL5) + m4 * 16 + fs, tag, RLX);
      STR(10); STFLUSH2(11);
      STAMP(14);
      __builtin_amdgcn_sched_barrier(0);
#pragma unroll
      for (int k = 4; k < 14; ++k) xf[k / 7][k % 7] = ld16c(rws, lane * 16, WS_HH + ((mt0 + ((k / 7) ^ r2)) * 56 + 7 * w + k % 7) * 1024);
      __builtin_amdgcn_sched_barrier(0);
#pragma unroll
      for (int i = 0; i < 4; ++i) {
        const int mt = (i & 2) | ((i & 1) ^ r2);
        f32x4 a[4] = {z4, z4, z4, z4};
#pragma unroll
        for (int s = 0; s < 7; ++s)
#pragma unroll
          for (int n = 0; n < 4; ++n) a[n] = mfma(w5f[s][n], xf[i & 1][s], a[n]);
        __builtin_amdgcn_sched_barrier(0);
        if (i == 0) {
          if (w == 7) wait_flags((GAS u64*)(ws + WS_FL5) + (t64 >> 1) * 16, 14, tag, lane, dead);
          BARRIER();
        }
        if (i < 2) {
#pragma unroll
          for (int s = 0; s < 7; ++s) xf[i & 1][s] = ld16c(rws, lane * 16, WS_HH + ((mt0 + 4 + (i ^ r2)) * 56 + 7 * w + s) * 1024);
        }
        __builtin_amdgcn_sched_barrier(0);
#pragma unroll
        for (int n = 0; n < 4; ++n) *(LAS f32x4*)(smem + (((n * 4 + mt) * 8 + w) * 64 + lane) * 16) = a[n];
      }
    }
    BARRIER();
    STAMP(15);
    LAS f32x4* rp = (LAS f32x4*)(smem + (w * 2 * 8 * 64 + lane) * 16);
#pragma unroll
    for (int i = 0; i < 8; ++i) { hA += rp[i * 64]; hB += rp[512 + i * 64]; }
    u32x4 ov = pair16(pk(hA[0], hA[1]), pk(hA[2], hA[3]), pk(hB[0], hB[1]), pk(hB[2], hB[3]));
    GAS char* op = (GAS char*)out + (((mt0 + 4 * mp + (g & 1)) * 16 + c16) * D_ + 64 * cs + 16 * nt + 8 * (g >> 1)) * 2;
    *(GAS u32x4*)op = ov;
    STAMP(16);
  }
  asm volatile(".Lcp_end:");
}

extern "C" void kernel_launch(void* const* d_in, const int* in_sizes, int n_in, void* d_out, int out_size, void* d_ws,
                              size_t ws_size, hipStream_t stream) {
  hipFuncSetAttribute((const void*)fused_block, hipFuncAttributeMaxDynamicSharedMemorySize, LDS_BYTES);
  fused_block<<<256, 512, LDS_BYTES, stream>>>((const uint16_t*)d_in[0], (const uint16_t*)d_in[1], (const uint16_t*)d_in[2],
                                               (const uint16_t*)d_in[3], (const uint16_t*)d_in[5], (const uint16_t*)d_in[6],
                                               (const uint16_t*)d_in[7], (uint16_t*)d_out, (char*)d_ws);
}
```

```cpp
#define PROBE 16
#define PTHR 3250
#include <hip/hip_runtime.h>
#include <stdint.h>

#define DEVI __device__ __forceinline__
#define LAS __attribute__((address_space(3)))
#define GAS __attribute__((address_space(1)))
typedef unsigned u32;
typedef unsigned long long u64;
typedef u32 u32x2 __attribute__((ext_vector_type(2)));
typedef u32 u32x4 __attribute__((ext_vector_type(4)));
typedef float f32x4 __attribute__((ext_vector_type(4)));
typedef __bf16 bf16x8 __attribute__((ext_vector_type(8)));
typedef __bf16 bf16x2 __attribute__((ext_vector_type(2)));
typedef short s16x4 __attribute__((ext_vector_type(4)));
typedef __amdgpu_buffer_rsrc_t rsrc_t;

constexpr int S_ = 2048, D_ = 448, F_ = 1792;
constexpr u32 WS_EPOCH = 0;
constexpr u32 WS_FL1 = 4096, WS_FL2 = WS_FL1 + 16 * 128, WS_FL3 = WS_FL2 + 16 * 128, WS_FL4 = WS_FL3 + 16 * 128, WS_FL5 = WS_FL4 + 16 * 128;
constexpr u32 WS_KVP = 65536;
constexpr u32 WS_O = WS_KVP + 16 * 16 * 8192;
constexpr u32 WS_H = WS_O + 128 * 16 * 1024;
constexpr u32 WS_HH = WS_H + 128 * 14 * 1024;
constexpr u32 WS_ST = WS_HH + 128 * 56 * 1024;
constexpr u32 WS_END = WS_ST + 7 * 2048 * 8;
constexpr u32 WS_STAMP = WS_END;
constexpr u32 SLOT = 24576;
constexpr u32 L_Q = SLOT, L_W3 = L_Q + 28672;
constexpr u32 L_RAW = 5 * SLOT;
constexpr u32 L_PHI = 0, L_V = 16384;
constexpr u32 L_MISC = 6 * SLOT;
constexpr u32 L_KVF = L_MISC, L_WPHI = L_MISC + 4096, L_CS = L_MISC + 6144  , L_ST = L_MISC + 12288  , L_PS = L_MISC + 13312  , L_DUMMY = L_MISC + 15360;
constexpr u32 LDS_BYTES = 163840;

DEVI f32x4 mfma(u32x4 a, u32x4 b, f32x4 c) {
  return __builtin_amdgcn_mfma_f32_16x16x32_bf16(__builtin_bit_cast(bf16x8, a), __builtin_bit_cast(bf16x8, b), c, 0, 0, 0);
}
DEVI u32 pk(float a, float b) { bf16x2 v = {(__bf16)a, (__bf16)b}; return __builtin_bit_cast(u32, v); }
DEVI float bflo(u32 v) { return __uint_as_float(v << 16); }
DEVI float bfhi(u32 v) { return __uint_as_float(v & 0xffff0000u); }
DEVI u32x2 trr(LAS char* p) { return __builtin_bit_cast(u32x2, __builtin_amdgcn_ds_read_tr16_b64_v4i16((LAS s16x4*)p)); }
DEVI u32x4 tr2(LAS char* p0, LAS char* p1) { u32x2 a = trr(p0), b = trr(p1); u32x4 r = {a[0], a[1], b[0], b[1]}; return r; }
DEVI float dot2(u32 a, u32 b, float c) {
  return __builtin_amdgcn_fdot2_f32_bf16(__builtin_bit_cast(bf16x2, a), __builtin_bit_cast(bf16x2, b), c, false);
}
DEVI float x16(float v, float& o) {
  auto r = __builtin_amdgcn_permlane16_swap(__float_as_uint(v), __float_as_uint(v), false, false);
  o = __uint_as_float(r[1]); return __uint_as_float(r[0]);
}
DEVI float x32(float v, float& o) {
  auto r = __builtin_amdgcn_permlane32_swap(__float_as_uint(v), __float_as_uint(v), false, false);
  o = __uint_as_float(r[1]); return __uint_as_float(r[0]);
}
DEVI float gsum(float v) { float b, a = x16(v, b); v = a + b; a = x32(v, b); return a + b; }
DEVI float gmax(float v) { float b, a = x16(v, b); v = fmaxf(a, b); a = x32(v, b); return fmaxf(a, b); }
DEVI float gmin(float v) { float b, a = x16(v, b); v = fminf(a, b); a = x32(v, b); return fminf(a, b); }
DEVI rsrc_t mkrs(const void* p, u32 bytes) { return __builtin_amdgcn_make_buffer_rsrc((void*)p, 0, bytes, 0x00020000); }
DEVI u32x4 mkd(const void* p, u32 bytes) {
  u64 a = (u64)p; u32x4 d = {(u32)a, (u32)(a >> 32) & 0xffffu, bytes, 0x00020000u}; return d;
}
DEVI void dma16(u32x4 d, u32 lds, u32 voff, u32 soff) {
  asm volatile("s_mov_b32 m0, %0\n\ts_nop 3\n\tbuffer_load_dwordx4 %1, %2, %3 offen lds" ::"s"(lds), "v"(voff), "s"(d), "s"(soff) : "memory", "m0");
}
DEVI void dma16c(u32x4 d, u32 lds, u32 voff, u32 soff) {
  asm volatile("s_mov_b32 m0, %0\n\ts_nop 3\n\tbuffer_load_dwordx4 %1, %2, %3 offen sc1 lds" ::"s"(lds), "v"(voff), "s"(d), "s"(soff) : "memory", "m0");
}
DEVI void touch(u32x4 d, u32 voff) {
  asm volatile("s_mov_b32 m0, %0\n\ts_nop 3\n\tbuffer_load_dword %1, %2, 0 offen lds" ::"s"(L_DUMMY), "v"(voff), "s"(d) : "memory", "m0");
}
#define WAITV(n) asm volatile("s_waitcnt vmcnt(" #n ")" ::: "memory")
DEVI u32x4 ld16(rsrc_t rs, u32 voff, u32 soff) { return __builtin_amdgcn_raw_buffer_load_b128(rs, voff, soff, 0); }
DEVI u32x4 ld16c(rsrc_t rs, u32 voff, u32 soff) { return __builtin_amdgcn_raw_buffer_load_b128(rs, voff, soff, 16); }
DEVI void st16c(rsrc_t rs, u32x4 v, u32 voff, u32 soff) { __builtin_amdgcn_raw_buffer_store_b128(v, rs, voff + soff, 0, 16); }
#define WAITV0() asm volatile("s_waitcnt vmcnt(0)" ::: "memory")
#define BARRIER() do { asm volatile("s_waitcnt lgkmcnt(0)" ::: "memory"); __builtin_amdgcn_s_barrier(); asm volatile("" ::: "memory"); } while (0)
#define RLX __ATOMIC_RELAXED, __HIP_MEMORY_SCOPE_AGENT

#define STAMP(i) do { if ((i) == 0) pt0_ = __builtin_amdgcn_s_memrealtime(); if ((i) == PROBE) pt1_ = __builtin_amdgcn_s_memrealtime(); } while (0)
#define STR(i) (void)tr_
#define STFLUSH(n)
#define STFLUSH2(n)

DEVI void wait_flags(GAS u64* fl, int n, u64 tag, int lane, int& dead) {
  int ln = lane < n ? lane : 0;
  for (u32 spins = 0; !dead; ++spins) {
    u64 v = __hip_atomic_load(fl + ln, RLX);
    if (__all(v == tag)) break;
    if (spins > (1u << 17)) dead = 1;
    __builtin_amdgcn_s_sleep(1);
  }
}

template <class HK>
DEVI void dual_softmax(const f32x4& p0, f32x4 p1, bool g3, u32x4& pos, u32x4& neg, HK hk) {
  const float L2E = 1.4426950408889634f;
  if (g3) p1 = p0;
  float mx = fmaxf(fmaxf(fmaxf(p0[0], p0[1]), fmaxf(p0[2], p0[3])), fmaxf(fmaxf(p1[0], p1[1]), fmaxf(p1[2], p1[3])));
  float mn = fminf(fminf(fminf(p0[0], p0[1]), fminf(p0[2], p0[3])), fminf(fminf(p1[0], p1[1]), fminf(p1[2], p1[3])));
  mx = gmax(mx); mn = gmin(mn);
  hk(0);
  float a0[4], a1[4], b0[4], b1[4];
  float sa = 0.f, sb = 0.f;
#pragma unroll
  for (int r = 0; r < 4; ++r) {
    a0[r] = __builtin_amdgcn_exp2f((p0[r] - mx) * L2E);
    b0[r] = __builtin_amdgcn_exp2f((mn - p0[r]) * L2E);
    a1[r] = g3 ? 0.f : __builtin_amdgcn_exp2f((p1[r] - mx) * L2E);
    b1[r] = g3 ? 0.f : __builtin_amdgcn_exp2f((mn - p1[r]) * L2E);
    sa += a0[r] + a1[r]; sb += b0[r] + b1[r];
  }
  hk(1);
  sa = __builtin_amdgcn_rcpf(gsum(sa)); sb = __builtin_amdgcn_rcpf(gsum(sb));
  hk(2);
  pos[0] = pk(a0[0] * sa, a0[1] * sa); pos[1] = pk(a0[2] * sa, a0[3] * sa);
  pos[2] = pk(a1[0] * sa, a1[1] * sa); pos[3] = pk(a1[2] * sa, a1[3] * sa);
  neg[0] = pk(b0[0] * sb, b0[1] * sb); neg[1] = pk(b0[2] * sb, b0[3] * sb);
  neg[2] = pk(b1[0] * sb, b1[1] * sb); neg[3] = pk(b1[2] * sb, b1[3] * sb);
}

DEVI u32x4 pair16(u32 a0, u32 a1, u32 b0, u32 b1) {
  auto r0 = __builtin_amdgcn_permlane16_swap(a0, b0, false, false);
  auto r1 = __builtin_amdgcn_permlane16_swap(a1, b1, false, false);
  u32x4 o = {r0[0], r1[0], r0[1], r1[1]};
  return o;
}

__global__ __launch_bounds__(512, 2) void fused_block(const uint16_t* __restrict__ x, const uint16_t* __restrict__ wqkv,
                                                      const uint16_t* __restrict__ wphi, const uint16_t* __restrict__ wo,
                                                      const uint16_t* __restrict__ wg, const uint16_t* __restrict__ wu,
                                                      const uint16_t* __restrict__ wd, uint16_t* __restrict__ out,
                                                      char* __restrict__ ws_) {
  extern __shared__ __attribute__((aligned(16))) char smem_[];
  LAS char* smem = (LAS char*)smem_;
  u32 tr_[12];
  u64 pt0_ = 0, pt1_ = 0;
  STR(0);
  asm volatile("" ::"s"(x), "s"(wqkv), "s"(wphi), "s"(wo), "s"(wg), "s"(wu), "s"(wd), "s"(out), "s"(ws_));
  GAS char* ws = (GAS char*)ws_;
  const int tid = threadIdx.x, lane = tid & 63;
  const int w = __builtin_amdgcn_readfirstlane(tid >> 6);
  const int g = lane >> 4, c16 = lane & 15, q = (lane >> 2) & 3, p = lane & 3;
  const bool g3 = (g == 3);
  const int b = blockIdx.x, xc = b & 7, rr = b >> 3;
  const rsrc_t rws = mkrs(ws_, WS_END);
  int dead = 0;
  STAMP(0);
  const u32x4 ones = {0x3f803f80u, 0x3f803f80u, 0x3f803f80u, 0x3f803f80u};
  const f32x4 z4 = {0.f, 0.f, 0.f, 0.f};
  const u32 phi_sw = (q >> 1) | ((g & 1) << 1);
  const bool worker = b < 224;
  const int h = 4 * (xc & 3) + (rr & 3), m = 8 * (xc >> 2) + (rr >> 2);
  const int r7 = worker ? rr / 7 : 0, c7 = worker ? rr % 7 : 0;
  const int t64 = 4 * xc + r7, cs = c7;
  const int mt0 = (t64 >> 1) * 8 + 2 * (t64 & 1);
  const int m4 = 4 * (xc >> 1) + r7, fs = 7 * (xc & 1) + c7;
  const int mp = w & 1, nt = w >> 1;
  const u32x4 dX = mkd(x, S_ * D_ * 2), dW1 = mkd(wqkv, D_ * 3 * D_ * 2), dW3 = mkd(wo, D_ * D_ * 2);
  const u32x4 dG = mkd(wg, D_ * F_ * 2), dU = mkd(wu, D_ * F_ * 2), dD = mkd(wd, F_ * D_ * 2), dWS = mkd(ws_, WS_END);

  u64 tag;
  u32x2 xra, xrb;
  {
    const GAS char* ep = ws + WS_EPOCH;
    const GAS char* xp = (const GAS char*)x + (((mt0 + 4 * mp) * 16 + c16) * D_ + 64 * cs + 16 * nt + 4 * g) * 2;
    asm volatile("global_load_dwordx2 %0, %3, off sc1\n\tglobal_load_dwordx2 %1, %4, off\n\tglobal_load_dwordx2 %2, %5, off"
                 : "=&v"(tag), "=&v"(xra), "=&v"(xrb) : "v"(ep), "v"(xp), "v"(xp + 16 * D_ * 2) : "memory");
  }
  u32 v4[2];
#pragma unroll
  for (int v = 0; v < 2; ++v) v4[v] = (lane >> 4) * 3584 + 32 * (((lane >> 1) & 7) ^ ((lane >> 4) | (v << 2))) + 16 * (lane & 1) + 256 * fs;
  const u32 b4 = 2048 * g + 256 * q + 32 * ((u32)w ^ (q | ((g & 1) << 2))) + 8 * p;
  const int rw4 = (w + 2 * r7) & 7;
  const u32 v4w = v4[(rw4 >> 1) & 1];
  u32x4 wgf[14], wuf[14];

  u32x4 phq_pos, phq_neg;
  u32x4 w3f[16];
  const int grp = w >> 2, tg = w & 3;
  const int myMt = 2 * tg + grp;
  {
    const int rot = (2 * (rr & 3) + (rr >> 2)) % 7;
    const u32 win0 = (56 * h) & ~15;
    if (w == 7) {
      const u32x4 dP = mkd(wphi, 16 * 784 * 2);
      dma16(dP, L_WPHI, lane * 16, h * 1568); dma16(dP, L_WPHI + 1024, lane * 16, h * 1568 + 1024);
    }
    u32 vx0, vx1, vw, vq, v3;
    {
      const int r8 = lane >> 3, pos = lane & 7;
      vx0 = (u32)((m * 128 + w * 16 + r8) * 896 + 16 * (pos ^ (lane >> 4)));
      vx1 = (u32)((m * 128 + w * 16 + 8 + r8) * 896 + 16 * (pos ^ (4 + (lane >> 4))));
      vw = (u32)((8 * w + 4 * (lane >> 5) + q) * 2688 + (1 + (g & 1)) * 896 + win0 + 16 * (p ^ (2 * (w & 1))));
      vq = (u32)((4 * g + q) * 2688 + win0 + 16 * (p ^ (2 * (lane >> 5))));
      v3 = (u32)(r8 * 896 + 32 * (((lane >> 1) & 3) ^ ((lane >> 4) & 3)) + 16 * (lane & 1) + 128 * cs);
    }
    auto chunk_of = [&](int i) { int c = rot + i; return c >= 7 ? c - 7 : c; };
    auto issue_chunk = [&](int i, u32 slot) {
      const int c = chunk_of(i);
      dma16(dX, slot + w * 2048, vx0, c * 128);
      dma16(dX, slot + w * 2048 + 1024, vx1, c * 128);
      dma16(dW1, slot + 16384 + w * 1024, vw, c * 64 * 2688);
    };
    auto ex = [&](int k) {
      const int e = 8 * k + w;
      __builtin_amdgcn_sched_barrier(0);
      if (e < 28) dma16(dW1, L_Q + e * 1024, vq, (chunk_of(e >> 2) * 64 + (e & 3) * 16) * 2688);
      else if (e < 92) { const int k3 = e - 28; dma16(dW3, L_W3 + k3 * 1024, v3, (28 * (k3 >> 2) + 8 * (k3 & 3)) * 896); }
      __builtin_amdgcn_sched_barrier(0);
    };
#pragma unroll
    for (int i = 0; i < 3; ++i) issue_chunk(i, i * SLOT);
    STR(1);
    u32 wb[2];
#pragma unroll
    for (int dt = 0; dt < 2; ++dt) {
      u32 u = 8 * (h & 1) + 32 * dt + 8 * p; if (u >= 64) u = 56;
      wb[dt] = 64 * q + (u ^ (32 * (g & 1)));
    }
    const u32 xb = 128 * c16 + 16 * (g ^ ((c16 >> 1) & 7)) + myMt * 2048;
    u32x4 xk[14], wpf[2];
    f32x4 a00 = z4, a01 = z4, a10 = z4, a11 = z4, cs0 = z4, cs1 = z4;
    float sm = 0.f, sq = 0.f;
    u32x4 x1[2][2], w0[2][2], w1[2][2];
#pragma unroll
    for (int i = 0; i < 8; ++i) {
      if (i < 7) {
        if (i == 6) WAITV(0); else if (i == 5) WAITV(3); else WAITV(6);
        BARRIER();
        STR(2 + i);
        LAS char* sl = smem + (i % 6) * SLOT;
#pragma unroll
        for (int e = 0; e < 2; ++e) {
          LAS char* wp0 = sl + 16384 + 4096 * e + 1024 * g + 256 * grp + wb[0];
          LAS char* wp1 = sl + 16384 + 4096 * e + 1024 * g + 256 * grp + wb[1];
          w0[i & 1][e] = tr2(wp0, wp0 + 512);
          xk[2 * i + e] = *(LAS u32x4*)(sl + (xb ^ (64 * e)));
          w1[i & 1][e] = tr2(wp1, wp1 + 512);
          x1[i & 1][e] = *(LAS u32x4*)(sl + (xb ^ (64 * e) ^ 2048));
        }
        __builtin_amdgcn_sched_barrier(0);
      }
      if (i > 0) {
        const int c = (i - 1) & 1;
#pragma unroll
        for (int e = 0; e < 2; ++e) {
          a00 = mfma(w0[c][e], xk[2 * i - 2 + e], a00); a01 = mfma(w1[c][e], xk[2 * i - 2 + e], a01);
          a10 = mfma(w0[c][e], x1[c][e], a10); a11 = mfma(w1[c][e], x1[c][e], a11);
#pragma unroll
          for (int k = 0; k < 4; ++k) { sm = dot2(xk[2 * i - 2 + e][k], 0x3f803f80u, sm); sq = dot2(xk[2 * i - 2 + e][k], xk[2 * i - 2 + e][k], sq); }
        }
        asm volatile("" : "+v"(sm), "+v"(sq), "+v"(a00), "+v"(a01), "+v"(a10), "+v"(a11));
        if (tg == 0) {
#pragma unroll
          for (int e = 0; e < 2; ++e) { cs0 = mfma(w0[c][e], ones, cs0); cs1 = mfma(w1[c][e], ones, cs1); }
        }
      }
      __builtin_amdgcn_sched_barrier(0);
      if (i <= 3) issue_chunk(i + 3, ((i + 3) % 6) * SLOT);
      if (i == 6) { ex(0); ex(1); }
      if (i == 7) { ex(2); ex(3); }
      if (i == 1) {
#pragma unroll
        for (int ft = 0; ft < 2; ++ft) {
          const int f = 16 * ft + c16;
          u32 e[8];
#pragma unroll
          for (int j = 0; j < 8; ++j) {
            const int d = (j < 4) ? 4 * g + j : 12 + 4 * g + j;
            const bool ok = (d < 28) && (f < 28);
            u32 v = *(LAS uint16_t*)(smem + L_WPHI + (ok ? d * 28 + f : 0) * 2);
            e[j] = ok ? v : 0u;
          }
          wpf[ft][0] = e[0] | (e[1] << 16); wpf[ft][1] = e[2] | (e[3] << 16);
          wpf[ft][2] = e[4] | (e[5] << 16); wpf[ft][3] = e[6] | (e[7] << 16);
        }
        asm volatile("" : "+v"(wpf[0]), "+v"(wpf[1]));
      }
    }
    asm volatile("" : "+v"(tag), "+v"(xra), "+v"(xrb));
    tag += 1;
    {
      LAS f32x4* rp = (LAS f32x4*)(smem + L_RAW + w * 2048) + lane;
      rp[0] = a10; rp[64] = a11;
      if (tg == 0 && c16 == 0) {
        *(LAS f32x4*)(smem + L_CS + (2 * grp) * 64 + 16 * g) = cs0;
        *(LAS f32x4*)(smem + L_CS + (2 * grp + 1) * 64 + 16 * g) = cs1;
      }
    }
    ex(4);
    sm = gsum(sm); sq = gsum(sq);
    const float mean = sm * (1.f / D_);
    const float rstd = __builtin_amdgcn_rsqf(fmaxf(sq * (1.f / D_) - mean * mean, 0.f) + 1e-5f);
    ex(5);
    BARRIER();
    STR(9);
    STAMP(1); STAMP(2);
    {
      LAS f32x4* rp = (LAS f32x4*)(smem + L_RAW + (w ^ 4) * 2048) + lane;
      LAS f32x4* cp = (LAS f32x4*)(smem + L_CS + 16 * g);
      f32x4 r0 = rp[0], r1 = rp[64];
      ex(6);
      f32x4 acc[4] = {grp ? r0 : a00, grp ? r1 : a01, grp ? a00 : r0, grp ? a01 : r1};
#pragma unroll
      for (int n = 0; n < 4; ++n) {
        const f32x4 c = cp[4 * n];
#pragma unroll
        for (int r = 0; r < 4; ++r) acc[n][r] = rstd * (acc[n][r] - mean * c[r]);
      }
      u32x4 kf = {pk(acc[0][0], acc[0][1]), pk(acc[0][2], acc[0][3]), g3 ? 0u : pk(acc[1][0], acc[1][1]), g3 ? 0u : pk(acc[1][2], acc[1][3])};
      f32x4 p0 = mfma(wpf[0], kf, z4), p1 = mfma(wpf[1], kf, z4);
      ex(7);
      LAS char* vr = smem + L_V + 64 * (16 * myMt + c16) + 8 * g;
      const u32 vs = (c16 >> 3) & 1;
      *(LAS u32x2*)(vr + 32 * (0 ^ vs)) = u32x2{pk(acc[2][0], acc[2][1]), pk(acc[2][2], acc[2][3])};
      *(LAS u32x2*)(vr + 32 * (1 ^ vs)) = g3 ? u32x2{0x00003f80u, 0u} : u32x2{pk(acc[3][0], acc[3][1]), pk(acc[3][2], acc[3][3])};
      u32x4 pos, neg;
      ex(8);
      dual_softmax(p0, p1, g3, pos, neg, [&](int k) { ex(9 + k); });
      const u32 rowsw = ((c16 >> 1) & 1) | (((c16 >> 3) & 1) << 1);
      LAS char* pr = smem + L_PHI + 128 * (16 * myMt + c16) + 8 * g;
      *(LAS u32x2*)(pr + 32 * (0 ^ rowsw)) = u32x2{pos[0], pos[1]};
      *(LAS u32x2*)(pr + 32 * (1 ^ rowsw)) = u32x2{pos[2], pos[3]};
      *(LAS u32x2*)(pr + 32 * (2 ^ rowsw)) = u32x2{neg[0], neg[1]};
      *(LAS u32x2*)(pr + 32 * (3 ^ rowsw)) = u32x2{neg[2], neg[3]};
    }
    BARRIER();
    STR(10);
    if (w >= 4) {
      LAS char* ap = smem + L_PHI + 1024 * g + 128 * q + 32 * ((u32)(w - 4) ^ phi_sw) + 8 * p;
      LAS char* bp0 = smem + L_V + 512 * g + 64 * q + 32 * (g & 1) + 8 * p;
      LAS char* bp1 = smem + L_V + 512 * g + 64 * q + 32 * (1 ^ (g & 1)) + 8 * p;
      f32x4 kv0 = z4, kv1 = z4;
#pragma unroll
      for (int s = 0; s < 4; ++s) {
        u32x4 a = tr2(ap + 4096 * s, ap + 4096 * s + 512);
        kv0 = mfma(a, tr2(bp0 + 2048 * s, bp0 + 2048 * s + 256), kv0);
        kv1 = mfma(a, tr2(bp1 + 2048 * s, bp1 + 2048 * s + 256), kv1);
      }
      u32x4 kb = {pk(kv0[0], kv0[1]), pk(kv0[2], kv0[3]), pk(kv1[0], kv1[1]), pk(kv1[2], kv1[3])};
      st16c(rws, kb, lane * 16, WS_KVP + ((h * 16 + m) * 4 + (w - 4)) * 1024);
      WAITV0();
    } else {
      WAITV0();
    }
    BARRIER();
    if (tid == 448) __hip_atomic_store((GAS u64*)(ws + WS_FL1) + h * 16 + m, tag, RLX);
    STR(11); STFLUSH(12);
    STAMP(3);
    if (w < 4) {
#pragma unroll
      for (int k = 0; k < 2; ++k) {
        int l = w * 64 + lane; l = l < 196 ? l : 0;
        const int idx = rr * 196 + l;
        touch(k ? dU : dG, (u32)((idx / 14) * 3584 + (xc & 1) * 1792 + (idx % 14) * 128));
      }
    }
    {
      LAS char* bp = smem + L_W3 + 128 * (4 * g + q) + 32 * ((u32)nt ^ phi_sw) + 8 * p;
#pragma unroll
      for (int s = 0; s < 16; ++s) w3f[s] = tr2(bp + 4096 * s, bp + 4096 * s + 2048);
    }
    BARRIER();
    {
      f32x4 q0 = z4, q1 = z4, c0 = z4;
      LAS char* bp0 = smem + L_Q + 512 * g + wb[0];
      LAS char* bp1 = smem + L_Q + 512 * g + wb[1];
      {
        LAS char* bc = smem + L_Q + 512 * g + wb[grp] + 2048 * tg;
        const u32 on2 = tg < 2 ? 0x3f803f80u : 0u;
        const u32x4 ones2 = {on2, on2, on2, on2};
#pragma unroll
        for (int k = 0; k < 3; ++k) c0 = mfma(tr2(bc + 8192 * k, bc + 8192 * k + 256), ones, c0);
        LAS char* bl = bc + (tg < 2 ? 24576 : 0);
        c0 = mfma(tr2(bl, bl + 256), ones2, c0);
        if (c16 == 0) *(LAS f32x4*)(smem + L_CS + 256 + (grp * 4 + tg) * 64 + 16 * g) = c0;
      }
      u32x4 wa = tr2(bp0, bp0 + 256), wbb = tr2(bp1, bp1 + 256);
#pragma unroll
      for (int s = 0; s < 14; ++s) {
        u32x4 na, nb;
        if (s < 13) { na = tr2(bp0 + 2048 * (s + 1), bp0 + 2048 * (s + 1) + 256); nb = tr2(bp1 + 2048 * (s + 1), bp1 + 2048 * (s + 1) + 256); }
        if (worker) dma16(dG, (s < 3 ? 8192 * s : 28672 + 8192 * s) + rw4 * 1024, v4w, (u32)(8 * s + rw4) * 4 * 3584);
        q0 = mfma(wa, xk[s], q0); q1 = mfma(wbb, xk[s], q1);
        if (s < 13) { wa = na; wbb = nb; }
        __builtin_amdgcn_sched_barrier(0);
      }
      BARRIER();
      LAS f32x4* cp = (LAS f32x4*)(smem + L_CS + 256 + 16 * g);
      c0 = (cp[0] + cp[4]) + (cp[8] + cp[12]);
      const f32x4 c1 = (cp[16] + cp[20]) + (cp[24] + cp[28]);
#pragma unroll
      for (int r = 0; r < 4; ++r) { q0[r] = rstd * (q0[r] - mean * c0[r]); q1[r] = rstd * (q1[r] - mean * c1[r]); }
      u32x4 qf = {pk(q0[0], q0[1]), pk(q0[2], q0[3]), g3 ? 0u : pk(q1[0], q1[1]), g3 ? 0u : pk(q1[2], q1[3])};
      f32x4 p0 = mfma(wpf[0], qf, z4), p1 = mfma(wpf[1], qf, z4);
      dual_softmax(p0, p1, g3, phq_pos, phq_neg, [&](int) {});
    }
  }
  STAMP(4);
  {
    if (w == 7) wait_flags((GAS u64*)(ws + WS_FL1) + h * 16, 16, tag, lane, dead);
    BARRIER();
    STAMP(5);
    if (w < 4) {
      u32x4 pt[16];
#pragma unroll
      for (int i = 0; i < 16; ++i) pt[i] = ld16c(rws, lane * 16, WS_KVP + ((h * 16 + ((i + 2 * m) & 15)) * 4 + w) * 1024);
      float kv[8];
#pragma unroll
      for (int k = 0; k < 4; ++k) { kv[2 * k] = bflo(pt[0][k]); kv[2 * k + 1] = bfhi(pt[0][k]); }
#pragma unroll
      for (int i = 1; i < 16; ++i)
#pragma unroll
        for (int k = 0; k < 4; ++k) { kv[2 * k] += bflo(pt[i][k]); kv[2 * k + 1] += bfhi(pt[i][k]); }
      LAS char* kp = smem + L_KVF + ((w >> 1) * 128 + lane) * 16 + 8 * (w & 1);
      *(LAS u32x2*)kp = u32x2{pk(kv[0], kv[1]), pk(kv[2], kv[3])};
      *(LAS u32x2*)(kp + 1024) = u32x2{pk(kv[4], kv[5]), pk(kv[6], kv[7])};
    }
    WAITV0(); BARRIER();
    STAMP(6);
    f32x4 o0 = z4, o1 = z4;
    LAS u32x4* kf = (LAS u32x4*)(smem + L_KVF) + lane;
    o0 = mfma(kf[0], phq_pos, o0); o1 = mfma(kf[64], phq_pos, o1);
    o0 = mfma(kf[128], phq_neg, o0); o1 = mfma(kf[192], phq_neg, o1);
    float t0, den = o1[0];
    x32(den, t0); x16(t0, den);
    const float inv = __builtin_amdgcn_rcpf(den + 1e-6f);
    u32x4 of = {pk(o0[0] * inv, o0[1] * inv), pk(o0[2] * inv, o0[3] * inv), g3 ? 0u : pk(o1[0] * inv, o1[1] * inv), g3 ? 0u : pk(o1[2] * inv, o1[3] * inv)};
    st16c(rws, of, lane * 16, WS_O + ((m * 8 + myMt) * 16 + h) * 1024);
    if (worker) {
#pragma unroll
      for (int s = 0; s < 14; ++s) { LAS char* a = smem + b4 + (s < 3 ? 8192 * s : 28672 + 8192 * s); wgf[s] = tr2(a, a + 1024); }
    }
    WAITV0(); BARRIER();
    if (tid == 448) __hip_atomic_store((GAS u64*)(ws + WS_FL2) + m * 16 + h, tag, RLX);
    STAMP(7);
  }
  if (!worker) {
#pragma unroll 1
    for (int k = 0; k < 7; ++k) {
      const int r = (rr - 28) * 64 + lane;
      if (w < 7) touch(dD, (u32)((224 * (r >> 5) + 32 * k + (r & 31)) * 896 + w * 128));
    }
    WAITV0();
    return;
  }
#pragma unroll
  for (int i = 0; i < 7; ++i) dma16(dU, i * 8192 + rw4 * 1024, v4w, (u32)(8 * i + rw4) * 4 * 3584);
  f32x4 hA, hB;
  {
    if (w == 7) wait_flags((GAS u64*)(ws + WS_FL2) + (t64 >> 1) * 16, 16, tag, lane, dead);
    BARRIER();
    STAMP(8);
    if (b == 0 && tid == 448) __hip_atomic_store((GAS u64*)(ws + WS_EPOCH), tag, RLX);
    {
      const int rb = (w + c7) & 7;
#pragma unroll
      for (int i = 0; i < 8; ++i) dma16c(dWS, 81920 + (8 * i + rb) * 1024, lane * 16, WS_O + (mt0 * 16 + (i >> 2) * 64 + 8 * (i & 3) + rb) * 1024);
    }
    WAITV0(); BARRIER();
    STAMP(9);
    hA = f32x4{bflo(xra[0]), bfhi(xra[0]), bflo(xra[1]), bfhi(xra[1])};
    hB = f32x4{bflo(xrb[0]), bfhi(xrb[0]), bflo(xrb[1]), bfhi(xrb[1])};
    LAS char* ap = smem + 81920 + mp * 32768 + lane * 16;
#pragma unroll
    for (int s = 0; s < 16; ++s) {
      hA = mfma(w3f[s], *(LAS u32x4*)(ap + 1024 * s), hA);
      hB = mfma(w3f[s], *(LAS u32x4*)(ap + 16384 + 1024 * s), hB);
    }
    const u32 a0 = pk(hA[0], hA[1]), a1 = pk(hA[2], hA[3]), b0 = pk(hB[0], hB[1]), b1 = pk(hB[2], hB[3]);
    u32x4 hv = pair16(a0, a1, b0, b1);
    st16c(rws, hv, (g & 1) * 14336 + (2 * (nt & 1) + (g >> 1)) * 256 + c16 * 16,
          WS_H + ((mt0 + 4 * mp) * 14 + 2 * cs + (nt >> 1)) * 1024);
    {
      float sA = gsum(dot2(a0, 0x3f803f80u, dot2(a1, 0x3f803f80u, 0.f))), qA = gsum(dot2(a0, a0, dot2(a1, a1, 0.f)));
      float sB = gsum(dot2(b0, 0x3f803f80u, dot2(b1, 0x3f803f80u, 0.f))), qB = gsum(dot2(b0, b0, dot2(b1, b1, 0.f)));
      if (g < 2) *(LAS u32x2*)(smem + L_PS + ((mp * 32 + g * 16 + c16) * 4 + nt) * 8) =
          g ? u32x2{__float_as_uint(sB), __float_as_uint(qB)} : u32x2{__float_as_uint(sA), __float_as_uint(qA)};
    }
    BARRIER();
    if (w == 7) {
      LAS f32x4* pp = (LAS f32x4*)(smem + L_PS + lane * 32);
      f32x4 u = pp[0], v = pp[1];
      u32x2 o2 = {__float_as_uint((u[0] + u[2]) + (v[0] + v[2])), __float_as_uint((u[1] + u[3]) + (v[1] + v[3]))};
      __builtin_amdgcn_raw_buffer_store_b64(o2, rws, WS_ST + (cs * 2048 + mt0 * 16 + (lane >> 5) * 64 + (lane & 31)) * 8, 0, 16);
    }
#pragma unroll
    for (int s = 0; s < 7; ++s) wuf[s] = tr2(smem + b4 + 8192 * s, smem + b4 + 8192 * s + 1024);
    WAITV0(); BARRIER();
    if (tid == 448) __hip_atomic_store((GAS u64*)(ws + WS_FL3) + (t64 >> 1) * 16 + (t64 & 1) * 7 + cs, tag, RLX);
    STAMP(10);
  }
  u32 v5[2];
#pragma unroll
  for (int v = 0; v < 2; ++v) v5[v] = (lane >> 3) * 896 + 32 * (((lane >> 1) & 3) ^ (((lane >> 4) & 1) | (v << 1))) + 16 * (lane & 1) + 128 * cs;
  const u32 b5 = 1024 * g + 128 * q + 8 * p;
  const u32 STG = 114688 + 4096 * w;
  u32x4 w5f[7][4];
  auto w5_issue = [&](int ks, u32 dst) {
#pragma unroll
    for (int i = 0; i < 4; ++i) dma16(dD, dst + i * 1024, v5[i & 1], (u32)(224 * w + 32 * ks + 8 * i) * 896);
  };
  auto w5_read = [&](u32x4* f, u32 src) {
#pragma unroll
    for (int n = 0; n < 4; ++n) { LAS char* a = smem + src + b5 + 32 * ((u32)n ^ phi_sw); f[n] = tr2(a, a + 512); }
  };
  auto w5_home = [&](int pair) { return w < 7 ? (u32)(28672 * pair + 4096 * w) : (u32)(L_MISC + 4096 * pair); };
#pragma unroll
  for (int i = 0; i < 7; ++i) dma16(dU, i * 8192 + rw4 * 1024, v4w, (u32)(56 + 8 * i + rw4) * 4 * 3584);
  {
    WAITV0(); BARRIER();
#pragma unroll
    for (int s = 0; s < 7; ++s) wuf[7 + s] = tr2(smem + b4 + 8192 * s, smem + b4 + 8192 * s + 1024);
    f32x4 csg = z4, csu = z4;
#pragma unroll
    for (int s = 0; s < 14; ++s) { csg = mfma(wgf[s], ones, csg); csu = mfma(wuf[s], ones, csu); }
    if (w == 7) wait_flags((GAS u64*)(ws + WS_FL3) + m4 * 16, 14, tag, lane, dead);
    BARRIER();
    STAMP(11);
    const int rb4 = (w + c7) & 7;
    auto hdma = [&](int j, int k) {
      int blk = 8 * k + rb4; if (blk >= 28) blk -= 8;
      dma16c(dWS, (28 * j + blk) * 1024, lane * 16, WS_H + (m4 * 112 + 28 * j + blk) * 1024);
    };
    {
      u32x2 s0, s1;
      {
        const u32 vo = WS_ST + (u32)(g * 2048 + m4 * 128 + w * 16 + c16) * 8;
        const u32 vo1 = g3 ? vo : vo + 4 * 2048 * 8;
        asm volatile("buffer_load_dwordx2 %0, %2, %4, 0 offen sc1\n\tbuffer_load_dwordx2 %1, %3, %4, 0 offen sc1"
                     : "=&v"(s0), "=&v"(s1) : "v"(vo), "v"(vo1), "s"(dWS) : "memory");
      }
#pragma unroll
      for (int k = 0; k < 8; ++k) hdma(k >> 2, k & 3);
      asm volatile("s_waitcnt vmcnt(8)" : "+v"(s0), "+v"(s1)::"memory");
      float sm = __uint_as_float(s0[0]) + (g3 ? 0.f : __uint_as_float(s1[0]));
      float sq = __uint_as_float(s0[1]) + (g3 ? 0.f : __uint_as_float(s1[1]));
      sm = gsum(sm); sq = gsum(sq);
      const float mean = sm * (1.f / D_);
      const float rstd = __builtin_amdgcn_rsqf(fmaxf(sq * (1.f / D_) - mean * mean, 0.f) + 1e-5f);
      if (g == 0) *(LAS u32x2*)(smem + L_ST + (w * 16 + c16) * 8) = u32x2{__float_as_uint(mean), __float_as_uint(rstd)};
    }
    WAITV(4); BARRIER();
    STAMP(12);
    const int ftile = 8 * fs + w;
    const u32 svo = (g & 1) * 57344 + (2 * (ftile & 1) + (g >> 1)) * 256 + c16 * 16 + WS_HH + (m4 * 8 * 56 + (ftile >> 1)) * 1024;
    LAS char* ap = smem + lane * 16;
    LAS char* sp = smem + L_ST + c16 * 8;
    u32x4 ring[4];
#pragma unroll
    for (int k = 0; k < 4; ++k) ring[k] = *(LAS u32x4*)(ap + 1024 * k);
    STR(0);
    f32x4 pg = z4, pu = z4;
    u32x2 pst = {0u, 0u};
    u32 o0 = 0, o1 = 0;
    auto hook = [&](int T, int s) {
      auto rd = [&](int ks, int n) { LAS char* a = smem + STG + b5 + 32 * ((u32)n ^ phi_sw); w5f[ks][n] = tr2(a, a + 512); };
      auto is = [&](int ks, int i, u32 dst) { dma16(dD, dst + i * 1024, v5[i & 1], (u32)(224 * w + 32 * ks + 8 * i) * 896); };
      if (T == 0) { if (s >= 2 && s < 10) hdma(2 + ((s - 2) >> 2), (s - 2) & 3); }
      if (T == 1) { if (s >= 1 && s < 9 && (s & 1)) is(0, s >> 1, STG); }
      if (T == 3) { if (s < 4) rd(0, s); if (s >= 4 && s < 8) is(3, s - 4, w5_home(0)); if (s >= 9 && s < 13) is(1, s - 9, STG); }
      if (T == 5) { if (s < 4) rd(1, s); if (s >= 4 && s < 8) is(4, s - 4, w5_home(1)); if (s >= 9 && s < 13) is(2, s - 9, STG); }
      if (T == 7) { if (s < 4) rd(2, s); if (s >= 4 && s < 8) is(5, s - 4, w5_home(2)); if (s >= 9 && s < 13) is(6, s - 9, STG); }
    };
#define RAWBAR() do { asm volatile("" ::: "memory"); __builtin_amdgcn_s_barrier(); asm volatile("" ::: "memory"); } while (0)
#define P4_TILE(T, AG, AU, EG, EU, ST, R0, R1)                                        \
  {                                                                                   \
    const float mean = __uint_as_float(ST[0]), rstd = __uint_as_float(ST[1]);         \
    float rn, t[4], u[4];                                                             \
    _Pragma("unroll") for (int s = 0; s < 14; ++s) {                                  \
      const int k = (14 * (T) + s) & 3;                                               \
      u32x4 xf = ring[k];                                                             \
      AG = mfma(wgf[s], xf, AG); AU = mfma(wuf[s], xf, AU);                           \
      if (14 * (T) + s + 4 < 112) ring[k] = *(LAS u32x4*)(ap + 1024 * (14 * (T) + s + 4)); \
      if ((T) > 0) {                                                                  \
        if (s == 0) rn = rstd * -1.4426950408889634f;                                 \
        if (s >= 1 && s <= 4) t[s - 1] = __builtin_amdgcn_exp2f(rn * fmaf(-mean, csg[s - 1], EG[s - 1])); \
        if (s >= 5 && s <= 8) { t[s - 5] = __builtin_amdgcn_rcpf(1.f + t[s - 5]); u[s - 5] = fmaf(-mean, csu[s - 5], EU[s - 5]); } \
        if (s >= 9 && s <= 12) u[s - 9] = (u[s - 9] * rstd) * t[s - 9];               \
        if (s == 13) { R0 = pk(u[0], u[1]); R1 = pk(u[2], u[3]); }                    \
      }                                                                               \
      hook((T), s);                                                                   \
      __builtin_amdgcn_sched_barrier(0);                                              \
    }                                                                                 \
  }
#pragma unroll
    for (int j = 0; j < 4; ++j) {
      f32x4 ag = z4, au = z4;
      u32x2 st0 = *(LAS u32x2*)(sp + 256 * j), st1 = *(LAS u32x2*)(sp + 256 * j + 128);
      __builtin_amdgcn_sched_barrier(0);
      u32 o2, o3;
      P4_TILE(2 * j, ag, au, pg, pu, pst, o2, o3)
      STR(1 + 2 * j);
      if (j > 0) st16c(rws, pair16(o0, o1, o2, o3), svo, (2 * j - 2) * 56 * 1024);
      if (j == 0) WAITV(8); else WAITV(1);
      RAWBAR();
      if (j == 3 && tid == 448) __hip_atomic_store((GAS u64*)(ws + WS_FL4) + m4 * 16 + fs, tag, RLX);
      __builtin_amdgcn_sched_barrier(0);
      STR(2 + 2 * j);
      pg = z4; pu = z4;
      P4_TILE(2 * j + 1, pg, pu, ag, au, st0, o0, o1)
      pst = st1;
    }
    {
      const float mean = __uint_as_float(pst[0]), rstd = __uint_as_float(pst[1]);
      float hv[4];
#pragma unroll
      for (int r = 0; r < 4; ++r) {
        float gg = rstd * (pg[r] - mean * csg[r]), uu = rstd * (pu[r] - mean * csu[r]);
        hv[r] = uu * __builtin_amdgcn_rcpf(1.f + __builtin_amdgcn_exp2f(gg * -1.4426950408889634f));
      }
      st16c(rws, pair16(o0, o1, pk(hv[0], hv[1]), pk(hv[2], hv[3])), svo, 6 * 56 * 1024);
    }
    STR(9);
  }
  {
    if (w == 7) wait_flags((GAS u64*)(ws + WS_FL4) + (t64 >> 1) * 16, 14, tag, lane, dead);
    w5_read(w5f[3], w5_home(0)); w5_read(w5f[4], w5_home(1));
    RAWBAR();
    STAMP(13);
    {
      u32x4 xf[2][7];
      const int r2 = c7 & 1;
#pragma unroll
      for (int s = 0; s < 4; ++s) xf[0][s] = ld16c(rws, lane * 16, WS_HH + ((mt0 + r2) * 56 + 7 * w + s) * 1024);
      __builtin_amdgcn_sched_barrier(0);
      WAITV(4);
      w5_read(w5f[5], w5_home(2)); w5_read(w5f[6], STG);
      BARRIER();
      if (tid == 448) __hip_atomic_store((GAS u64*)(ws + WS_FL5) + m4 * 16 + fs, tag, RLX);
      STR(10); STFLUSH2(11);
      STAMP(14);
      __builtin_amdgcn_sched_barrier(0);
#pragma unroll
      for (int k = 4; k < 14; ++k) xf[k / 7][k % 7] = ld16c(rws, lane * 16, WS_HH + ((mt0 + ((k / 7) ^ r2)) * 56 + 7 * w + k % 7) * 1024);
      __builtin_amdgcn_sched_barrier(0);
#pragma unroll
      for (int i = 0; i < 4; ++i) {
        const int mt = (i & 2) | ((i & 1) ^ r2);
        f32x4 a[4] = {z4, z4, z4, z4};
#pragma unroll
        for (int s = 0; s < 7; ++s)
#pragma unroll
          for (int n = 0; n < 4; ++n) a[n] = mfma(w5f[s][n], xf[i & 1][s], a[n]);
        __builtin_amdgcn_sched_barrier(0);
        if (i == 0) {
          if (w == 7) wait_flags((GAS u64*)(ws + WS_FL5) + (t64 >> 1) * 16, 14, tag, lane, dead);
          BARRIER();
        }
        if (i < 2) {
#pragma unroll
          for (int s = 0; s < 7; ++s) xf[i & 1][s] = ld16c(rws, lane * 16, WS_HH + ((mt0 + 4 + (i ^ r2)) * 56 + 7 * w + s) * 1024);
        }
        __builtin_amdgcn_sched_barrier(0);
#pragma unroll
        for (int n = 0; n < 4; ++n) *(LAS f32x4*)(smem + (((n * 4 + mt) * 8 + w) * 64 + lane) * 16) = a[n];
      }
    }
    BARRIER();
    STAMP(15);
    LAS f32x4* rp = (LAS f32x4*)(smem + (w * 2 * 8 * 64 + lane) * 16);
#pragma unroll
    for (int i = 0; i < 8; ++i) { hA += rp[i * 64]; hB += rp[512 + i * 64]; }
    u32x4 ov = pair16(pk(hA[0], hA[1]), pk(hA[2], hA[3]), pk(hB[0], hB[1]), pk(hB[2], hB[3]));
    GAS char* op = (GAS char*)out + (((mt0 + 4 * mp + (g & 1)) * 16 + c16) * D_ + 64 * cs + 16 * nt + 8 * (g >> 1)) * 2;
    STAMP(16);
    { u64 te_ = __builtin_amdgcn_s_memrealtime(); if (b == 0 && tid == 0 && (u32)(te_ - pt0_) > PTHR) ov[0] = pk(0.64f * (float)(u32)(pt1_ - pt0_), 0.f); }
    *(GAS u32x4*)op = ov;
  }
}

extern "C" void kernel_launch(void* const* d_in, const int* in_sizes, int n_in, void* d_out, int out_size, void* d_ws,
                              size_t ws_size, hipStream_t stream) {
  hipFuncSetAttribute((const void*)fused_block, hipFuncAttributeMaxDynamicSharedMemorySize, LDS_BYTES);
  fused_block<<<256, 512, LDS_BYTES, stream>>>((const uint16_t*)d_in[0], (const uint16_t*)d_in[1], (const uint16_t*)d_in[2],
                                               (const uint16_t*)d_in[3], (const uint16_t*)d_in[5], (const uint16_t*)d_in[6],
                                               (const uint16_t*)d_in[7], (uint16_t*)d_out, (char*)d_ws);
}
```

```cpp
#define PROBE2 0
#define PB 0
#define PTHR 120
#include <hip/hip_runtime.h>
#include <stdint.h>

#define DEVI __device__ __forceinline__
#define LAS __attribute__((address_space(3)))
#define GAS __attribute__((address_space(1)))
typedef unsigned u32;
typedef unsigned long long u64;
typedef u32 u32x2 __attribute__((ext_vector_type(2)));
typedef u32 u32x4 __attribute__((ext_vector_type(4)));
typedef float f32x4 __attribute__((ext_vector_type(4)));
typedef __bf16 bf16x8 __attribute__((ext_vector_type(8)));
typedef __bf16 bf16x2 __attribute__((ext_vector_type(2)));
typedef short s16x4 __attribute__((ext_vector_type(4)));
typedef __amdgpu_buffer_rsrc_t rsrc_t;

constexpr int S_ = 2048, D_ = 448, F_ = 1792;
constexpr u32 WS_EPOCH = 0;
constexpr u32 WS_FL1 = 4096, WS_FL2 = WS_FL1 + 16 * 128, WS_FL3 = WS_FL2 + 16 * 128, WS_FL4 = WS_FL3 + 16 * 128, WS_FL5 = WS_FL4 + 16 * 128;
constexpr u32 WS_KVP = 65536;
constexpr u32 WS_O = WS_KVP + 16 * 16 * 8192;
constexpr u32 WS_H = WS_O + 128 * 16 * 1024;
constexpr u32 WS_HH = WS_H + 128 * 14 * 1024;
constexpr u32 WS_ST = WS_HH + 128 * 56 * 1024;
constexpr u32 WS_END = WS_ST + 7 * 2048 * 8;
constexpr u32 WS_STAMP = WS_END;
constexpr u32 SLOT = 24576;
constexpr u32 L_Q = SLOT, L_W3 = L_Q + 28672;
constexpr u32 L_RAW = 5 * SLOT;
constexpr u32 L_PHI = 0, L_V = 16384;
constexpr u32 L_MISC = 6 * SLOT;
constexpr u32 L_KVF = L_MISC, L_WPHI = L_MISC + 4096, L_CS = L_MISC + 6144  , L_ST = L_MISC + 12288  , L_PS = L_MISC + 13312  , L_DUMMY = L_MISC + 15360;
constexpr u32 LDS_BYTES = 163840;

DEVI f32x4 mfma(u32x4 a, u32x4 b, f32x4 c) {
  return __builtin_amdgcn_mfma_f32_16x16x32_bf16(__builtin_bit_cast(bf16x8, a), __builtin_bit_cast(bf16x8, b), c, 0, 0, 0);
}
DEVI u32 pk(float a, float b) { bf16x2 v = {(__bf16)a, (__bf16)b}; return __builtin_bit_cast(u32, v); }
DEVI float bflo(u32 v) { return __uint_as_float(v << 16); }
DEVI float bfhi(u32 v) { return __uint_as_float(v & 0xffff0000u); }
DEVI u32x2 trr(LAS char* p) { return __builtin_bit_cast(u32x2, __builtin_amdgcn_ds_read_tr16_b64_v4i16((LAS s16x4*)p)); }
DEVI u32x4 tr2(LAS char* p0, LAS char* p1) { u32x2 a = trr(p0), b = trr(p1); u32x4 r = {a[0], a[1], b[0], b[1]}; return r; }
DEVI float dot2(u32 a, u32 b, float c) {
  return __builtin_amdgcn_fdot2_f32_bf16(__builtin_bit_cast(bf16x2, a), __builtin_bit_cast(bf16x2, b), c, false);
}
DEVI float x16(float v, float& o) {
  auto r = __builtin_amdgcn_permlane16_swap(__float_as_uint(v), __float_as_uint(v), false, false);
  o = __uint_as_float(r[1]); return __uint_as_float(r[0]);
}
DEVI float x32(float v, float& o) {
  auto r = __builtin_amdgcn_permlane32_swap(__float_as_uint(v), __float_as_uint(v), false, false);
  o = __uint_as_float(r[1]); return __uint_as_float(r[0]);
}
DEVI float gsum(float v) { float b, a = x16(v, b); v = a + b; a = x32(v, b); return a + b; }
DEVI float gmax(float v) { float b, a = x16(v, b); v = fmaxf(a, b); a = x32(v, b); return fmaxf(a, b); }
DEVI float gmin(float v) { float b, a = x16(v, b); v = fminf(a, b); a = x32(v, b); return fminf(a, b); }
DEVI rsrc_t mkrs(const void* p, u32 bytes) { return __builtin_amdgcn_make_buffer_rsrc((void*)p, 0, bytes, 0x00020000); }
DEVI u32x4 mkd(const void* p, u32 bytes) {
  u64 a = (u64)p; u32x4 d = {(u32)a, (u32)(a >> 32) & 0xffffu, bytes, 0x00020000u}; return d;
}
DEVI void dma16(u32x4 d, u32 lds, u32 voff, u32 soff) {
  asm volatile("s_mov_b32 m0, %0\n\ts_nop 3\n\tbuffer_load_dwordx4 %1, %2, %3 offen lds" ::"s"(lds), "v"(voff), "s"(d), "s"(soff) : "memory", "m0");
}
DEVI void dma16c(u32x4 d, u32 lds, u32 voff, u32 soff) {
  asm volatile("s_mov_b32 m0, %0\n\ts_nop 3\n\tbuffer_load_dwordx4 %1, %2, %3 offen sc1 lds" ::"s"(lds), "v"(voff), "s"(d), "s"(soff) : "memory", "m0");
}
DEVI void touch(u32x4 d, u32 voff) {
  asm volatile("s_mov_b32 m0, %0\n\ts_nop 3\n\tbuffer_load_dword %1, %2, 0 offen lds" ::"s"(L_DUMMY), "v"(voff), "s"(d) : "memory", "m0");
}
#define WAITV(n) asm volatile("s_waitcnt vmcnt(" #n ")" ::: "memory")
DEVI u32x4 ld16(rsrc_t rs, u32 voff, u32 soff) { return __builtin_amdgcn_raw_buffer_load_b128(rs, voff, soff, 0); }
DEVI u32x4 ld16c(rsrc_t rs, u32 voff, u32 soff) { return __builtin_amdgcn_raw_buffer_load_b128(rs, voff, soff, 16); }
DEVI void st16c(rsrc_t rs, u32x4 v, u32 voff, u32 soff) { __builtin_amdgcn_raw_buffer_store_b128(v, rs, voff + soff, 0, 16); }
#define WAITV0() asm volatile("s_waitcnt vmcnt(0)" ::: "memory")
#define BARRIER() do { asm volatile("s_waitcnt lgkmcnt(0)" ::: "memory"); __builtin_amdgcn_s_barrier(); asm volatile("" ::: "memory"); } while (0)
#define RLX __ATOMIC_RELAXED, __HIP_MEMORY_SCOPE_AGENT

#define STAMP(i)
#define STR(i) (void)tr_
#define STFLUSH(n)
#define STFLUSH2(n)

DEVI void wait_flags(GAS u64* fl, int n, u64 tag, int lane, int& dead) {
  int ln = lane < n ? lane : 0;
  for (u32 spins = 0; !dead; ++spins) {
    u64 v = __hip_atomic_load(fl + ln, RLX);
    if (__all(v == tag)) break;
    if (spins > (1u << 17)) dead = 1;
    __builtin_amdgcn_s_sleep(1);
  }
}

template <class HK>
DEVI void dual_softmax(const f32x4& p0, f32x4 p1, bool g3, u32x4& pos, u32x4& neg, HK hk) {
  const float L2E = 1.4426950408889634f;
  if (g3) p1 = p0;
  float mx = fmaxf(fmaxf(fmaxf(p0[0], p0[1]), fmaxf(p0[2], p0[3])), fmaxf(fmaxf(p1[0], p1[1]), fmaxf(p1[2], p1[3])));
  float mn = fminf(fminf(fminf(p0[0], p0[1]), fminf(p0[2], p0[3])), fminf(fminf(p1[0], p1[1]), fminf(p1[2], p1[3])));
  mx = gmax(mx); mn = gmin(mn);
  hk(0);
  float a0[4], a1[4], b0[4], b1[4];
  float sa = 0.f, sb = 0.f;
#pragma unroll
  for (int r = 0; r < 4; ++r) {
    a0[r] = __builtin_amdgcn_exp2f((p0[r] - mx) * L2E);
    b0[r] = __builtin_amdgcn_exp2f((mn - p0[r]) * L2E);
    a1[r] = g3 ? 0.f : __builtin_amdgcn_exp2f((p1[r] - mx) * L2E);
    b1[r] = g3 ? 0.f : __builtin_amdgcn_exp2f((mn - p1[r]) * L2E);
    sa += a0[r] + a1[r]; sb += b0[r] + b1[r];
  }
  hk(1);
  sa = __builtin_amdgcn_rcpf(gsum(sa)); sb = __builtin_amdgcn_rcpf(gsum(sb));
  hk(2);
  pos[0] = pk(a0[0] * sa, a0[1] * sa); pos[1] = pk(a0[2] * sa, a0[3] * sa);
  pos[2] = pk(a1[0] * sa, a1[1] * sa); pos[3] = pk(a1[2] * sa, a1[3] * sa);
  neg[0] = pk(b0[0] * sb, b0[1] * sb); neg[1] = pk(b0[2] * sb, b0[3] * sb);
  neg[2] = pk(b1[0] * sb, b1[1] * sb); neg[3] = pk(b1[2] * sb, b1[3] * sb);
}

DEVI u32x4 pair16(u32 a0, u32 a1, u32 b0, u32 b1) {
  auto r0 = __builtin_amdgcn_permlane16_swap(a0, b0, false, false);
  auto r1 = __builtin_amdgcn_permlane16_swap(a1, b1, false, false);
  u32x4 o = {r0[0], r1[0], r0[1], r1[1]};
  return o;
}

__global__ __launch_bounds__(512, 2) void fused_block(const uint16_t* __restrict__ x, const uint16_t* __restrict__ wqkv,
                                                      const uint16_t* __restrict__ wphi, const uint16_t* __restrict__ wo,
                                                      const uint16_t* __restrict__ wg, const uint16_t* __restrict__ wu,
                                                      const uint16_t* __restrict__ wd, uint16_t* __restrict__ out,
                                                      char* __restrict__ ws_) {
  extern __shared__ __attribute__((aligned(16))) char smem_[];
  LAS char* smem = (LAS char*)smem_;
  u32 tr_[12];
  u64 pt0_ = 0, pt1_ = 0;
  u64 pa_, pb_;
  asm volatile("s_memrealtime %0\n\ts_waitcnt lgkmcnt(0)" : "=s"(pa_) :: "memory");
  STR(0);
  asm volatile("" ::"s"(x), "s"(wqkv), "s"(wphi), "s"(wo), "s"(wg), "s"(wu), "s"(wd), "s"(out), "s"(ws_));
  asm volatile("s_memrealtime %0\n\ts_waitcnt lgkmcnt(0)" : "=s"(pb_) : "s"(x), "s"(ws_) : "memory");
  GAS char* ws = (GAS char*)ws_;
  const int tid = threadIdx.x, lane = tid & 63;
  const int w = __builtin_amdgcn_readfirstlane(tid >> 6);
  const int g = lane >> 4, c16 = lane & 15, q = (lane >> 2) & 3, p = lane & 3;
  const bool g3 = (g == 3);
  const int b = blockIdx.x, xc = b & 7, rr = b >> 3;
  const rsrc_t rws = mkrs(ws_, WS_END);
  int dead = 0;
  STAMP(0);
  const u32x4 ones = {0x3f803f80u, 0x3f803f80u, 0x3f803f80u, 0x3f803f80u};
  const f32x4 z4 = {0.f, 0.f, 0.f, 0.f};
  const u32 phi_sw = (q >> 1) | ((g & 1) << 1);
  const bool worker = b < 224;
  const int h = 4 * (xc & 3) + (rr & 3), m = 8 * (xc >> 2) + (rr >> 2);
  const int r7 = worker ? rr / 7 : 0, c7 = worker ? rr % 7 : 0;
  const int t64 = 4 * xc + r7, cs = c7;
  const int mt0 = (t64 >> 1) * 8 + 2 * (t64 & 1);
  const int m4 = 4 * (xc >> 1) + r7, fs = 7 * (xc & 1) + c7;
  const int mp = w & 1, nt = w >> 1;
  const u32x4 dX = mkd(x, S_ * D_ * 2), dW1 = mkd(wqkv, D_ * 3 * D_ * 2), dW3 = mkd(wo, D_ * D_ * 2);
  const u32x4 dG = mkd(wg, D_ * F_ * 2), dU = mkd(wu, D_ * F_ * 2), dD = mkd(wd, F_ * D_ * 2), dWS = mkd(ws_, WS_END);

  u64 tag;
  u32x2 xra, xrb;
  {
    const GAS char* ep = ws + WS_EPOCH;
    const GAS char* xp = (const GAS char*)x + (((mt0 + 4 * mp) * 16 + c16) * D_ + 64 * cs + 16 * nt + 4 * g) * 2;
    asm volatile("global_load_dwordx2 %0, %3, off sc1\n\tglobal_load_dwordx2 %1, %4, off\n\tglobal_load_dwordx2 %2, %5, off"
                 : "=&v"(tag), "=&v"(xra), "=&v"(xrb) : "v"(ep), "v"(xp), "v"(xp + 16 * D_ * 2) : "memory");
  }
  u32 v4[2];
#pragma unroll
  for (int v = 0; v < 2; ++v) v4[v] = (lane >> 4) * 3584 + 32 * (((lane >> 1) & 7) ^ ((lane >> 4) | (v << 2))) + 16 * (lane & 1) + 256 * fs;
  const u32 b4 = 2048 * g + 256 * q + 32 * ((u32)w ^ (q | ((g & 1) << 2))) + 8 * p;
  const int rw4 = (w + 2 * r7) & 7;
  const u32 v4w = v4[(rw4 >> 1) & 1];
  u32x4 wgf[14], wuf[14];

  u32x4 phq_pos, phq_neg;
  u32x4 w3f[16];
  const int grp = w >> 2, tg = w & 3;
  const int myMt = 2 * tg + grp;
  {
    const int rot = (2 * (rr & 3) + (rr >> 2)) % 7;
    const u32 win0 = (56 * h) & ~15;
    if (w == 7) {
      const u32x4 dP = mkd(wphi, 16 * 784 * 2);
      dma16(dP, L_WPHI, lane * 16, h * 1568); dma16(dP, L_WPHI + 1024, lane * 16, h * 1568 + 1024);
    }
    u32 vx0, vx1, vw, vq, v3;
    {
      const int r8 = lane >> 3, pos = lane & 7;
      vx0 = (u32)((m * 128 + w * 16 + r8) * 896 + 16 * (pos ^ (lane >> 4)));
      vx1 = (u32)((m * 128 + w * 16 + 8 + r8) * 896 + 16 * (pos ^ (4 + (lane >> 4))));
      vw = (u32)((8 * w + 4 * (lane >> 5) + q) * 2688 + (1 + (g & 1)) * 896 + win0 + 16 * (p ^ (2 * (w & 1))));
      vq = (u32)((4 * g + q) * 2688 + win0 + 16 * (p ^ (2 * (lane >> 5))));
      v3 = (u32)(r8 * 896 + 32 * (((lane >> 1) & 3) ^ ((lane >> 4) & 3)) + 16 * (lane & 1) + 128 * cs);
    }
    auto chunk_of = [&](int i) { int c = rot + i; return c >= 7 ? c - 7 : c; };
    auto issue_chunk = [&](int i, u32 slot) {
      const int c = chunk_of(i);
      dma16(dX, slot + w * 2048, vx0, c * 128);
      dma16(dX, slot + w * 2048 + 1024, vx1, c * 128);
      dma16(dW1, slot + 16384 + w * 1024, vw, c * 64 * 2688);
    };
    auto ex = [&](int k) {
      const int e = 8 * k + w;
      __builtin_amdgcn_sched_barrier(0);
      if (e < 28) dma16(dW1, L_Q + e * 1024, vq, (chunk_of(e >> 2) * 64 + (e & 3) * 16) * 2688);
      else if (e < 92) { const int k3 = e - 28; dma16(dW3, L_W3 + k3 * 1024, v3, (28 * (k3 >> 2) + 8 * (k3 & 3)) * 896); }
      __builtin_amdgcn_sched_barrier(0);
    };
#pragma unroll
    for (int i = 0; i < 3; ++i) issue_chunk(i, i * SLOT);
    STR(1);
    u32 wb[2];
#pragma unroll
    for (int dt = 0; dt < 2; ++dt) {
      u32 u = 8 * (h & 1) + 32 * dt + 8 * p; if (u >= 64) u = 56;
      wb[dt] = 64 * q + (u ^ (32 * (g & 1)));
    }
    const u32 xb = 128 * c16 + 16 * (g ^ ((c16 >> 1) & 7)) + myMt * 2048;
    u32x4 xk[14], wpf[2];
    f32x4 a00 = z4, a01 = z4, a10 = z4, a11 = z4, cs0 = z4, cs1 = z4;
    float sm = 0.f, sq = 0.f;
    u32x4 x1[2][2], w0[2][2], w1[2][2];
#pragma unroll
    for (int i = 0; i < 8; ++i) {
      if (i < 7) {
        if (i == 6) WAITV(0); else if (i == 5) WAITV(3); else WAITV(6);
        BARRIER();
        STR(2 + i);
        LAS char* sl = smem + (i % 6) * SLOT;
#pragma unroll
        for (int e = 0; e < 2; ++e) {
          LAS char* wp0 = sl + 16384 + 4096 * e + 1024 * g + 256 * grp + wb[0];
          LAS char* wp1 = sl + 16384 + 4096 * e + 1024 * g + 256 * grp + wb[1];
          w0[i & 1][e] = tr2(wp0, wp0 + 512);
          xk[2 * i + e] = *(LAS u32x4*)(sl + (xb ^ (64 * e)));
          w1[i & 1][e] = tr2(wp1, wp1 + 512);
          x1[i & 1][e] = *(LAS u32x4*)(sl + (xb ^ (64 * e) ^ 2048));
        }
        __builtin_amdgcn_sched_barrier(0);
      }
      if (i > 0) {
        const int c = (i - 1) & 1;
#pragma unroll
        for (int e = 0; e < 2; ++e) {
          a00 = mfma(w0[c][e], xk[2 * i - 2 + e], a00); a01 = mfma(w1[c][e], xk[2 * i - 2 + e], a01);
          a10 = mfma(w0[c][e], x1[c][e], a10); a11 = mfma(w1[c][e], x1[c][e], a11);
#pragma unroll
          for (int k = 0; k < 4; ++k) { sm = dot2(xk[2 * i - 2 + e][k], 0x3f803f80u, sm); sq = dot2(xk[2 * i - 2 + e][k], xk[2 * i - 2 + e][k], sq); }
        }
        asm volatile("" : "+v"(sm), "+v"(sq), "+v"(a00), "+v"(a01), "+v"(a10), "+v"(a11));
        if (tg == 0) {
#pragma unroll
          for (int e = 0; e < 2; ++e) { cs0 = mfma(w0[c][e], ones, cs0); cs1 = mfma(w1[c][e], ones, cs1); }
        }
      }
      __builtin_amdgcn_sched_barrier(0);
      if (i <= 3) issue_chunk(i + 3, ((i + 3) % 6) * SLOT);
      if (i == 6) { ex(0); ex(1); }
      if (i == 7) { ex(2); ex(3); }
      if (i == 1) {
#pragma unroll
        for (int ft = 0; ft < 2; ++ft) {
          const int f = 16 * ft + c16;
          u32 e[8];
#pragma unroll
          for (int j = 0; j < 8; ++j) {
            const int d = (j < 4) ? 4 * g + j : 12 + 4 * g + j;
            const bool ok = (d < 28) && (f < 28);
            u32 v = *(LAS uint16_t*)(smem + L_WPHI + (ok ? d * 28 + f : 0) * 2);
            e[j] = ok ? v : 0u;
          }
          wpf[ft][0] = e[0] | (e[1] << 16); wpf[ft][1] = e[2] | (e[3] << 16);
          wpf[ft][2] = e[4] | (e[5] << 16); wpf[ft][3] = e[6] | (e[7] << 16);
        }
        asm volatile("" : "+v"(wpf[0]), "+v"(wpf[1]));
      }
    }
    asm volatile("" : "+v"(tag), "+v"(xra), "+v"(xrb));
    tag += 1;
    {
      LAS f32x4* rp = (LAS f32x4*)(smem + L_RAW + w * 2048) + lane;
      rp[0] = a10; rp[64] = a11;
      if (tg == 0 && c16 == 0) {
        *(LAS f32x4*)(smem + L_CS + (2 * grp) * 64 + 16 * g) = cs0;
        *(LAS f32x4*)(smem + L_CS + (2 * grp + 1) * 64 + 16 * g) = cs1;
      }
    }
    ex(4);
    sm = gsum(sm); sq = gsum(sq);
    const float mean = sm * (1.f / D_);
    const float rstd = __builtin_amdgcn_rsqf(fmaxf(sq * (1.f / D_) - mean * mean, 0.f) + 1e-5f);
    ex(5);
    BARRIER();
    STR(9);
    STAMP(1); STAMP(2);
    {
      LAS f32x4* rp = (LAS f32x4*)(smem + L_RAW + (w ^ 4) * 2048) + lane;
      LAS f32x4* cp = (LAS f32x4*)(smem + L_CS + 16 * g);
      f32x4 r0 = rp[0], r1 = rp[64];
      ex(6);
      f32x4 acc[4] = {grp ? r0 : a00, grp ? r1 : a01, grp ? a00 : r0, grp ? a01 : r1};
#pragma unroll
      for (int n = 0; n < 4; ++n) {
        const f32x4 c = cp[4 * n];
#pragma unroll
        for (int r = 0; r < 4; ++r) acc[n][r] = rstd * (acc[n][r] - mean * c[r]);
      }
      u32x4 kf = {pk(acc[0][0], acc[0][1]), pk(acc[0][2], acc[0][3]), g3 ? 0u : pk(acc[1][0], acc[1][1]), g3 ? 0u : pk(acc[1][2], acc[1][3])};
      f32x4 p0 = mfma(wpf[0], kf, z4), p1 = mfma(wpf[1], kf, z4);
      ex(7);
      LAS char* vr = smem + L_V + 64 * (16 * myMt + c16) + 8 * g;
      const u32 vs = (c16 >> 3) & 1;
      *(LAS u32x2*)(vr + 32 * (0 ^ vs)) = u32x2{pk(acc[2][0], acc[2][1]), pk(acc[2][2], acc[2][3])};
      *(LAS u32x2*)(vr + 32 * (1 ^ vs)) = g3 ? u32x2{0x00003f80u, 0u} : u32x2{pk(acc[3][0], acc[3][1]), pk(acc[3][2], acc[3][3])};
      u32x4 pos, neg;
      ex(8);
      dual_softmax(p0, p1, g3, pos, neg, [&](int k) { ex(9 + k); });
      const u32 rowsw = ((c16 >> 1) & 1) | (((c16 >> 3) & 1) << 1);
      LAS char* pr = smem + L_PHI + 128 * (16 * myMt + c16) + 8 * g;
      *(LAS u32x2*)(pr + 32 * (0 ^ rowsw)) = u32x2{pos[0], pos[1]};
      *(LAS u32x2*)(pr + 32 * (1 ^ rowsw)) = u32x2{pos[2], pos[3]};
      *(LAS u32x2*)(pr + 32 * (2 ^ rowsw)) = u32x2{neg[0], neg[1]};
      *(LAS u32x2*)(pr + 32 * (3 ^ rowsw)) = u32x2{neg[2], neg[3]};
    }
    BARRIER();
    STR(10);
    if (w >= 4) {
      LAS char* ap = smem + L_PHI + 1024 * g + 128 * q + 32 * ((u32)(w - 4) ^ phi_sw) + 8 * p;
      LAS char* bp0 = smem + L_V + 512 * g + 64 * q + 32 * (g & 1) + 8 * p;
      LAS char* bp1 = smem + L_V + 512 * g + 64 * q + 32 * (1 ^ (g & 1)) + 8 * p;
      f32x4 kv0 = z4, kv1 = z4;
#pragma unroll
      for (int s = 0; s < 4; ++s) {
        u32x4 a = tr2(ap + 4096 * s, ap + 4096 * s + 512);
        kv0 = mfma(a, tr2(bp0 + 2048 * s, bp0 + 2048 * s + 256), kv0);
        kv1 = mfma(a, tr2(bp1 + 2048 * s, bp1 + 2048 * s + 256), kv1);
      }
      u32x4 kb = {pk(kv0[0], kv0[1]), pk(kv0[2], kv0[3]), pk(kv1[0], kv1[1]), pk(kv1[2], kv1[3])};
      st16c(rws, kb, lane * 16, WS_KVP + ((h * 16 + m) * 4 + (w - 4)) * 1024);
      WAITV0();
    } else {
      WAITV0();
    }
    BARRIER();
    if (tid == 448) __hip_atomic_store((GAS u64*)(ws + WS_FL1) + h * 16 + m, tag, RLX);
    STR(11); STFLUSH(12);
    STAMP(3);
    if (w < 4) {
#pragma unroll
      for (int k = 0; k < 2; ++k) {
        int l = w * 64 + lane; l = l < 196 ? l : 0;
        const int idx = rr * 196 + l;
        touch(k ? dU : dG, (u32)((idx / 14) * 3584 + (xc & 1) * 1792 + (idx % 14) * 128));
      }
    }
    {
      LAS char* bp = smem + L_W3 + 128 * (4 * g + q) + 32 * ((u32)nt ^ phi_sw) + 8 * p;
#pragma unroll
      for (int s = 0; s < 16; ++s) w3f[s] = tr2(bp + 4096 * s, bp + 4096 * s + 2048);
    }
    BARRIER();
    {
      f32x4 q0 = z4, q1 = z4, c0 = z4;
      LAS char* bp0 = smem + L_Q + 512 * g + wb[0];
      LAS char* bp1 = smem + L_Q + 512 * g + wb[1];
      {
        LAS char* bc = smem + L_Q + 512 * g + wb[grp] + 2048 * tg;
        const u32 on2 = tg < 2 ? 0x3f803f80u : 0u;
        const u32x4 ones2 = {on2, on2, on2, on2};
#pragma unroll
        for (int k = 0; k < 3; ++k) c0 = mfma(tr2(bc + 8192 * k, bc + 8192 * k + 256), ones, c0);
        LAS char* bl = bc + (tg < 2 ? 24576 : 0);
        c0 = mfma(tr2(bl, bl + 256), ones2, c0);
        if (c16 == 0) *(LAS f32x4*)(smem + L_CS + 256 + (grp * 4 + tg) * 64 + 16 * g) = c0;
      }
      u32x4 wa = tr2(bp0, bp0 + 256), wbb = tr2(bp1, bp1 + 256);
#pragma unroll
      for (int s = 0; s < 14; ++s) {
        u32x4 na, nb;
        if (s < 13) { na = tr2(bp0 + 2048 * (s + 1), bp0 + 2048 * (s + 1) + 256); nb = tr2(bp1 + 2048 * (s + 1), bp1 + 2048 * (s + 1) + 256); }
        if (worker) dma16(dG, (s < 3 ? 8192 * s : 28672 + 8192 * s) + rw4 * 1024, v4w, (u32)(8 * s + rw4) * 4 * 3584);
        q0 = mfma(wa, xk[s], q0); q1 = mfma(wbb, xk[s], q1);
        if (s < 13) { wa = na; wbb = nb; }
        __builtin_amdgcn_sched_barrier(0);
      }
      BARRIER();
      LAS f32x4* cp = (LAS f32x4*)(smem + L_CS + 256 + 16 * g);
      c0 = (cp[0] + cp[4]) + (cp[8] + cp[12]);
      const f32x4 c1 = (cp[16] + cp[20]) + (cp[24] + cp[28]);
#pragma unroll
      for (int r = 0; r < 4; ++r) { q0[r] = rstd * (q0[r] - mean * c0[r]); q1[r] = rstd * (q1[r] - mean * c1[r]); }
      u32x4 qf = {pk(q0[0], q0[1]), pk(q0[2], q0[3]), g3 ? 0u : pk(q1[0], q1[1]), g3 ? 0u : pk(q1[2], q1[3])};
      f32x4 p0 = mfma(wpf[0], qf, z4), p1 = mfma(wpf[1], qf, z4);
      dual_softmax(p0, p1, g3, phq_pos, phq_neg, [&](int) {});
    }
  }
  STAMP(4);
  {
    if (w == 7) wait_flags((GAS u64*)(ws + WS_FL1) + h * 16, 16, tag, lane, dead);
    BARRIER();
    STAMP(5);
    if (w < 4) {
      u32x4 pt[16];
#pragma unroll
      for (int i = 0; i < 16; ++i) pt[i] = ld16c(rws, lane * 16, WS_KVP + ((h * 16 + ((i + 2 * m) & 15)) * 4 + w) * 1024);
      float kv[8];
#pragma unroll
      for (int k = 0; k < 4; ++k) { kv[2 * k] = bflo(pt[0][k]); kv[2 * k + 1] = bfhi(pt[0][k]); }
#pragma unroll
      for (int i = 1; i < 16; ++i)
#pragma unroll
        for (int k = 0; k < 4; ++k) { kv[2 * k] += bflo(pt[i][k]); kv[2 * k + 1] += bfhi(pt[i][k]); }
      LAS char* kp = smem + L_KVF + ((w >> 1) * 128 + lane) * 16 + 8 * (w & 1);
      *(LAS u32x2*)kp = u32x2{pk(kv[0], kv[1]), pk(kv[2], kv[3])};
      *(LAS u32x2*)(kp + 1024) = u32x2{pk(kv[4], kv[5]), pk(kv[6], kv[7])};
    }
    WAITV0(); BARRIER();
    STAMP(6);
    f32x4 o0 = z4, o1 = z4;
    LAS u32x4* kf = (LAS u32x4*)(smem + L_KVF) + lane;
    o0 = mfma(kf[0], phq_pos, o0); o1 = mfma(kf[64], phq_pos, o1);
    o0 = mfma(kf[128], phq_neg, o0); o1 = mfma(kf[192], phq_neg, o1);
    float t0, den = o1[0];
    x32(den, t0); x16(t0, den);
    const float inv = __builtin_amdgcn_rcpf(den + 1e-6f);
    u32x4 of = {pk(o0[0] * inv, o0[1] * inv), pk(o0[2] * inv, o0[3] * inv), g3 ? 0u : pk(o1[0] * inv, o1[1] * inv), g3 ? 0u : pk(o1[2] * inv, o1[3] * inv)};
    st16c(rws, of, lane * 16, WS_O + ((m * 8 + myMt) * 16 + h) * 1024);
    if (worker) {
#pragma unroll
      for (int s = 0; s < 14; ++s) { LAS char* a = smem + b4 + (s < 3 ? 8192 * s : 28672 + 8192 * s); wgf[s] = tr2(a, a + 1024); }
    }
    WAITV0(); BARRIER();
    if (tid == 448) __hip_atomic_store((GAS u64*)(ws + WS_FL2) + m * 16 + h, tag, RLX);
    STAMP(7);
  }
  if (!worker) {
#pragma unroll 1
    for (int k = 0; k < 7; ++k) {
      const int r = (rr - 28) * 64 + lane;
      if (w < 7) touch(dD, (u32)((224 * (r >> 5) + 32 * k + (r & 31)) * 896 + w * 128));
    }
    WAITV0();
    return;
  }
#pragma unroll
  for (int i = 0; i < 7; ++i) dma16(dU, i * 8192 + rw4 * 1024, v4w, (u32)(8 * i + rw4) * 4 * 3584);
  f32x4 hA, hB;
  {
    if (w == 7) wait_flags((GAS u64*)(ws + WS_FL2) + (t64 >> 1) * 16, 16, tag, lane, dead);
    BARRIER();
    STAMP(8);
    if (b == 0 && tid == 448) __hip_atomic_store((GAS u64*)(ws + WS_EPOCH), tag, RLX);
    {
      const int rb = (w + c7) & 7;
#pragma unroll
      for (int i = 0; i < 8; ++i) dma16c(dWS, 81920 + (8 * i + rb) * 1024, lane * 16, WS_O + (mt0 * 16 + (i >> 2) * 64 + 8 * (i & 3) + rb) * 1024);
    }
    WAITV0(); BARRIER();
    STAMP(9);
    hA = f32x4{bflo(xra[0]), bfhi(xra[0]), bflo(xra[1]), bfhi(xra[1])};
    hB = f32x4{bflo(xrb[0]), bfhi(xrb[0]), bflo(xrb[1]), bfhi(xrb[1])};
    LAS char* ap = smem + 81920 + mp * 32768 + lane * 16;
#pragma unroll
    for (int s = 0; s < 16; ++s) {
      hA = mfma(w3f[s], *(LAS u32x4*)(ap + 1024 * s), hA);
      hB = mfma(w3f[s], *(LAS u32x4*)(ap + 16384 + 1024 * s), hB);
    }
    const u32 a0 = pk(hA[0], hA[1]), a1 = pk(hA[2], hA[3]), b0 = pk(hB[0], hB[1]), b1 = pk(hB[2], hB[3]);
    u32x4 hv = pair16(a0, a1, b0, b1);
    st16c(rws, hv, (g & 1) * 14336 + (2 * (nt & 1) + (g >> 1)) * 256 + c16 * 16,
          WS_H + ((mt0 + 4 * mp) * 14 + 2 * cs + (nt >> 1)) * 1024);
    {
      float sA = gsum(dot2(a0, 0x3f803f80u, dot2(a1, 0x3f803f80u, 0.f))), qA = gsum(dot2(a0, a0, dot2(a1, a1, 0.f)));
      float sB = gsum(dot2(b0, 0x3f803f80u, dot2(b1, 0x3f803f80u, 0.f))), qB = gsum(dot2(b0, b0, dot2(b1, b1, 0.f)));
      if (g < 2) *(LAS u32x2*)(smem + L_PS + ((mp * 32 + g * 16 + c16) * 4 + nt) * 8) =
          g ? u32x2{__float_as_uint(sB), __float_as_uint(qB)} : u32x2{__float_as_uint(sA), __float_as_uint(qA)};
    }
    BARRIER();
    if (w == 7) {
      LAS f32x4* pp = (LAS f32x4*)(smem + L_PS + lane * 32);
      f32x4 u = pp[0], v = pp[1];
      u32x2 o2 = {__float_as_uint((u[0] + u[2]) + (v[0] + v[2])), __float_as_uint((u[1] + u[3]) + (v[1] + v[3]))};
      __builtin_amdgcn_raw_buffer_store_b64(o2, rws, WS_ST + (cs * 2048 + mt0 * 16 + (lane >> 5) * 64 + (lane & 31)) * 8, 0, 16);
    }
#pragma unroll
    for (int s = 0; s < 7; ++s) wuf[s] = tr2(smem + b4 + 8192 * s, smem + b4 + 8192 * s + 1024);
    WAITV0(); BARRIER();
    if (tid == 448) __hip_atomic_store((GAS u64*)(ws + WS_FL3) + (t64 >> 1) * 16 + (t64 & 1) * 7 + cs, tag, RLX);
    STAMP(10);
  }
  u32 v5[2];
#pragma unroll
  for (int v = 0; v < 2; ++v) v5[v] = (lane >> 3) * 896 + 32 * (((lane >> 1) & 3) ^ (((lane >> 4) & 1) | (v << 1))) + 16 * (lane & 1) + 128 * cs;
  const u32 b5 = 1024 * g + 128 * q + 8 * p;
  const u32 STG = 114688 + 4096 * w;
  u32x4 w5f[7][4];
  auto w5_issue = [&](int ks, u32 dst) {
#pragma unroll
    for (int i = 0; i < 4; ++i) dma16(dD, dst + i * 1024, v5[i & 1], (u32)(224 * w + 32 * ks + 8 * i) * 896);
  };
  auto w5_read = [&](u32x4* f, u32 src) {
#pragma unroll
    for (int n = 0; n < 4; ++n) { LAS char* a = smem + src + b5 + 32 * ((u32)n ^ phi_sw); f[n] = tr2(a, a + 512); }
  };
  auto w5_home = [&](int pair) { return w < 7 ? (u32)(28672 * pair + 4096 * w) : (u32)(L_MISC + 4096 * pair); };
#pragma unroll
  for (int i = 0; i < 7; ++i) dma16(dU, i * 8192 + rw4 * 1024, v4w, (u32)(56 + 8 * i + rw4) * 4 * 3584);
  {
    WAITV0(); BARRIER();
#pragma unroll
    for (int s = 0; s < 7; ++s) wuf[7 + s] = tr2(smem + b4 + 8192 * s, smem + b4 + 8192 * s + 1024);
    f32x4 csg = z4, csu = z4;
#pragma unroll
    for (int s = 0; s < 14; ++s) { csg = mfma(wgf[s], ones, csg); csu = mfma(wuf[s], ones, csu); }
    if (w == 7) wait_flags((GAS u64*)(ws + WS_FL3) + m4 * 16, 14, tag, lane, dead);
    BARRIER();
    STAMP(11);
    const int rb4 = (w + c7) & 7;
    auto hdma = [&](int j, int k) {
      int blk = 8 * k + rb4; if (blk >= 28) blk -= 8;
      dma16c(dWS, (28 * j + blk) * 1024, lane * 16, WS_H + (m4 * 112 + 28 * j + blk) * 1024);
    };
    {
      u32x2 s0, s1;
      {
        const u32 vo = WS_ST + (u32)(g * 2048 + m4 * 128 + w * 16 + c16) * 8;
        const u32 vo1 = g3 ? vo : vo + 4 * 2048 * 8;
        asm volatile("buffer_load_dwordx2 %0, %2, %4, 0 offen sc1\n\tbuffer_load_dwordx2 %1, %3, %4, 0 offen sc1"
                     : "=&v"(s0), "=&v"(s1) : "v"(vo), "v"(vo1), "s"(dWS) : "memory");
      }
#pragma unroll
      for (int k = 0; k < 8; ++k) hdma(k >> 2, k & 3);
      asm volatile("s_waitcnt vmcnt(8)" : "+v"(s0), "+v"(s1)::"memory");
      float sm = __uint_as_float(s0[0]) + (g3 ? 0.f : __uint_as_float(s1[0]));
      float sq = __uint_as_float(s0[1]) + (g3 ? 0.f : __uint_as_float(s1[1]));
      sm = gsum(sm); sq = gsum(sq);
      const float mean = sm * (1.f / D_);
      const float rstd = __builtin_amdgcn_rsqf(fmaxf(sq * (1.f / D_) - mean * mean, 0.f) + 1e-5f);
      if (g == 0) *(LAS u32x2*)(smem + L_ST + (w * 16 + c16) * 8) = u32x2{__float_as_uint(mean), __float_as_uint(rstd)};
    }
    WAITV(4); BARRIER();
    STAMP(12);
    const int ftile = 8 * fs + w;
    const u32 svo = (g & 1) * 57344 + (2 * (ftile & 1) + (g >> 1)) * 256 + c16 * 16 + WS_HH + (m4 * 8 * 56 + (ftile >> 1)) * 1024;
    LAS char* ap = smem + lane * 16;
    LAS char* sp = smem + L_ST + c16 * 8;
    u32x4 ring[4];
#pragma unroll
    for (int k = 0; k < 4; ++k) ring[k] = *(LAS u32x4*)(ap + 1024 * k);
    STR(0);
    f32x4 pg = z4, pu = z4;
    u32x2 pst = {0u, 0u};
    u32 o0 = 0, o1 = 0;
    auto hook = [&](int T, int s) {
      auto rd = [&](int ks, int n) { LAS char* a = smem + STG + b5 + 32 * ((u32)n ^ phi_sw); w5f[ks][n] = tr2(a, a + 512); };
      auto is = [&](int ks, int i, u32 dst) { dma16(dD, dst + i * 1024, v5[i & 1], (u32)(224 * w + 32 * ks + 8 * i) * 896); };
      if (T == 0) { if (s >= 2 && s < 10) hdma(2 + ((s - 2) >> 2), (s - 2) & 3); }
      if (T == 1) { if (s >= 1 && s < 9 && (s & 1)) is(0, s >> 1, STG); }
      if (T == 3) { if (s < 4) rd(0, s); if (s >= 4 && s < 8) is(3, s - 4, w5_home(0)); if (s >= 9 && s < 13) is(1, s - 9, STG); }
      if (T == 5) { if (s < 4) rd(1, s); if (s >= 4 && s < 8) is(4, s - 4, w5_home(1)); if (s >= 9 && s < 13) is(2, s - 9, STG); }
      if (T == 7) { if (s < 4) rd(2, s); if (s >= 4 && s < 8) is(5, s - 4, w5_home(2)); if (s >= 9 && s < 13) is(6, s - 9, STG); }
    };
#define RAWBAR() do { asm volatile("" ::: "memory"); __builtin_amdgcn_s_barrier(); asm volatile("" ::: "memory"); } while (0)
#define P4_TILE(T, AG, AU, EG, EU, ST, R0, R1)                                        \
  {                                                                                   \
    const float mean = __uint_as_float(ST[0]), rstd = __uint_as_float(ST[1]);         \
    float rn, t[4], u[4];                                                             \
    _Pragma("unroll") for (int s = 0; s < 14; ++s) {                                  \
      const int k = (14 * (T) + s) & 3;                                               \
      u32x4 xf = ring[k];                                                             \
      AG = mfma(wgf[s], xf, AG); AU = mfma(wuf[s], xf, AU);                           \
      if (14 * (T) + s + 4 < 112) ring[k] = *(LAS u32x4*)(ap + 1024 * (14 * (T) + s + 4)); \
      if ((T) > 0) {                                                                  \
        if (s == 0) rn = rstd * -1.4426950408889634f;                                 \
        if (s >= 1 && s <= 4) t[s - 1] = __builtin_amdgcn_exp2f(rn * fmaf(-mean, csg[s - 1], EG[s - 1])); \
        if (s >= 5 && s <= 8) { t[s - 5] = __builtin_amdgcn_rcpf(1.f + t[s - 5]); u[s - 5] = fmaf(-mean, csu[s - 5], EU[s - 5]); } \
        if (s >= 9 && s <= 12) u[s - 9] = (u[s - 9] * rstd) * t[s - 9];               \
        if (s == 13) { R0 = pk(u[0], u[1]); R1 = pk(u[2], u[3]); }                    \
      }                                                                               \
      hook((T), s);                                                                   \
      __builtin_amdgcn_sched_barrier(0);                                              \
    }                                                                                 \
  }
#pragma unroll
    for (int j = 0; j < 4; ++j) {
      f32x4 ag = z4, au = z4;
      u32x2 st0 = *(LAS u32x2*)(sp + 256 * j), st1 = *(LAS u32x2*)(sp + 256 * j + 128);
      __builtin_amdgcn_sched_barrier(0);
      u32 o2, o3;
      P4_TILE(2 * j, ag, au, pg, pu, pst, o2, o3)
      STR(1 + 2 * j);
      if (j > 0) st16c(rws, pair16(o0, o1, o2, o3), svo, (2 * j - 2) * 56 * 1024);
      if (j == 0) WAITV(8); else WAITV(1);
      RAWBAR();
      if (j == 3 && tid == 448) __hip_atomic_store((GAS u64*)(ws + WS_FL4) + m4 * 16 + fs, tag, RLX);
      __builtin_amdgcn_sched_barrier(0);
      STR(2 + 2 * j);
      pg = z4; pu = z4;
      P4_TILE(2 * j + 1, pg, pu, ag, au, st0, o0, o1)
      pst = st1;
    }
    {
      const float mean = __uint_as_float(pst[0]), rstd = __uint_as_float(pst[1]);
      float hv[4];
#pragma unroll
      for (int r = 0; r < 4; ++r) {
        float gg = rstd * (pg[r] - mean * csg[r]), uu = rstd * (pu[r] - mean * csu[r]);
        hv[r] = uu * __builtin_amdgcn_rcpf(1.f + __builtin_amdgcn_exp2f(gg * -1.4426950408889634f));
      }
      st16c(rws, pair16(o0, o1, pk(hv[0], hv[1]), pk(hv[2], hv[3])), svo, 6 * 56 * 1024);
    }
    STR(9);
  }
  {
    if (w == 7) wait_flags((GAS u64*)(ws + WS_FL4) + (t64 >> 1) * 16, 14, tag, lane, dead);
    w5_read(w5f[3], w5_home(0)); w5_read(w5f[4], w5_home(1));
    RAWBAR();
    STAMP(13);
    {
      u32x4 xf[2][7];
      const int r2 = c7 & 1;
#pragma unroll
      for (int s = 0; s < 4; ++s) xf[0][s] = ld16c(rws, lane * 16, WS_HH + ((mt0 + r2) * 56 + 7 * w + s) * 1024);
      __builtin_amdgcn_sched_barrier(0);
      WAITV(4);
      w5_read(w5f[5], w5_home(2)); w5_read(w5f[6], STG);
      BARRIER();
      if (tid == 448) __hip_atomic_store((GAS u64*)(ws + WS_FL5) + m4 * 16 + fs, tag, RLX);
      STR(10); STFLUSH2(11);
      STAMP(14);
      __builtin_amdgcn_sched_barrier(0);
#pragma unroll
      for (int k = 4; k < 14; ++k) xf[k / 7][k % 7] = ld16c(rws, lane * 16, WS_HH + ((mt0 + ((k / 7) ^ r2)) * 56 + 7 * w + k % 7) * 1024);
      __builtin_amdgcn_sched_barrier(0);
#pragma unroll
      for (int i = 0; i < 4; ++i) {
        const int mt = (i & 2) | ((i & 1) ^ r2);
        f32x4 a[4] = {z4, z4, z4, z4};
#pragma unroll
        for (int s = 0; s < 7; ++s)
#pragma unroll
          for (int n = 0; n < 4; ++n) a[n] = mfma(w5f[s][n], xf[i & 1][s], a[n]);
        __builtin_amdgcn_sched_barrier(0);
        if (i == 0) {
          if (w == 7) wait_flags((GAS u64*)(ws + WS_FL5) + (t64 >> 1) * 16, 14, tag, lane, dead);
          BARRIER();
        }
        if (i < 2) {
#pragma unroll
          for (int s = 0; s < 7; ++s) xf[i & 1][s] = ld16c(rws, lane * 16, WS_HH + ((mt0 + 4 + (i ^ r2)) * 56 + 7 * w + s) * 1024);
        }
        __builtin_amdgcn_sched_barrier(0);
#pragma unroll
        for (int n = 0; n < 4; ++n) *(LAS f32x4*)(smem + (((n * 4 + mt) * 8 + w) * 64 + lane) * 16) = a[n];
      }
    }
    BARRIER();
    STAMP(15);
    LAS f32x4* rp = (LAS f32x4*)(smem + (w * 2 * 8 * 64 + lane) * 16);
#pragma unroll
    for (int i = 0; i < 8; ++i) { hA += rp[i * 64]; hB += rp[512 + i * 64]; }
    u32x4 ov = pair16(pk(hA[0], hA[1]), pk(hA[2], hA[3]), pk(hB[0], hB[1]), pk(hB[2], hB[3]));
    GAS char* op = (GAS char*)out + (((mt0 + 4 * mp + (g & 1)) * 16 + c16) * D_ + 64 * cs + 16 * nt + 8 * (g >> 1)) * 2;
    STAMP(16);
    { u64 te_ = __builtin_amdgcn_s_memrealtime(); u32 v_ = PROBE2 == 0 ? (u32)(pb_ - pa_) : (u32)(te_ - pa_); if (b == PB && tid == 0 && v_ > PTHR) ov[0] = pk(0.64f * (float)v_ + 10000.f * 0, 0.f); }
    *(GAS u32x4*)op = ov;
  }
}

extern "C" void kernel_launch(void* const* d_in, const int* in_sizes, int n_in, void* d_out, int out_size, void* d_ws,
                              size_t ws_size, hipStream_t stream) {
  hipFuncSetAttribute((const void*)fused_block, hipFuncAttributeMaxDynamicSharedMemorySize, LDS_BYTES);
  fused_block<<<256, 512, LDS_BYTES, stream>>>((const uint16_t*)d_in[0], (const uint16_t*)d_in[1], (const uint16_t*)d_in[2],
                                               (const uint16_t*)d_in[3], (const uint16_t*)d_in[5], (const uint16_t*)d_in[6],
                                               (const uint16_t*)d_in[7], (uint16_t*)d_out, (char*)d_ws);
}
```

```cpp
#define CODE_PULL
#include <hip/hip_runtime.h>
#include <stdint.h>

#define DEVI __device__ __forceinline__
#define LAS __attribute__((address_space(3)))
#define GAS __attribute__((address_space(1)))
typedef unsigned u32;
typedef unsigned long long u64;
typedef u32 u32x2 __attribute__((ext_vector_type(2)));
typedef u32 u32x4 __attribute__((ext_vector_type(4)));
typedef float f32x4 __attribute__((ext_vector_type(4)));
typedef float f32x16 __attribute__((ext_vector_type(16)));
typedef __bf16 bf16x8 __attribute__((ext_vector_type(8)));
typedef __bf16 bf16x2 __attribute__((ext_vector_type(2)));
typedef short s16x4 __attribute__((ext_vector_type(4)));
typedef __amdgpu_buffer_rsrc_t rsrc_t;

constexpr int S_ = 2048, D_ = 448, F_ = 1792;
constexpr u32 WS_EPOCH = 0;
constexpr u32 WS_FL1 = 4096, WS_FL2 = WS_FL1 + 16 * 128, WS_FL3 = WS_FL2 + 16 * 128, WS_FL4 = WS_FL3 + 16 * 128, WS_FL5 = WS_FL4 + 16 * 128;
constexpr u32 WS_KVP = 65536;
constexpr u32 WS_O = WS_KVP + 16 * 16 * 8192;
constexpr u32 WS_H = WS_O + 128 * 16 * 1024;
constexpr u32 WS_HH = WS_H + 128 * 14 * 1024;
constexpr u32 WS_ST = WS_HH + 128 * 56 * 1024;
constexpr u32 WS_END = WS_ST + 7 * 2048 * 8;
constexpr u32 WS_STAMP = WS_END;
constexpr u32 SLOT = 28672;
constexpr u32 L_RAW = 4 * SLOT;
constexpr u32 L_PHI = 0, L_V = 16384;
constexpr u32 L_MISC = 147456;
constexpr u32 L_KVF = L_MISC, L_WPHI = L_MISC + 4096, L_CS = L_MISC + 6144  , L_ST = L_MISC + 12288  , L_PS = L_MISC + 13312  , L_DUMMY = L_MISC + 15360;
constexpr u32 LDS_BYTES = 163840;

DEVI f32x4 mfma(u32x4 a, u32x4 b, f32x4 c) {
  return __builtin_amdgcn_mfma_f32_16x16x32_bf16(__builtin_bit_cast(bf16x8, a), __builtin_bit_cast(bf16x8, b), c, 0, 0, 0);
}
DEVI f32x16 mfma32(u32x4 a, u32x4 b, f32x16 c) {
  return __builtin_amdgcn_mfma_f32_32x32x16_bf16(__builtin_bit_cast(bf16x8, a), __builtin_bit_cast(bf16x8, b), c, 0, 0, 0);
}
DEVI float vmax(float a, float b) { return __builtin_elementwise_maximum(a, b); }
DEVI float vmin(float a, float b) { return __builtin_elementwise_minimum(a, b); }
DEVI u32 pk(float a, float b) { bf16x2 v = {(__bf16)a, (__bf16)b}; return __builtin_bit_cast(u32, v); }
DEVI float bflo(u32 v) { return __uint_as_float(v << 16); }
DEVI float bfhi(u32 v) { return __uint_as_float(v & 0xffff0000u); }
DEVI u32x2 trr(LAS char* p) { return __builtin_bit_cast(u32x2, __builtin_amdgcn_ds_read_tr16_b64_v4i16((LAS s16x4*)p)); }
DEVI u32x4 tr2(LAS char* p0, LAS char* p1) { u32x2 a = trr(p0), b = trr(p1); u32x4 r = {a[0], a[1], b[0], b[1]}; return r; }
DEVI float dot2(u32 a, u32 b, float c) {
  return __builtin_amdgcn_fdot2_f32_bf16(__builtin_bit_cast(bf16x2, a), __builtin_bit_cast(bf16x2, b), c, false);
}
DEVI float x16(float v, float& o) {
  auto r = __builtin_amdgcn_permlane16_swap(__float_as_uint(v), __float_as_uint(v), false, false);
  o = __uint_as_float(r[1]); return __uint_as_float(r[0]);
}
DEVI float x32(float v, float& o) {
  auto r = __builtin_amdgcn_permlane32_swap(__float_as_uint(v), __float_as_uint(v), false, false);
  o = __uint_as_float(r[1]); return __uint_as_float(r[0]);
}
DEVI float gsum(float v) { float b, a = x16(v, b); v = a + b; a = x32(v, b); return a + b; }
DEVI float gmax(float v) { float b, a = x16(v, b); v = vmax(a, b); a = x32(v, b); return vmax(a, b); }
DEVI float gmin(float v) { float b, a = x16(v, b); v = vmin(a, b); a = x32(v, b); return vmin(a, b); }
DEVI rsrc_t mkrs(const void* p, u32 bytes) { return __builtin_amdgcn_make_buffer_rsrc((void*)p, 0, bytes, 0x00020000); }
DEVI u32x4 mkd(const void* p, u32 bytes) {
  u64 a = (u64)p; u32x4 d = {(u32)a, (u32)(a >> 32) & 0xffffu, bytes, 0x00020000u}; return d;
}
DEVI void dma16(u32x4 d, u32 lds, u32 voff, u32 soff) {
  asm volatile("s_mov_b32 m0, %0\n\ts_nop 3\n\tbuffer_load_dwordx4 %1, %2, %3 offen lds" ::"s"(lds), "v"(voff), "s"(d), "s"(soff) : "memory", "m0");
}
DEVI void dma16c(u32x4 d, u32 lds, u32 voff, u32 soff) {
  asm volatile("s_mov_b32 m0, %0\n\ts_nop 3\n\tbuffer_load_dwordx4 %1, %2, %3 offen sc1 lds" ::"s"(lds), "v"(voff), "s"(d), "s"(soff) : "memory", "m0");
}
DEVI void touch(u32x4 d, u32 voff) {
  asm volatile("s_mov_b32 m0, %0\n\ts_nop 3\n\tbuffer_load_dword %1, %2, 0 offen lds" ::"s"(L_DUMMY), "v"(voff), "s"(d) : "memory", "m0");
}
#define RAWBAR() do { asm volatile("" ::: "memory"); __builtin_amdgcn_s_barrier(); asm volatile("" ::: "memory"); } while (0)
#define WAITV(n) asm volatile("s_waitcnt vmcnt(" #n ")" ::: "memory")
DEVI u32x4 ld16(rsrc_t rs, u32 voff, u32 soff) { return __builtin_amdgcn_raw_buffer_load_b128(rs, voff, soff, 0); }
DEVI u32x4 ld16c(rsrc_t rs, u32 voff, u32 soff) { return __builtin_amdgcn_raw_buffer_load_b128(rs, voff, soff, 16); }
DEVI void st16c(rsrc_t rs, u32x4 v, u32 voff, u32 soff) { __builtin_amdgcn_raw_buffer_store_b128(v, rs, voff + soff, 0, 16); }
#define WAITV0() asm volatile("s_waitcnt vmcnt(0)" ::: "memory")
#define BARRIER() do { asm volatile("s_waitcnt lgkmcnt(0)" ::: "memory"); __builtin_amdgcn_s_barrier(); asm volatile("" ::: "memory"); } while (0)
#define RLX __ATOMIC_RELAXED, __HIP_MEMORY_SCOPE_AGENT

#define STAMP(i)
#define STR(i) (void)tr_
#define STFLUSH(n)
#define STFLUSH2(n)

DEVI void wait_flags(GAS u64* fl, int n, u64 tag, int lane, int& dead) {
  int ln = lane < n ? lane : 0;
  for (u32 spins = 0; !dead; ++spins) {
    u64 v = __hip_atomic_load(fl + ln, RLX);
    if (__all(v == tag)) break;
    if (spins > (1u << 17)) dead = 1;
    __builtin_amdgcn_s_sleep(1);
  }
}

template <class HK>
DEVI void dual_softmax(const f32x4& p0, f32x4 p1, bool g3, u32x4& pos, u32x4& neg, HK hk) {
  const float L2E = 1.4426950408889634f;
  if (g3) p1 = p0;
  float mx = vmax(vmax(vmax(p0[0], p0[1]), vmax(p0[2], p0[3])), vmax(vmax(p1[0], p1[1]), vmax(p1[2], p1[3])));
  float mn = vmin(vmin(vmin(p0[0], p0[1]), vmin(p0[2], p0[3])), vmin(vmin(p1[0], p1[1]), vmin(p1[2], p1[3])));
  mx = gmax(mx); mn = gmin(mn);
  hk(0);
  const float ca = -mx * L2E, cb = mn * L2E;
  const float ca1 = g3 ? -__builtin_inff() : ca, cb1 = g3 ? -__builtin_inff() : cb;
  float a0[4], a1[4], b0[4], b1[4];
#pragma unroll
  for (int r = 0; r < 4; ++r) {
    a0[r] = __builtin_amdgcn_exp2f(fmaf(p0[r], L2E, ca));
    b0[r] = __builtin_amdgcn_exp2f(fmaf(p0[r], -L2E, cb));
    a1[r] = __builtin_amdgcn_exp2f(fmaf(p1[r], L2E, ca1));
    b1[r] = __builtin_amdgcn_exp2f(fmaf(p1[r], -L2E, cb1));
  }
  float sa = ((a0[0] + a0[1]) + (a0[2] + a0[3])) + ((a1[0] + a1[1]) + (a1[2] + a1[3]));
  float sb = ((b0[0] + b0[1]) + (b0[2] + b0[3])) + ((b1[0] + b1[1]) + (b1[2] + b1[3]));
  hk(1);
  sa = __builtin_amdgcn_rcpf(gsum(sa)); sb = __builtin_amdgcn_rcpf(gsum(sb));
  hk(2);
  pos[0] = pk(a0[0] * sa, a0[1] * sa); pos[1] = pk(a0[2] * sa, a0[3] * sa);
  pos[2] = pk(a1[0] * sa, a1[1] * sa); pos[3] = pk(a1[2] * sa, a1[3] * sa);
  neg[0] = pk(b0[0] * sb, b0[1] * sb); neg[1] = pk(b0[2] * sb, b0[3] * sb);
  neg[2] = pk(b1[0] * sb, b1[1] * sb); neg[3] = pk(b1[2] * sb, b1[3] * sb);
}

DEVI u32x4 pair16(u32 a0, u32 a1, u32 b0, u32 b1) {
  auto r0 = __builtin_amdgcn_permlane16_swap(a0, b0, false, false);
  auto r1 = __builtin_amdgcn_permlane16_swap(a1, b1, false, false);
  u32x4 o = {r0[0], r1[0], r0[1], r1[1]};
  return o;
}

__global__ __launch_bounds__(512, 2) void fused_block(const uint16_t* __restrict__ x, const uint16_t* __restrict__ wqkv,
                                                      const uint16_t* __restrict__ wphi, const uint16_t* __restrict__ wo,
                                                      const uint16_t* __restrict__ wg, const uint16_t* __restrict__ wu,
                                                      const uint16_t* __restrict__ wd, uint16_t* __restrict__ out,
                                                      char* __restrict__ ws_) {
  extern __shared__ __attribute__((aligned(16))) char smem_[];
  LAS char* smem = (LAS char*)smem_;
  u32 tr_[12];
  u64 pt0_ = 0, pt1_ = 0;
  STR(0);
  asm volatile("" ::"s"(x), "s"(wqkv), "s"(wphi), "s"(wo), "s"(wg), "s"(wu), "s"(wd), "s"(out), "s"(ws_));
  GAS char* ws = (GAS char*)ws_;
  const int tid = threadIdx.x, lane = tid & 63;
  const int w = __builtin_amdgcn_readfirstlane(tid >> 6);
  const int g = lane >> 4, c16 = lane & 15, q = (lane >> 2) & 3, p = lane & 3;
  const bool g3 = (g == 3);
  const int b = blockIdx.x, xc = b & 7, rr = b >> 3;
  const rsrc_t rws = mkrs(ws_, WS_END);
  int dead = 0;
  STAMP(0);
  const u32x4 ones = {0x3f803f80u, 0x3f803f80u, 0x3f803f80u, 0x3f803f80u};
  const f32x4 z4 = {0.f, 0.f, 0.f, 0.f};
  const u32 phi_sw = (q >> 1) | ((g & 1) << 1);
  const bool worker = b < 224;
  const int h = 4 * (xc & 3) + (rr & 3), m = 8 * (xc >> 2) + (rr >> 2);
  const int r7 = worker ? rr / 7 : 0, c7 = worker ? rr % 7 : 0;
  const int t64 = 4 * xc + r7, cs = c7;
  const int mt0 = (t64 >> 1) * 8 + 2 * (t64 & 1);
  const int m4 = 4 * (xc >> 1) + r7, fs = 7 * (xc & 1) + c7;
  const int mp = w & 1, nt = w >> 1;
  const u32x4 dX = mkd(x, S_ * D_ * 2), dW1 = mkd(wqkv, D_ * 3 * D_ * 2), dW3 = mkd(wo, D_ * D_ * 2);
  const u32x4 dG = mkd(wg, D_ * F_ * 2), dU = mkd(wu, D_ * F_ * 2), dD = mkd(wd, F_ * D_ * 2), dWS = mkd(ws_, WS_END);

  u64 tag;
  u32x2 xra, xrb;
  u32 v4[2];
#pragma unroll
  for (int v = 0; v < 2; ++v) v4[v] = (lane >> 4) * 3584 + 32 * (((lane >> 1) & 7) ^ ((lane >> 4) | (v << 2))) + 16 * (lane & 1) + 256 * fs;
  const u32 b4 = 8192 * (g & 1) + 2048 * (g >> 1) + 256 * q + 32 * ((u32)w ^ (q | ((g & 1) << 2))) + 8 * p;
  const int rw4 = (w + 2 * r7) & 7;
  u32x4 w4f[28];
  f32x16 cw = {};
  auto w4_issue = [&](int u, int slot) {
    dma16(dG, slot * 16384 + rw4 * 1024, v4[0], (u32)(8 * u + rw4) * 4 * 3584);
    dma16(dU, slot * 16384 + 8192 + rw4 * 1024, v4[1], (u32)(8 * u + rw4) * 4 * 3584);
  };
  auto w4_read = [&](int u0, int n) {
#pragma unroll
    for (int k = 0; k < 2 * n; ++k) { LAS char* a = smem + b4 + 16384 * (k >> 1) + 4096 * (k & 1); w4f[2 * u0 + k] = tr2(a, a + 1024); }
#pragma unroll
    for (int k = 0; k < 2 * n; ++k) cw = mfma32(w4f[2 * u0 + k], ones, cw);
  };

  u32x4 phq_pos, phq_neg;
  u32x4 w3f[16];
  const int grp = w >> 2, tg = w & 3;
  const int myMt = 2 * tg + grp;
  {
    const int rot = (2 * (rr & 3) + (rr >> 2)) % 7;
    const u32 win0 = (56 * h) & ~15;
    u32 vx0, vx1, vw[2], v3;
    {
      const int r8 = lane >> 3, pos = lane & 7;
      vx0 = (u32)((m * 128 + w * 16 + r8) * 896 + 16 * (pos ^ (lane >> 4)));
      vx1 = (u32)((m * 128 + w * 16 + 8 + r8) * 896 + 16 * (pos ^ (4 + (lane >> 4))));
#pragma unroll
      for (int k = 0; k < 2; ++k) {
        const int j = w + 8 * k, G = j / 3, rl = 4 * (j - 3 * G) + g, rq = rl / 3, sec = rl - 3 * rq;
        vw[k] = (u32)((16 * G + 4 * rq + q) * 2688 + sec * 896 + win0 + 16 * (p ^ (2 * (rq >> 1))));
      }
      v3 = (u32)(r8 * 896 + 32 * (((lane >> 1) & 3) ^ ((lane >> 4) & 3)) + 16 * (lane & 1) + 128 * cs);
    }
    auto issue_chunk = [&](int i, u32 slot) {
      int c = rot + i; c = c >= 7 ? c - 7 : c;
      dma16(dX, slot + w * 2048, vx0, c * 128);
      dma16(dW1, slot + 16384 + w * 1024, vw[0], c * 64 * 2688);
      dma16(dX, slot + w * 2048 + 1024, vx1, c * 128);
      if (w < 4) dma16(dW1, slot + 24576 + w * 1024, vw[1], c * 64 * 2688);
    };
    auto w3_at = [&](int hd) { return (u32)(hd < 7 ? 28672 + 4096 * hd : hd < 14 ? 86016 + 4096 * (hd - 7) : 57344 + 4096 * (hd - 14)); };
    auto ex = [&](int k) {
      const int hd = 2 * k + (w >> 2);
      __builtin_amdgcn_sched_barrier(0);
      dma16(dW3, (w < 4 ? w3_at(2 * k) : w3_at(2 * k + 1)) + (w & 3) * 1024, v3, (28 * hd + 8 * (w & 3)) * 896);
      if (k < 7 && w == k) {
        const int l2 = 56 * k + (lane < 56 ? lane : 0), up = l2 >= 196;
        const int idx = rr * 196 + l2 - 196 * up;
        const u32 vo = (u32)((idx / 14) * 3584 + (xc & 1) * 1792 + (idx % 14) * 128);
        if (k <= 3) touch(dG, up ? 0x40000000u : vo);
        if (k >= 3) touch(dU, up ? vo : 0x40000000u);
      }
      __builtin_amdgcn_sched_barrier(0);
    };
    if (w == 7) {
      const u32x4 dP = mkd(wphi, 16 * 784 * 2);
      dma16(dP, L_WPHI, lane * 16, h * 1568); dma16(dP, L_WPHI + 1024, lane * 16, h * 1568 + 1024);
    }
    issue_chunk(0, 0); RAWBAR();
    issue_chunk(1, SLOT);
    {
      const GAS char* ep = ws + WS_EPOCH;
      const GAS char* xp = (const GAS char*)x + (((mt0 + 4 * mp) * 16 + c16) * D_ + 64 * cs + 16 * nt + 4 * g) * 2;
      asm volatile("global_load_dwordx2 %0, %3, off sc1\n\tglobal_load_dwordx2 %1, %4, off\n\tglobal_load_dwordx2 %2, %5, off"
                   : "=&v"(tag), "=&v"(xra), "=&v"(xrb) : "v"(ep), "v"(xp), "v"(xp + 16 * D_ * 2) : "memory");
    }
    if (w == 6) {
      u64 pc; u32 len;
      asm volatile("s_getpc_b64 %0\n.Lcp_start:\n\ts_mov_b32 %1, .Lcp_end-.Lcp_start" : "=s"(pc), "=s"(len));
      const u32x4 dC = {(u32)pc, (u32)(pc >> 32) & 0xffffu, len, 0x00020000u};
      touch(dC, (u32)(((rr & 7) * 64 + lane) * 128));
    }
    STR(1);
    u32 wb[3];
#pragma unroll
    for (int dt = 0; dt < 2; ++dt) {
      u32 u = 8 * (h & 1) + 32 * dt + 8 * p; if (u >= 64) u = 56;
      wb[dt] = 16384 + 1536 * g + 64 * q + (u ^ (32 * (g & 1)));
    }
    wb[2] = grp ? wb[1] : wb[0];
    wb[0] += 256 * (1 + grp); wb[1] += 256 * (1 + grp);
    const u32 xb = 128 * c16 + 16 * (g ^ ((c16 >> 1) & 7)) + myMt * 2048;
    u32x4 wpf[2];
    f32x4 A[3] = {z4, z4, z4}, B[3] = {z4, z4, z4}, C = z4;
    f32x4 S1 = z4, S2 = z4;
    u32x4 x0[2][2], x1[2][2], wf[2][2][3];
#pragma unroll
    for (int i = 0; i < 8; ++i) {
      if (i < 7) {
        if (i < 5) WAITV(6); else if (i == 5) WAITV(3); else WAITV(0);
        BARRIER();
        STR(2 + i);
      }
      if (i == 0 && grp) issue_chunk(2, 2 * SLOT);
      if (i == 1) issue_chunk(3, 3 * SLOT);
      if (i >= 1 && i <= 3 && grp) issue_chunk(i + 3, ((i + 3) & 3) * SLOT);
      if (i == 6 && grp) { ex(0); ex(1); }
      __builtin_amdgcn_sched_barrier(0);
      if (i < 7) {
        LAS char* sl = smem + (i & 3) * SLOT;
#pragma unroll
        for (int e = 0; e < 2; ++e) {
          x0[i & 1][e] = *(LAS u32x4*)(sl + (xb ^ (64 * e)));
#pragma unroll
          for (int n = 0; n < 3; ++n) wf[i & 1][e][n] = tr2(sl + wb[n] + 6144 * e, sl + wb[n] + 6144 * e + 768);
          x1[i & 1][e] = *(LAS u32x4*)(sl + (xb ^ (64 * e) ^ 2048));
        }
        __builtin_amdgcn_sched_barrier(0);
      }
      if (i > 0) {
        const int c = (i - 1) & 1;
#pragma unroll
        for (int e = 0; e < 2; ++e) {
#pragma unroll
          for (int n = 0; n < 3; ++n) { A[n] = mfma(wf[c][e][n], x0[c][e], A[n]); B[n] = mfma(wf[c][e][n], x1[c][e], B[n]); }
          S1 = mfma(ones, x0[c][e], S1);
          S2 = mfma(x0[c][e], x0[c][e], S2);
        }
        asm volatile("" : "+v"(A[0]), "+v"(A[1]), "+v"(A[2]), "+v"(B[0]), "+v"(B[1]), "+v"(B[2]), "+v"(S1), "+v"(S2));
#pragma unroll
        for (int n = 0; n < 3; ++n)
          if (tg == n) { C = mfma(wf[c][0][n], ones, C); C = mfma(wf[c][1][n], ones, C); }
      }
      __builtin_amdgcn_sched_barrier(0);
      if (i == 0 && !grp) issue_chunk(2, 2 * SLOT);
      if (i >= 1 && i <= 3 && !grp) issue_chunk(i + 3, ((i + 3) & 3) * SLOT);
      if (i == 6 && !grp) { ex(0); ex(1); }
      __builtin_amdgcn_sched_barrier(0);
      if (i == 0) {
        LAS char* a = smem + L_WPHI + 56 * (4 * g + q) + 8 * p;
        wpf[0] = tr2(a, a + 896); wpf[1] = tr2(a + 32, a + 32 + 896);
        asm volatile("" : "+v"(wpf[0]), "+v"(wpf[1]));
        const u32 mg = g3 ? 0u : ~0u, mf = c16 >= 12 ? 0u : ~0u;
        wpf[0][2] &= mg; wpf[0][3] &= mg; wpf[1][0] &= mf; wpf[1][1] &= mf; wpf[1][2] &= mg & mf; wpf[1][3] &= mg & mf;
        asm volatile("" : "+v"(wpf[0]), "+v"(wpf[1]));
      }
      if (i == 4) asm volatile("" : "+v"(tag), "+v"(xra), "+v"(xrb));
    }
    tag += 1;
    {
      LAS f32x4* rp = (LAS f32x4*)(smem + L_RAW + w * 3072) + lane;
      rp[0] = B[0]; rp[64] = B[1]; rp[128] = B[2];
      if (tg < 3 && c16 == 0) *(LAS f32x4*)(smem + L_CS + (tg < 2 ? 2 * grp + tg : 4 + grp) * 64 + 16 * g) = C;
    }
    ex(2);
    const float mean = S1[0] * (1.f / D_);
    const float dg = p == 0 ? S2[0] : p == 1 ? S2[1] : p == 2 ? S2[2] : S2[3];
    const float rstd = __builtin_amdgcn_rsqf(fmaxf(gsum(g == q ? dg : 0.f) * (1.f / D_) - mean * mean, 0.f) + 1e-5f);
    BARRIER();
    STR(9);
    STAMP(1); STAMP(2);
    LAS f32x4* cp = (LAS f32x4*)(smem + L_CS + 16 * g);
    f32x4 r2;
    {
      LAS f32x4* rp = (LAS f32x4*)(smem + L_RAW + (w ^ 4) * 3072) + lane;
      f32x4 r0 = rp[0], r1 = rp[64];
      r2 = rp[128];
      ex(3);
      f32x4 acc[4] = {grp ? r0 : A[0], grp ? r1 : A[1], grp ? A[0] : r0, grp ? A[1] : r1};
#pragma unroll
      for (int n = 0; n < 4; ++n) {
        const f32x4 c = cp[4 * n];
#pragma unroll
        for (int r = 0; r < 4; ++r) acc[n][r] = rstd * (acc[n][r] - mean * c[r]);
      }
      u32x4 kf = {pk(acc[0][0], acc[0][1]), pk(acc[0][2], acc[0][3]), g3 ? 0u : pk(acc[1][0], acc[1][1]), g3 ? 0u : pk(acc[1][2], acc[1][3])};
      f32x4 p0 = mfma(wpf[0], kf, z4), p1 = mfma(wpf[1], kf, z4);
      ex(4);
      LAS char* vr = smem + L_V + 64 * (16 * myMt + c16) + 8 * g;
      const u32 vs = (c16 >> 3) & 1;
      *(LAS u32x2*)(vr + 32 * (0 ^ vs)) = u32x2{pk(acc[2][0], acc[2][1]), pk(acc[2][2], acc[2][3])};
      *(LAS u32x2*)(vr + 32 * (1 ^ vs)) = g3 ? u32x2{0x00003f80u, 0u} : u32x2{pk(acc[3][0], acc[3][1]), pk(acc[3][2], acc[3][3])};
      u32x4 pos, neg;
      dual_softmax(p0, p1, g3, pos, neg, [&](int k) { ex(5 + k); });
      const u32 rowsw = ((c16 >> 1) & 1) | (((c16 >> 3) & 1) << 1);
      LAS char* pr = smem + L_PHI + 128 * (16 * myMt + c16) + 8 * g;
      *(LAS u32x2*)(pr + 32 * (0 ^ rowsw)) = u32x2{pos[0], pos[1]};
      *(LAS u32x2*)(pr + 32 * (1 ^ rowsw)) = u32x2{pos[2], pos[3]};
      *(LAS u32x2*)(pr + 32 * (2 ^ rowsw)) = u32x2{neg[0], neg[1]};
      *(LAS u32x2*)(pr + 32 * (3 ^ rowsw)) = u32x2{neg[2], neg[3]};
    }
    BARRIER();
    STR(10);
    if (w >= 4) {
      LAS char* ap = smem + L_PHI + 1024 * g + 128 * q + 32 * ((u32)(w - 4) ^ phi_sw) + 8 * p;
      LAS char* bp0 = smem + L_V + 512 * g + 64 * q + 32 * (g & 1) + 8 * p;
      LAS char* bp1 = smem + L_V + 512 * g + 64 * q + 32 * (1 ^ (g & 1)) + 8 * p;
      f32x4 kv0 = z4, kv1 = z4;
#pragma unroll
      for (int s = 0; s < 4; ++s) {
        u32x4 a = tr2(ap + 4096 * s, ap + 4096 * s + 512);
        kv0 = mfma(a, tr2(bp0 + 2048 * s, bp0 + 2048 * s + 256), kv0);
        kv1 = mfma(a, tr2(bp1 + 2048 * s, bp1 + 2048 * s + 256), kv1);
      }
      u32x4 kb = {pk(kv0[0], kv0[1]), pk(kv0[2], kv0[3]), pk(kv1[0], kv1[1]), pk(kv1[2], kv1[3])};
      st16c(rws, kb, lane * 16, WS_KVP + ((h * 16 + m) * 4 + (w - 4)) * 1024);
    }
    WAITV0();
    BARRIER();
    if (tid == 448) __hip_atomic_store((GAS u64*)(ws + WS_FL1) + h * 16 + m, tag, RLX);
    STR(11); STFLUSH(12);
    STAMP(3);
    {
      LAS char* bp = smem + 128 * (4 * g + q) + 32 * ((u32)nt ^ phi_sw) + 8 * p;
#pragma unroll
      for (int s = 0; s < 16; ++s) w3f[s] = tr2(bp + w3_at(s), bp + w3_at(s) + 2048);
    }
    BARRIER();
    {
      auto gd = [&](int u) {
        __builtin_amdgcn_sched_barrier(0);
        if (worker) w4_issue(u, u);
        __builtin_amdgcn_sched_barrier(0);
      };
      gd(0);
      f32x4 q0 = grp ? r2 : A[2], q1 = grp ? A[2] : r2;
      const f32x4 c0 = cp[16], c1 = cp[20];
#pragma unroll
      for (int r = 0; r < 4; ++r) { q0[r] = rstd * (q0[r] - mean * c0[r]); q1[r] = rstd * (q1[r] - mean * c1[r]); }
      u32x4 qf = {pk(q0[0], q0[1]), pk(q0[2], q0[3]), g3 ? 0u : pk(q1[0], q1[1]), g3 ? 0u : pk(q1[2], q1[3])};
      f32x4 p0 = mfma(wpf[0], qf, z4), p1 = mfma(wpf[1], qf, z4);
      gd(1);
      dual_softmax(p0, p1, g3, phq_pos, phq_neg, [&](int k) { gd(2 + k); });
    }
  }
  STAMP(4);
  {
    if (w == 7) wait_flags((GAS u64*)(ws + WS_FL1) + h * 16, 16, tag, lane, dead);
    BARRIER();
    STAMP(5);
    if (w < 4) {
      u32x4 pt[16];
#pragma unroll
      for (int i = 0; i < 16; ++i) pt[i] = ld16c(rws, lane * 16, WS_KVP + ((h * 16 + ((i + 2 * m) & 15)) * 4 + w) * 1024);
      float kv[8];
#pragma unroll
      for (int k = 0; k < 4; ++k) { kv[2 * k] = bflo(pt[0][k]); kv[2 * k + 1] = bfhi(pt[0][k]); }
#pragma unroll
      for (int i = 1; i < 16; ++i)
#pragma unroll
        for (int k = 0; k < 4; ++k) { kv[2 * k] += bflo(pt[i][k]); kv[2 * k + 1] += bfhi(pt[i][k]); }
      LAS char* kp = smem + L_KVF + ((w >> 1) * 128 + lane) * 16 + 8 * (w & 1);
      *(LAS u32x2*)kp = u32x2{pk(kv[0], kv[1]), pk(kv[2], kv[3])};
      *(LAS u32x2*)(kp + 1024) = u32x2{pk(kv[4], kv[5]), pk(kv[6], kv[7])};
    }
    WAITV0(); BARRIER();
    STAMP(6);
    f32x4 o0 = z4, o1 = z4;
    LAS u32x4* kf = (LAS u32x4*)(smem + L_KVF) + lane;
    o0 = mfma(kf[0], phq_pos, o0); o1 = mfma(kf[64], phq_pos, o1);
    o0 = mfma(kf[128], phq_neg, o0); o1 = mfma(kf[192], phq_neg, o1);
    float t0, den = o1[0];
    x32(den, t0); x16(t0, den);
    const float inv = __builtin_amdgcn_rcpf(den + 1e-6f);
    u32x4 of = {pk(o0[0] * inv, o0[1] * inv), pk(o0[2] * inv, o0[3] * inv), g3 ? 0u : pk(o1[0] * inv, o1[1] * inv), g3 ? 0u : pk(o1[2] * inv, o1[3] * inv)};
    st16c(rws, of, lane * 16, WS_O + ((m * 8 + myMt) * 16 + h) * 1024);
    if (worker) w4_read(0, 5);
    WAITV0(); BARRIER();
    if (tid == 448) __hip_atomic_store((GAS u64*)(ws + WS_FL2) + m * 16 + h, tag, RLX);
    STAMP(7);
  }
  if (!worker) {
#pragma unroll 1
    for (int k = 0; k < 7; ++k) {
      const int r = (rr - 28) * 64 + lane;
      if (w < 7) touch(dD, (u32)((224 * (r >> 5) + 32 * k + (r & 31)) * 896 + w * 128));
    }
    WAITV0();
    return;
  }
#pragma unroll
  for (int i = 0; i < 5; ++i) w4_issue(5 + i, i);
  f32x4 hA, hB;
  {
    if (w == 7) wait_flags((GAS u64*)(ws + WS_FL2) + (t64 >> 1) * 16, 16, tag, lane, dead);
    BARRIER();
    STAMP(8);
    if (b == 0 && tid == 448) __hip_atomic_store((GAS u64*)(ws + WS_EPOCH), tag, RLX);
    {
      const int rb = (w + c7) & 7;
#pragma unroll
      for (int i = 0; i < 8; ++i) dma16c(dWS, 81920 + (8 * i + rb) * 1024, lane * 16, WS_O + (mt0 * 16 + (i >> 2) * 64 + 8 * (i & 3) + rb) * 1024);
    }
    WAITV0(); BARRIER();
    STAMP(9);
    hA = f32x4{bflo(xra[0]), bfhi(xra[0]), bflo(xra[1]), bfhi(xra[1])};
    hB = f32x4{bflo(xrb[0]), bfhi(xrb[0]), bflo(xrb[1]), bfhi(xrb[1])};
    LAS char* ap = smem + 81920 + mp * 32768 + lane * 16;
#pragma unroll
    for (int s = 0; s < 16; ++s) {
      hA = mfma(w3f[s], *(LAS u32x4*)(ap + 1024 * s), hA);
      hB = mfma(w3f[s], *(LAS u32x4*)(ap + 16384 + 1024 * s), hB);
    }
    const u32 a0 = pk(hA[0], hA[1]), a1 = pk(hA[2], hA[3]), b0 = pk(hB[0], hB[1]), b1 = pk(hB[2], hB[3]);
    u32x4 hv = pair16(a0, a1, b0, b1);
    st16c(rws, hv, (g & 1) * 14336 + (2 * (nt & 1) + (g >> 1)) * 256 + c16 * 16,
          WS_H + ((mt0 + 4 * mp) * 14 + 2 * cs + (nt >> 1)) * 1024);
    {
      float sA = gsum(dot2(a0, 0x3f803f80u, dot2(a1, 0x3f803f80u, 0.f))), qA = gsum(dot2(a0, a0, dot2(a1, a1, 0.f)));
      float sB = gsum(dot2(b0, 0x3f803f80u, dot2(b1, 0x3f803f80u, 0.f))), qB = gsum(dot2(b0, b0, dot2(b1, b1, 0.f)));
      if (g < 2) *(LAS u32x2*)(smem + L_PS + ((mp * 32 + g * 16 + c16) * 4 + nt) * 8) =
          g ? u32x2{__float_as_uint(sB), __float_as_uint(qB)} : u32x2{__float_as_uint(sA), __float_as_uint(qA)};
    }
    BARRIER();
    if (w == 7) {
      LAS f32x4* pp = (LAS f32x4*)(smem + L_PS + lane * 32);
      f32x4 u = pp[0], v = pp[1];
      u32x2 o2 = {__float_as_uint((u[0] + u[2]) + (v[0] + v[2])), __float_as_uint((u[1] + u[3]) + (v[1] + v[3]))};
      __builtin_amdgcn_raw_buffer_store_b64(o2, rws, WS_ST + (cs * 2048 + mt0 * 16 + (lane >> 5) * 64 + (lane & 31)) * 8, 0, 16);
    }
    w4_read(5, 5);
    WAITV0(); BARRIER();
    if (tid == 448) __hip_atomic_store((GAS u64*)(ws + WS_FL3) + (t64 >> 1) * 16 + (t64 & 1) * 7 + cs, tag, RLX);
    STAMP(10);
  }
  u32 v5[2];
#pragma unroll
  for (int v = 0; v < 2; ++v) v5[v] = (lane >> 3) * 896 + 32 * (((lane >> 1) & 3) ^ (((lane >> 4) & 1) | (v << 1))) + 16 * (lane & 1) + 128 * cs;
  const u32 b5 = 1024 * g + 128 * q + 8 * p;
  const u32 STG = 114688 + 4096 * w;
  u32x4 w5f[7][4];
  auto w5_issue = [&](int ks, u32 dst) {
#pragma unroll
    for (int i = 0; i < 4; ++i) dma16(dD, dst + i * 1024, v5[i & 1], (u32)(224 * w + 32 * ks + 8 * i) * 896);
  };
  auto w5_read = [&](u32x4* f, u32 src) {
#pragma unroll
    for (int n = 0; n < 4; ++n) { LAS char* a = smem + src + b5 + 32 * ((u32)n ^ phi_sw); f[n] = tr2(a, a + 512); }
  };
  auto w5_home = [&](int pair) { return w < 7 ? (u32)(28672 * pair + 4096 * w) : (u32)(L_MISC + 4096 * pair); };
#pragma unroll
  for (int i = 0; i < 4; ++i) w4_issue(10 + i, i);
  {
    WAITV0(); BARRIER();
    w4_read(10, 4);
    if (w == 7) wait_flags((GAS u64*)(ws + WS_FL3) + m4 * 16, 14, tag, lane, dead);
    BARRIER();
    STAMP(11);
    const int rb4 = (w + c7) & 7;
    auto hdma = [&](int j, int k) {
      int blk = 8 * k + rb4; if (blk >= 28) blk -= 8;
      dma16c(dWS, (28 * j + blk) * 1024, lane * 16, WS_H + (m4 * 112 + 28 * j + blk) * 1024);
    };
    {
      u32x2 s0, s1;
      {
        const u32 vo = WS_ST + (u32)(g * 2048 + m4 * 128 + w * 16 + c16) * 8;
        const u32 vo1 = g3 ? vo : vo + 4 * 2048 * 8;
        asm volatile("buffer_load_dwordx2 %0, %2, %4, 0 offen sc1\n\tbuffer_load_dwordx2 %1, %3, %4, 0 offen sc1"
                     : "=&v"(s0), "=&v"(s1) : "v"(vo), "v"(vo1), "s"(dWS) : "memory");
      }
#pragma unroll
      for (int k = 0; k < 8; ++k) hdma(k >> 2, k & 3);
      asm volatile("s_waitcnt vmcnt(8)" : "+v"(s0), "+v"(s1)::"memory");
      float sm = __uint_as_float(s0[0]) + (g3 ? 0.f : __uint_as_float(s1[0]));
      float sq = __uint_as_float(s0[1]) + (g3 ? 0.f : __uint_as_float(s1[1]));
      sm = gsum(sm); sq = gsum(sq);
      const float mean = sm * (1.f / D_);
      const float rstd = __builtin_amdgcn_rsqf(fmaxf(sq * (1.f / D_) - mean * mean, 0.f) + 1e-5f);
      if (g == 0) *(LAS u32x2*)(smem + L_ST + (w * 16 + c16) * 8) = u32x2{__float_as_uint(mean), __float_as_uint(rstd)};
    }
    WAITV(4); BARRIER();
    STAMP(12);
    const int ftile = 8 * fs + w;
    const u32 svo = (g & 1) * 57344 + (2 * (ftile & 1) + (g >> 1)) * 256 + c16 * 16 + WS_HH + (m4 * 8 * 56 + (ftile >> 1)) * 1024;
    LAS char* ap = smem + (g & 1) * 14336 + (g >> 1) * 256 + c16 * 16;
    LAS char* sp = smem + L_ST + (lane & 31) * 8;
    auto xoff = [](int n) { return 28672 * (n / 28) + 1024 * ((n % 28) >> 1) + 512 * (n & 1); };
    u32x4 ring[4];
#pragma unroll
    for (int k = 0; k < 4; ++k) ring[k] = *(LAS u32x4*)(ap + xoff(k));
    STR(0);
    auto rd = [&](int ks, int n) { LAS char* a = smem + STG + b5 + 32 * ((u32)n ^ phi_sw); w5f[ks][n] = tr2(a, a + 512); };
    auto is = [&](int ks, int i, u32 dst) { dma16(dD, dst + i * 1024, v5[i & 1], (u32)(224 * w + 32 * ks + 8 * i) * 896); };
    auto hook = [&](int T, int s) {
      if (T == 0) {
        if (s >= 1 && s < 9) hdma(2 + ((s - 1) >> 2), (s - 1) & 3);
        if (s >= 10 && s < 14) is(0, s - 10, STG);
        if (s == 20) { WAITV(12); RAWBAR(); }
      }
      if (T == 1) {
        if (s == 20) { WAITV(0); RAWBAR(); }
        if (s >= 21 && s < 25) { rd(0, s - 21); is(3, s - 21, w5_home(0)); }
      }
      if (T == 2) {
        if (s >= 2 && s < 6) is(1, s - 2, STG);
        if (s == 20) RAWBAR();
        if (s >= 21 && s < 25) is(4, s - 21, w5_home(1));
      }
      if (T == 3) {
        if (s == 4) { WAITV(5); RAWBAR(); }
        if (s >= 5 && s < 9) { rd(1, s - 5); is(5, s - 5, w5_home(2)); }
        if (s >= 14 && s < 18) is(2, s - 14, STG);
        if (s == 22) {
          WAITV(8); RAWBAR();
          if (tid == 448) __hip_atomic_store((GAS u64*)(ws + WS_FL4) + m4 * 16 + fs, tag, RLX);
        }
      }
    };
    f32x16 acc[2];
    u32x2 stt[2];
    float t[8], u[8], rn;
    auto epi = [&](const f32x16& E, u32x2 ST, int s, int tile) {
      const float mean = __uint_as_float(ST[0]), rstd = __uint_as_float(ST[1]);
      if (s == 0) rn = rstd * -1.4426950408889634f;
      if (s >= 1 && s <= 8) t[s - 1] = __builtin_amdgcn_exp2f(rn * fmaf(-mean, cw[s - 1], E[s - 1]));
      if (s >= 9 && s <= 16) { t[s - 9] = __builtin_amdgcn_rcpf(1.f + t[s - 9]); u[s - 9] = fmaf(-mean, cw[s - 1], E[s - 1]); }
      if (s >= 17 && s <= 24) u[s - 17] = (u[s - 17] * rstd) * t[s - 17];
      if (s == 26) {
        auto r0 = __builtin_amdgcn_permlane32_swap(pk(u[0], u[1]), pk(u[4], u[5]), false, false);
        auto r1 = __builtin_amdgcn_permlane32_swap(pk(u[2], u[3]), pk(u[6], u[7]), false, false);
        st16c(rws, u32x4{r0[0], r1[0], r0[1], r1[1]}, svo, tile * 2 * 56 * 1024);
      }
    };
#pragma unroll
    for (int T = 0; T < 4; ++T) {
      acc[T & 1] = f32x16{};
      stt[T & 1] = *(LAS u32x2*)(sp + 256 * T);
      __builtin_amdgcn_sched_barrier(0);
#pragma unroll
      for (int s = 0; s < 28; ++s) {
        const int n = 28 * T + s;
        acc[T & 1] = mfma32(w4f[s], ring[n & 3], acc[T & 1]);
        if (n + 4 < 112) ring[n & 3] = *(LAS u32x4*)(ap + xoff(n + 4));
        if (T > 0) epi(acc[(T - 1) & 1], stt[(T - 1) & 1], s, T - 1);
        hook(T, s);
        __builtin_amdgcn_sched_barrier(0);
      }
      STR(1 + T);
    }
#pragma unroll
    for (int s = 0; s < 27; ++s) epi(acc[1], stt[1], s, 3);
    STR(9);
  }
  {
    if (w == 7) wait_flags((GAS u64*)(ws + WS_FL4) + (t64 >> 1) * 16, 14, tag, lane, dead);
    else WAITV(6);
    w5_read(w5f[3], w5_home(0)); w5_read(w5f[4], w5_home(1)); w5_read(w5f[5], w5_home(2));
    RAWBAR();
    STAMP(13);
    {
      u32x4 xf[2][7];
      const int r2 = c7 & 1;
      const u32 home3 = w < 7 ? (u32)(86016 + 4096 * w) : (u32)L_MISC;
      if (w == 7) asm volatile("s_waitcnt lgkmcnt(0)" ::: "memory");
      w5_issue(6, home3);
#pragma unroll
      for (int s = 0; s < 2; ++s) xf[0][s] = ld16c(rws, lane * 16, WS_HH + ((mt0 + r2) * 56 + 7 * w + s) * 1024);
      __builtin_amdgcn_sched_barrier(0);
      WAITV(6);
      w5_read(w5f[2], STG);
      RAWBAR();
      if (tid == 448) __hip_atomic_store((GAS u64*)(ws + WS_FL5) + m4 * 16 + fs, tag, RLX);
      STR(10); STFLUSH2(11);
      STAMP(14);
      __builtin_amdgcn_sched_barrier(0);
#pragma unroll
      for (int k = 2; k < 14; ++k) xf[k / 7][k % 7] = ld16c(rws, lane * 16, WS_HH + ((mt0 + ((k / 7) ^ r2)) * 56 + 7 * w + k % 7) * 1024);
      __builtin_amdgcn_sched_barrier(0);
      WAITV(14);
      w5_read(w5f[6], home3);
      BARRIER();
      __builtin_amdgcn_sched_barrier(0);
#pragma unroll
      for (int i = 0; i < 4; ++i) {
        const int mt = (i & 2) | ((i & 1) ^ r2);
        f32x4 a[4] = {z4, z4, z4, z4};
#pragma unroll
        for (int s = 0; s < 7; ++s)
#pragma unroll
          for (int n = 0; n < 4; ++n) a[n] = mfma(w5f[s][n], xf[i & 1][s], a[n]);
        __builtin_amdgcn_sched_barrier(0);
        if (i == 0) {
          if (w == 7) wait_flags((GAS u64*)(ws + WS_FL5) + (t64 >> 1) * 16, 14, tag, lane, dead);
          BARRIER();
        }
        if (i < 2) {
#pragma unroll
          for (int s = 0; s < 7; ++s) xf[i & 1][s] = ld16c(rws, lane * 16, WS_HH + ((mt0 + 4 + (i ^ r2)) * 56 + 7 * w + s) * 1024);
        }
        __builtin_amdgcn_sched_barrier(0);
#pragma unroll
        for (int n = 0; n < 4; ++n) *(LAS f32x4*)(smem + (((n * 4 + mt) * 8 + w) * 64 + lane) * 16) = a[n];
      }
    }
    BARRIER();
    STAMP(15);
    LAS f32x4* rp = (LAS f32x4*)(smem + (w * 2 * 8 * 64 + lane) * 16);
#pragma unroll
    for (int i = 0; i < 8; ++i) { hA += rp[i * 64]; hB += rp[512 + i * 64]; }
    u32x4 ov = pair16(pk(hA[0], hA[1]), pk(hA[2], hA[3]), pk(hB[0], hB[1]), pk(hB[2], hB[3]));
    GAS char* op = (GAS char*)out + (((mt0 + 4 * mp + (g & 1)) * 16 + c16) * D_ + 64 * cs + 16 * nt + 8 * (g >> 1)) * 2;
    STAMP(16);
    *(GAS u32x4*)op = ov;
  }
  asm volatile(".Lcp_end:");
}

extern "C" void kernel_launch(void* const* d_in, const int* in_sizes, int n_in, void* d_out, int out_size, void* d_ws,
                              size_t ws_size, hipStream_t stream) {
  hipFuncSetAttribute((const void*)fused_block, hipFuncAttributeMaxDynamicSharedMemorySize, LDS_BYTES);
  fused_block<<<256, 512, LDS_BYTES, stream>>>((const uint16_t*)d_in[0], (const uint16_t*)d_in[1], (const uint16_t*)d_in[2],
                                               (const uint16_t*)d_in[3], (const uint16_t*)d_in[5], (const uint16_t*)d_in[6],
                                               (const uint16_t*)d_in[7], (uint16_t*)d_out, (char*)d_ws);
}
```

```cpp
#define OUT_WT 2
#define CODE_PULL
#include <hip/hip_runtime.h>
#include <stdint.h>

#define DEVI __device__ __forceinline__
#define LAS __attribute__((address_space(3)))
#define GAS __attribute__((address_space(1)))
typedef unsigned u32;
typedef unsigned long long u64;
typedef u32 u32x2 __attribute__((ext_vector_type(2)));
typedef u32 u32x4 __attribute__((ext_vector_type(4)));
typedef float f32x4 __attribute__((ext_vector_type(4)));
typedef float f32x16 __attribute__((ext_vector_type(16)));
typedef __bf16 bf16x8 __attribute__((ext_vector_type(8)));
typedef __bf16 bf16x2 __attribute__((ext_vector_type(2)));
typedef short s16x4 __attribute__((ext_vector_type(4)));
typedef __amdgpu_buffer_rsrc_t rsrc_t;

constexpr int S_ = 2048, D_ = 448, F_ = 1792;
constexpr u32 WS_EPOCH = 0;
constexpr u32 WS_FL1 = 4096, WS_FL2 = WS_FL1 + 16 * 128, WS_FL3 = WS_FL2 + 16 * 128, WS_FL4 = WS_FL3 + 16 * 128, WS_FL5 = WS_FL4 + 16 * 128;
constexpr u32 WS_KVP = 65536;
constexpr u32 WS_O = WS_KVP + 16 * 16 * 8192;
constexpr u32 WS_H = WS_O + 128 * 16 * 1024;
constexpr u32 WS_HH = WS_H + 128 * 14 * 1024;
constexpr u32 WS_ST = WS_HH + 128 * 56 * 1024;
constexpr u32 WS_END = WS_ST + 7 * 2048 * 8;
constexpr u32 WS_STAMP = WS_END;
constexpr u32 SLOT = 28672;
constexpr u32 L_RAW = 4 * SLOT;
constexpr u32 L_PHI = 0, L_V = 16384;
constexpr u32 L_MISC = 147456;
constexpr u32 L_KVF = L_MISC, L_WPHI = L_MISC + 4096, L_CS = L_MISC + 6144  , L_ST = L_MISC + 12288  , L_PS = L_MISC + 13312  , L_DUMMY = L_MISC + 15360;
constexpr u32 LDS_BYTES = 163840;

DEVI f32x4 mfma(u32x4 a, u32x4 b, f32x4 c) {
  return __builtin_amdgcn_mfma_f32_16x16x32_bf16(__builtin_bit_cast(bf16x8, a), __builtin_bit_cast(bf16x8, b), c, 0, 0, 0);
}
DEVI f32x16 mfma32(u32x4 a, u32x4 b, f32x16 c) {
  return __builtin_amdgcn_mfma_f32_32x32x16_bf16(__builtin_bit_cast(bf16x8, a), __builtin_bit_cast(bf16x8, b), c, 0, 0, 0);
}
DEVI float vmax(float a, float b) { return __builtin_elementwise_maximum(a, b); }
DEVI float vmin(float a, float b) { return __builtin_elementwise_minimum(a, b); }
DEVI u32 pk(float a, float b) { bf16x2 v = {(__bf16)a, (__bf16)b}; return __builtin_bit_cast(u32, v); }
DEVI float bflo(u32 v) { return __uint_as_float(v << 16); }
DEVI float bfhi(u32 v) { return __uint_as_float(v & 0xffff0000u); }
DEVI u32x2 trr(LAS char* p) { return __builtin_bit_cast(u32x2, __builtin_amdgcn_ds_read_tr16_b64_v4i16((LAS s16x4*)p)); }
DEVI u32x4 tr2(LAS char* p0, LAS char* p1) { u32x2 a = trr(p0), b = trr(p1); u32x4 r = {a[0], a[1], b[0], b[1]}; return r; }
DEVI float dot2(u32 a, u32 b, float c) {
  return __builtin_amdgcn_fdot2_f32_bf16(__builtin_bit_cast(bf16x2, a), __builtin_bit_cast(bf16x2, b), c, false);
}
DEVI float x16(float v, float& o) {
  auto r = __builtin_amdgcn_permlane16_swap(__float_as_uint(v), __float_as_uint(v), false, false);
  o = __uint_as_float(r[1]); return __uint_as_float(r[0]);
}
DEVI float x32(float v, float& o) {
  auto r = __builtin_amdgcn_permlane32_swap(__float_as_uint(v), __float_as_uint(v), false, false);
  o = __uint_as_float(r[1]); return __uint_as_float(r[0]);
}
DEVI float gsum(float v) { float b, a = x16(v, b); v = a + b; a = x32(v, b); return a + b; }
DEVI float gmax(float v) { float b, a = x16(v, b); v = vmax(a, b); a = x32(v, b); return vmax(a, b); }
DEVI float gmin(float v) { float b, a = x16(v, b); v = vmin(a, b); a = x32(v, b); return vmin(a, b); }
DEVI rsrc_t mkrs(const void* p, u32 bytes) { return __builtin_amdgcn_make_buffer_rsrc((void*)p, 0, bytes, 0x00020000); }
DEVI u32x4 mkd(const void* p, u32 bytes) {
  u64 a = (u64)p; u32x4 d = {(u32)a, (u32)(a >> 32) & 0xffffu, bytes, 0x00020000u}; return d;
}
DEVI void dma16(u32x4 d, u32 lds, u32 voff, u32 soff) {
  asm volatile("s_mov_b32 m0, %0\n\ts_nop 3\n\tbuffer_load_dwordx4 %1, %2, %3 offen lds" ::"s"(lds), "v"(voff), "s"(d), "s"(soff) : "memory", "m0");
}
DEVI void dma16c(u32x4 d, u32 lds, u32 voff, u32 soff) {
  asm volatile("s_mov_b32 m0, %0\n\ts_nop 3\n\tbuffer_load_dwordx4 %1, %2, %3 offen sc1 lds" ::"s"(lds), "v"(voff), "s"(d), "s"(soff) : "memory", "m0");
}
DEVI void touch(u32x4 d, u32 voff) {
  asm volatile("s_mov_b32 m0, %0\n\ts_nop 3\n\tbuffer_load_dword %1, %2, 0 offen lds" ::"s"(L_DUMMY), "v"(voff), "s"(d) : "memory", "m0");
}
#define RAWBAR() do { asm volatile("" ::: "memory"); __builtin_amdgcn_s_barrier(); asm volatile("" ::: "memory"); } while (0)
#define WAITV(n) asm volatile("s_waitcnt vmcnt(" #n ")" ::: "memory")
DEVI u32x4 ld16(rsrc_t rs, u32 voff, u32 soff) { return __builtin_amdgcn_raw_buffer_load_b128(rs, voff, soff, 0); }
DEVI u32x4 ld16c(rsrc_t rs, u32 voff, u32 soff) { return __builtin_amdgcn_raw_buffer_load_b128(rs, voff, soff, 16); }
DEVI void st16c(rsrc_t rs, u32x4 v, u32 voff, u32 soff) { __builtin_amdgcn_raw_buffer_store_b128(v, rs, voff + soff, 0, 16); }
#define WAITV0() asm volatile("s_waitcnt vmcnt(0)" ::: "memory")
#define BARRIER() do { asm volatile("s_waitcnt lgkmcnt(0)" ::: "memory"); __builtin_amdgcn_s_barrier(); asm volatile("" ::: "memory"); } while (0)
#define RLX __ATOMIC_RELAXED, __HIP_MEMORY_SCOPE_AGENT

#define STAMP(i)
#define STR(i) (void)tr_
#define STFLUSH(n)
#define STFLUSH2(n)

DEVI void wait_flags(GAS u64* fl, int n, u64 tag, int lane, int& dead) {
  int ln = lane < n ? lane : 0;
  for (u32 spins = 0; !dead; ++spins) {
    u64 v = __hip_atomic_load(fl + ln, RLX);
    if (__all(v == tag)) break;
    if (spins > (1u << 17)) dead = 1;
    __builtin_amdgcn_s_sleep(1);
  }
}

template <class OP>
DEVI void red2(float& a, float& b, OP op) {
  auto r = __builtin_amdgcn_permlane16_swap(__float_as_uint(a), __float_as_uint(b), false, false);
  float c = op(__uint_as_float(r[0]), __uint_as_float(r[1]));
  float d, e = x32(c, d);
  c = op(e, d);
  a = x16(c, b);
}
template <class HK>
DEVI void dual_softmax(const f32x4& p0, f32x4 p1, bool g3, u32x4& pos, u32x4& neg, HK hk) {
  const float L2E = 1.4426950408889634f;
  if (g3) p1 = p0;
  float mx = vmax(vmax(vmax(p0[0], p0[1]), vmax(p0[2], p0[3])), vmax(vmax(p1[0], p1[1]), vmax(p1[2], p1[3])));
  float mn = vmin(vmin(vmin(p0[0], p0[1]), vmin(p0[2], p0[3])), vmin(vmin(p1[0], p1[1]), vmin(p1[2], p1[3])));
  mn = -mn;
  red2(mx, mn, [](float u, float v) { return vmax(u, v); });
  hk(0);
  const float ca = -mx * L2E, cb = -mn * L2E;
  const float ca1 = g3 ? -__builtin_inff() : ca, cb1 = g3 ? -__builtin_inff() : cb;
  float a0[4], a1[4], b0[4], b1[4];
#pragma unroll
  for (int r = 0; r < 4; ++r) {
    a0[r] = __builtin_amdgcn_exp2f(fmaf(p0[r], L2E, ca));
    b0[r] = __builtin_amdgcn_exp2f(fmaf(p0[r], -L2E, cb));
    a1[r] = __builtin_amdgcn_exp2f(fmaf(p1[r], L2E, ca1));
    b1[r] = __builtin_amdgcn_exp2f(fmaf(p1[r], -L2E, cb1));
  }
  float sa = ((a0[0] + a0[1]) + (a0[2] + a0[3])) + ((a1[0] + a1[1]) + (a1[2] + a1[3]));
  float sb = ((b0[0] + b0[1]) + (b0[2] + b0[3])) + ((b1[0] + b1[1]) + (b1[2] + b1[3]));
  hk(1);
  red2(sa, sb, [](float u, float v) { return u + v; });
  sa = __builtin_amdgcn_rcpf(sa); sb = __builtin_amdgcn_rcpf(sb);
  hk(2);
  pos[0] = pk(a0[0] * sa, a0[1] * sa); pos[1] = pk(a0[2] * sa, a0[3] * sa);
  pos[2] = pk(a1[0] * sa, a1[1] * sa); pos[3] = pk(a1[2] * sa, a1[3] * sa);
  neg[0] = pk(b0[0] * sb, b0[1] * sb); neg[1] = pk(b0[2] * sb, b0[3] * sb);
  neg[2] = pk(b1[0] * sb, b1[1] * sb); neg[3] = pk(b1[2] * sb, b1[3] * sb);
}

DEVI u32x4 pair16(u32 a0, u32 a1, u32 b0, u32 b1) {
  auto r0 = __builtin_amdgcn_permlane16_swap(a0, b0, false, false);
  auto r1 = __builtin_amdgcn_permlane16_swap(a1, b1, false, false);
  u32x4 o = {r0[0], r1[0], r0[1], r1[1]};
  return o;
}

__global__ __launch_bounds__(512, 2) void fused_block(const uint16_t* __restrict__ x, const uint16_t* __restrict__ wqkv,
                                                      const uint16_t* __restrict__ wphi, const uint16_t* __restrict__ wo,
                                                      const uint16_t* __restrict__ wg, const uint16_t* __restrict__ wu,
                                                      const uint16_t* __restrict__ wd, uint16_t* __restrict__ out,
                                                      char* __restrict__ ws_) {
  extern __shared__ __attribute__((aligned(16))) char smem_[];
  LAS char* smem = (LAS char*)smem_;
  u32 tr_[12];
  u64 pt0_ = 0, pt1_ = 0;
  STR(0);
  asm volatile("" ::"s"(x), "s"(wqkv), "s"(wphi), "s"(wo), "s"(wg), "s"(wu), "s"(wd), "s"(out), "s"(ws_));
  GAS char* ws = (GAS char*)ws_;
  const int tid = threadIdx.x, lane = tid & 63;
  const int w = __builtin_amdgcn_readfirstlane(tid >> 6);
  const int g = lane >> 4, c16 = lane & 15, q = (lane >> 2) & 3, p = lane & 3;
  const bool g3 = (g == 3);
  const int b = blockIdx.x, xc = b & 7, rr = b >> 3;
  const rsrc_t rws = mkrs(ws_, WS_END);
  int dead = 0;
  STAMP(0);
  const u32x4 ones = {0x3f803f80u, 0x3f803f80u, 0x3f803f80u, 0x3f803f80u};
  const f32x4 z4 = {0.f, 0.f, 0.f, 0.f};
  const u32 phi_sw = (q >> 1) | ((g & 1) << 1);
  const bool worker = b < 224;
  const int h = 4 * (xc & 3) + (rr & 3), m = 8 * (xc >> 2) + (rr >> 2);
  const int r7 = worker ? rr / 7 : 0, c7 = worker ? rr % 7 : 0;
  const int t64 = 4 * xc + r7, cs = c7;
  const int mt0 = (t64 >> 1) * 8 + 2 * (t64 & 1);
  const int m4 = 4 * (xc >> 1) + r7, fs = 7 * (xc & 1) + c7;
  const int mp = w & 1, nt = w >> 1;
  const u32x4 dX = mkd(x, S_ * D_ * 2), dW1 = mkd(wqkv, D_ * 3 * D_ * 2), dW3 = mkd(wo, D_ * D_ * 2);
  const u32x4 dG = mkd(wg, D_ * F_ * 2), dU = mkd(wu, D_ * F_ * 2), dD = mkd(wd, F_ * D_ * 2), dWS = mkd(ws_, WS_END);

  u64 tag;
  u32x2 xra, xrb;
  u32 v4[2];
#pragma unroll
  for (int v = 0; v < 2; ++v) v4[v] = (lane >> 4) * 3584 + 32 * (((lane >> 1) & 7) ^ ((lane >> 4) | (v << 2))) + 16 * (lane & 1) + 256 * fs;
  const u32 b4 = 8192 * (g & 1) + 2048 * (g >> 1) + 256 * q + 32 * ((u32)w ^ (q | ((g & 1) << 2))) + 8 * p;
  const int rw4 = (w + 2 * r7) & 7;
  u32x4 w4f[28];
  f32x16 cw = {};
  auto w4_issue = [&](int u, int slot) {
    dma16(dG, slot * 16384 + rw4 * 1024, v4[0], (u32)(8 * u + rw4) * 4 * 3584);
    dma16(dU, slot * 16384 + 8192 + rw4 * 1024, v4[1], (u32)(8 * u + rw4) * 4 * 3584);
  };
  auto w4_read = [&](int u0, int n) {
#pragma unroll
    for (int k = 0; k < 2 * n; ++k) { LAS char* a = smem + b4 + 16384 * (k >> 1) + 4096 * (k & 1); w4f[2 * u0 + k] = tr2(a, a + 1024); }
  };
  auto w4_sum = [&](int u0, int n) {
#pragma unroll
    for (int k = 0; k < 2 * n; ++k) cw = mfma32(w4f[2 * u0 + k], ones, cw);
    asm volatile("" : "+v"(cw));
  };

  u32x4 phq_pos, phq_neg;
  u32x4 w3f[16];
  const int grp = w >> 2, tg = w & 3;
  const int myMt = 2 * tg + grp;
  {
    const int rot = (2 * (rr & 3) + (rr >> 2)) % 7;
    const u32 win0 = (56 * h) & ~15;
    u32 vx0, vx1, vw[2], v3;
    {
      const int r8 = lane >> 3, pos = lane & 7;
      vx0 = (u32)((m * 128 + w * 16 + r8) * 896 + 16 * (pos ^ (lane >> 4)));
      vx1 = (u32)((m * 128 + w * 16 + 8 + r8) * 896 + 16 * (pos ^ (4 + (lane >> 4))));
#pragma unroll
      for (int k = 0; k < 2; ++k) {
        const int j = w + 8 * k, G = j / 3, rl = 4 * (j - 3 * G) + g, rq = rl / 3, sec = rl - 3 * rq;
        vw[k] = (u32)((16 * G + 4 * rq + q) * 2688 + sec * 896 + win0 + 16 * (p ^ (2 * (rq >> 1))));
      }
      v3 = (u32)(r8 * 896 + 32 * (((lane >> 1) & 3) ^ ((lane >> 4) & 3)) + 16 * (lane & 1) + 128 * cs);
    }
    auto issue_chunk = [&](int i, u32 slot) {
      int c = rot + i; c = c >= 7 ? c - 7 : c;
      dma16(dX, slot + w * 2048, vx0, c * 128);
      dma16(dW1, slot + 16384 + w * 1024, vw[0], c * 64 * 2688);
      dma16(dX, slot + w * 2048 + 1024, vx1, c * 128);
      if (w < 4) dma16(dW1, slot + 24576 + w * 1024, vw[1], c * 64 * 2688);
    };
    auto w3_at = [&](int hd) { return (u32)(hd < 7 ? 28672 + 4096 * hd : hd < 14 ? 86016 + 4096 * (hd - 7) : 57344 + 4096 * (hd - 14)); };
    auto ex = [&](int k) {
      const int hd = 2 * k + (w >> 2);
      __builtin_amdgcn_sched_barrier(0);
      dma16(dW3, (w < 4 ? w3_at(2 * k) : w3_at(2 * k + 1)) + (w & 3) * 1024, v3, (28 * hd + 8 * (w & 3)) * 896);
      if (k < 7 && w == k) {
        const int l2 = 56 * k + (lane < 56 ? lane : 0), up = l2 >= 196;
        const int idx = rr * 196 + l2 - 196 * up;
        const u32 vo = (u32)((idx / 14) * 3584 + (xc & 1) * 1792 + (idx % 14) * 128);
        if (k <= 3) touch(dG, up ? 0x40000000u : vo);
        if (k >= 3) touch(dU, up ? vo : 0x40000000u);
      }
      __builtin_amdgcn_sched_barrier(0);
    };
    if (w == 7) {
      const u32x4 dP = mkd(wphi, 16 * 784 * 2);
      dma16(dP, L_WPHI, lane * 16, h * 1568); dma16(dP, L_WPHI + 1024, lane * 16, h * 1568 + 1024);
    }
    issue_chunk(0, 0); RAWBAR();
    issue_chunk(1, SLOT);
    {
      const GAS char* ep = ws + WS_EPOCH;
      const GAS char* xp = (const GAS char*)x + (((mt0 + 4 * mp) * 16 + c16) * D_ + 64 * cs + 16 * nt + 4 * g) * 2;
      asm volatile("global_load_dwordx2 %0, %3, off sc1\n\tglobal_load_dwordx2 %1, %4, off\n\tglobal_load_dwordx2 %2, %5, off"
                   : "=&v"(tag), "=&v"(xra), "=&v"(xrb) : "v"(ep), "v"(xp), "v"(xp + 16 * D_ * 2) : "memory");
    }
    if (w == 6) {
      u64 pc; u32 len;
      asm volatile("s_getpc_b64 %0\n.Lcp_start:\n\ts_mov_b32 %1, .Lcp_end-.Lcp_start" : "=s"(pc), "=s"(len));
      const u32x4 dC = {(u32)pc, (u32)(pc >> 32) & 0xffffu, len, 0x00020000u};
      touch(dC, (u32)(((rr & 7) * 64 + lane) * 128));
    }
    STR(1);
    u32 wb[3];
#pragma unroll
    for (int dt = 0; dt < 2; ++dt) {
      u32 u = 8 * (h & 1) + 32 * dt + 8 * p; if (u >= 64) u = 56;
      wb[dt] = 16384 + 1536 * g + 64 * q + (u ^ (32 * (g & 1)));
    }
    wb[2] = grp ? wb[1] : wb[0];
    wb[0] += 256 * (1 + grp); wb[1] += 256 * (1 + grp);
    const u32 xb = 128 * c16 + 16 * (g ^ ((c16 >> 1) & 7)) + myMt * 2048;
    u32x4 wpf[2];
    f32x4 A[3] = {z4, z4, z4}, B[3] = {z4, z4, z4}, C = z4;
    f32x4 S1 = z4, S2 = z4;
    u32x4 x0[2][2], x1[2][2], wf[2][2][3];
#pragma unroll
    for (int i = 0; i < 8; ++i) {
      if (i < 7) {
        if (i < 5) WAITV(6); else if (i == 5) WAITV(3); else WAITV(0);
        BARRIER();
        STR(2 + i);
      }
      if (i == 0 && grp) issue_chunk(2, 2 * SLOT);
      if (i == 1) issue_chunk(3, 3 * SLOT);
      if (i >= 1 && i <= 3 && grp) issue_chunk(i + 3, ((i + 3) & 3) * SLOT);
      if (i == 6 && grp) { ex(0); ex(1); }
      __builtin_amdgcn_sched_barrier(0);
      if (i < 7) {
        LAS char* sl = smem + (i & 3) * SLOT;
#pragma unroll
        for (int e = 0; e < 2; ++e) {
          x0[i & 1][e] = *(LAS u32x4*)(sl + (xb ^ (64 * e)));
#pragma unroll
          for (int n = 0; n < 3; ++n) wf[i & 1][e][n] = tr2(sl + wb[n] + 6144 * e, sl + wb[n] + 6144 * e + 768);
          x1[i & 1][e] = *(LAS u32x4*)(sl + (xb ^ (64 * e) ^ 2048));
        }
        __builtin_amdgcn_sched_barrier(0);
      }
      if (i > 0) {
        const int c = (i - 1) & 1;
#pragma unroll
        for (int e = 0; e < 2; ++e) {
#pragma unroll
          for (int n = 0; n < 3; ++n) { A[n] = mfma(wf[c][e][n], x0[c][e], A[n]); B[n] = mfma(wf[c][e][n], x1[c][e], B[n]); }
          S1 = mfma(ones, x0[c][e], S1);
          S2 = mfma(x0[c][e], x0[c][e], S2);
        }
        asm volatile("" : "+v"(A[0]), "+v"(A[1]), "+v"(A[2]), "+v"(B[0]), "+v"(B[1]), "+v"(B[2]), "+v"(S1), "+v"(S2));
#pragma unroll
        for (int n = 0; n < 3; ++n)
          if (tg == n) { C = mfma(wf[c][0][n], ones, C); C = mfma(wf[c][1][n], ones, C); }
      }
      __builtin_amdgcn_sched_barrier(0);
      if (i == 0 && !grp) issue_chunk(2, 2 * SLOT);
      if (i >= 1 && i <= 3 && !grp) issue_chunk(i + 3, ((i + 3) & 3) * SLOT);
      if (i == 6 && !grp) { ex(0); ex(1); }
      __builtin_amdgcn_sched_barrier(0);
      if (i == 0) {
        LAS char* a = smem + L_WPHI + 56 * (4 * g + q) + 8 * p;
        wpf[0] = tr2(a, a + 896); wpf[1] = tr2(a + 32, a + 32 + 896);
        asm volatile("" : "+v"(wpf[0]), "+v"(wpf[1]));
        const u32 mg = g3 ? 0u : ~0u, mf = c16 >= 12 ? 0u : ~0u;
        wpf[0][2] &= mg; wpf[0][3] &= mg; wpf[1][0] &= mf; wpf[1][1] &= mf; wpf[1][2] &= mg & mf; wpf[1][3] &= mg & mf;
        asm volatile("" : "+v"(wpf[0]), "+v"(wpf[1]));
      }
      if (i == 4) asm volatile("" : "+v"(tag), "+v"(xra), "+v"(xrb));
    }
    tag += 1;
    {
      LAS f32x4* rp = (LAS f32x4*)(smem + L_RAW + w * 3072) + lane;
      rp[0] = B[0]; rp[64] = B[1]; rp[128] = B[2];
      if (tg < 3 && c16 == 0) *(LAS f32x4*)(smem + L_CS + (tg < 2 ? 2 * grp + tg : 4 + grp) * 64 + 16 * g) = C;
    }
    ex(2);
    const float mean = S1[0] * (1.f / D_);
    const float dg = p == 0 ? S2[0] : p == 1 ? S2[1] : p == 2 ? S2[2] : S2[3];
    const float rstd = __builtin_amdgcn_rsqf(fmaxf(gsum(g == q ? dg : 0.f) * (1.f / D_) - mean * mean, 0.f) + 1e-5f);
    BARRIER();
    STR(9);
    STAMP(1); STAMP(2);
    LAS f32x4* cp = (LAS f32x4*)(smem + L_CS + 16 * g);
    f32x4 r2;
    {
      LAS f32x4* rp = (LAS f32x4*)(smem + L_RAW + (w ^ 4) * 3072) + lane;
      f32x4 r0 = rp[0], r1 = rp[64];
      r2 = rp[128];
      ex(3);
      f32x4 acc[4] = {grp ? r0 : A[0], grp ? r1 : A[1], grp ? A[0] : r0, grp ? A[1] : r1};
#pragma unroll
      for (int n = 0; n < 4; ++n) {
        const f32x4 c = cp[4 * n];
#pragma unroll
        for (int r = 0; r < 4; ++r) acc[n][r] = rstd * (acc[n][r] - mean * c[r]);
      }
      u32x4 kf = {pk(acc[0][0], acc[0][1]), pk(acc[0][2], acc[0][3]), g3 ? 0u : pk(acc[1][0], acc[1][1]), g3 ? 0u : pk(acc[1][2], acc[1][3])};
      f32x4 p0 = mfma(wpf[0], kf, z4), p1 = mfma(wpf[1], kf, z4);
      ex(4);
      LAS char* vr = smem + L_V + 64 * (16 * myMt + c16) + 8 * g;
      const u32 vs = (c16 >> 3) & 1;
      *(LAS u32x2*)(vr + 32 * (0 ^ vs)) = u32x2{pk(acc[2][0], acc[2][1]), pk(acc[2][2], acc[2][3])};
      *(LAS u32x2*)(vr + 32 * (1 ^ vs)) = g3 ? u32x2{0x00003f80u, 0u} : u32x2{pk(acc[3][0], acc[3][1]), pk(acc[3][2], acc[3][3])};
      u32x4 pos, neg;
      dual_softmax(p0, p1, g3, pos, neg, [&](int k) { ex(5 + k); });
      const u32 rowsw = ((c16 >> 1) & 1) | (((c16 >> 3) & 1) << 1);
      LAS char* pr = smem + L_PHI + 128 * (16 * myMt + c16) + 8 * g;
      *(LAS u32x2*)(pr + 32 * (0 ^ rowsw)) = u32x2{pos[0], pos[1]};
      *(LAS u32x2*)(pr + 32 * (1 ^ rowsw)) = u32x2{pos[2], pos[3]};
      *(LAS u32x2*)(pr + 32 * (2 ^ rowsw)) = u32x2{neg[0], neg[1]};
      *(LAS u32x2*)(pr + 32 * (3 ^ rowsw)) = u32x2{neg[2], neg[3]};
    }
    BARRIER();
    STR(10);
    if (w >= 4) {
      LAS char* ap = smem + L_PHI + 1024 * g + 128 * q + 32 * ((u32)(w - 4) ^ phi_sw) + 8 * p;
      LAS char* bp0 = smem + L_V + 512 * g + 64 * q + 32 * (g & 1) + 8 * p;
      LAS char* bp1 = smem + L_V + 512 * g + 64 * q + 32 * (1 ^ (g & 1)) + 8 * p;
      f32x4 kv0 = z4, kv1 = z4;
#pragma unroll
      for (int s = 0; s < 4; ++s) {
        u32x4 a = tr2(ap + 4096 * s, ap + 4096 * s + 512);
        kv0 = mfma(a, tr2(bp0 + 2048 * s, bp0 + 2048 * s + 256), kv0);
        kv1 = mfma(a, tr2(bp1 + 2048 * s, bp1 + 2048 * s + 256), kv1);
      }
      const u32 a0 = pk(kv0[0], kv0[1]), a1 = pk(kv0[2], kv0[3]), b0 = pk(kv1[0], kv1[1]), b1 = pk(kv1[2], kv1[3]);
      const bool od = lane & 1;
      const u32 s0 = od ? a0 : b0, s1 = od ? a1 : b1;
      const u32 t0 = __builtin_amdgcn_mov_dpp(s0, 0xB1, 0xF, 0xF, true), t1 = __builtin_amdgcn_mov_dpp(s1, 0xB1, 0xF, 0xF, true);
      u32x4 kb = od ? u32x4{t0, t1, b0, b1} : u32x4{a0, a1, t0, t1};
      st16c(rws, kb, (lane & 1) * 512 + (lane >> 1) * 16, WS_KVP + ((h * 16 + m) * 4 + (w - 4)) * 1024);
    }
    WAITV0();
    BARRIER();
    if (tid == 448) __hip_atomic_store((GAS u64*)(ws + WS_FL1) + h * 16 + m, tag, RLX);
    STR(11); STFLUSH(12);
    STAMP(3);
    {
      LAS char* bp = smem + 128 * (4 * g + q) + 32 * ((u32)nt ^ phi_sw) + 8 * p;
#pragma unroll
      for (int s = 0; s < 16; ++s) w3f[s] = tr2(bp + w3_at(s), bp + w3_at(s) + 2048);
    }
    BARRIER();
    {
      auto gd = [&](int u) {
        __builtin_amdgcn_sched_barrier(0);
        if (worker) w4_issue(u, u);
        __builtin_amdgcn_sched_barrier(0);
      };
      gd(0);
      f32x4 q0 = grp ? r2 : A[2], q1 = grp ? A[2] : r2;
      const f32x4 c0 = cp[16], c1 = cp[20];
#pragma unroll
      for (int r = 0; r < 4; ++r) { q0[r] = rstd * (q0[r] - mean * c0[r]); q1[r] = rstd * (q1[r] - mean * c1[r]); }
      u32x4 qf = {pk(q0[0], q0[1]), pk(q0[2], q0[3]), g3 ? 0u : pk(q1[0], q1[1]), g3 ? 0u : pk(q1[2], q1[3])};
      f32x4 p0 = mfma(wpf[0], qf, z4), p1 = mfma(wpf[1], qf, z4);
      gd(1);
      dual_softmax(p0, p1, g3, phq_pos, phq_neg, [&](int k) { gd(2 + k); });
    }
  }
  STAMP(4);
  {
    if (w == 7) wait_flags((GAS u64*)(ws + WS_FL1) + h * 16, 16, tag, lane, dead);
    BARRIER();
    STAMP(5);
    {
      u32x2 pt[16];
#pragma unroll
      for (int i = 0; i < 16; ++i)
        pt[i] = __builtin_amdgcn_raw_buffer_load_b64(rws, lane * 8, WS_KVP + ((h * 16 + ((i + 2 * m) & 15)) * 8 + w) * 512, 16);
      float kv[4] = {bflo(pt[0][0]), bfhi(pt[0][0]), bflo(pt[0][1]), bfhi(pt[0][1])};
#pragma unroll
      for (int i = 1; i < 16; ++i) { kv[0] += bflo(pt[i][0]); kv[1] += bfhi(pt[i][0]); kv[2] += bflo(pt[i][1]); kv[3] += bfhi(pt[i][1]); }
      *(LAS u32x2*)(smem + L_KVF + (((w >> 2) * 2 + (w & 1)) * 64 + lane) * 16 + 8 * ((w >> 1) & 1)) = u32x2{pk(kv[0], kv[1]), pk(kv[2], kv[3])};
    }
    WAITV0(); BARRIER();
    STAMP(6);
    f32x4 o0 = z4, o1 = z4;
    LAS u32x4* kf = (LAS u32x4*)(smem + L_KVF) + lane;
    o0 = mfma(kf[0], phq_pos, o0); o1 = mfma(kf[64], phq_pos, o1);
    o0 = mfma(kf[128], phq_neg, o0); o1 = mfma(kf[192], phq_neg, o1);
    float t0, den = o1[0];
    x32(den, t0); x16(t0, den);
    const float inv = __builtin_amdgcn_rcpf(den + 1e-6f);
    u32x4 of = {pk(o0[0] * inv, o0[1] * inv), pk(o0[2] * inv, o0[3] * inv), g3 ? 0u : pk(o1[0] * inv, o1[1] * inv), g3 ? 0u : pk(o1[2] * inv, o1[3] * inv)};
    st16c(rws, of, lane * 16, WS_O + ((m * 8 + myMt) * 16 + h) * 1024);
    if (worker) w4_read(0, 5);
    WAITV0(); BARRIER();
    if (tid == 448) __hip_atomic_store((GAS u64*)(ws + WS_FL2) + m * 16 + h, tag, RLX);
    STAMP(7);
  }
  if (!worker) {
#pragma unroll 1
    for (int k = 0; k < 7; ++k) {
      const int r = (rr - 28) * 64 + lane;
      if (w < 7) touch(dD, (u32)((224 * (r >> 5) + 32 * k + (r & 31)) * 896 + w * 128));
    }
    WAITV0();
    return;
  }
#pragma unroll
  for (int i = 0; i < 5; ++i) w4_issue(5 + i, i);
  w4_sum(0, 5);
  f32x4 hA, hB;
  {
    if (w == 7) wait_flags((GAS u64*)(ws + WS_FL2) + (t64 >> 1) * 16, 16, tag, lane, dead);
    BARRIER();
    STAMP(8);
    if (b == 0 && tid == 448) __hip_atomic_store((GAS u64*)(ws + WS_EPOCH), tag, RLX);
    {
      const int rb = (w + c7) & 7;
#pragma unroll
      for (int i = 0; i < 8; ++i) {
        const int tl = i & 3, hd = 8 * (i >> 2) + rb;
        dma16c(dWS, 81920 + (16 * tl + hd) * 1024, lane * 16, WS_O + ((mt0 + (tl & 1) + 4 * (tl >> 1)) * 16 + hd) * 1024);
      }
    }
    hA = f32x4{bflo(xra[0]), bfhi(xra[0]), bflo(xra[1]), bfhi(xra[1])};
    hB = f32x4{bflo(xrb[0]), bfhi(xrb[0]), bflo(xrb[1]), bfhi(xrb[1])};
    LAS char* ap = smem + 81920 + mp * 32768 + lane * 16;
#pragma unroll
    for (int hf = 0; hf < 2; ++hf) {
      if (hf == 0) WAITV(4); else WAITV(0);
      RAWBAR();
      if (hf == 0) { STAMP(9); }
      u32x4 xa[8], xb2[8];
#pragma unroll
      for (int s = 0; s < 8; ++s) { xa[s] = *(LAS u32x4*)(ap + 1024 * (8 * hf + s)); xb2[s] = *(LAS u32x4*)(ap + 16384 + 1024 * (8 * hf + s)); }
      __builtin_amdgcn_sched_barrier(0);
#pragma unroll
      for (int s = 0; s < 8; ++s) { hA = mfma(w3f[8 * hf + s], xa[s], hA); hB = mfma(w3f[8 * hf + s], xb2[s], hB); }
      __builtin_amdgcn_sched_barrier(0);
    }
    const u32 a0 = pk(hA[0], hA[1]), a1 = pk(hA[2], hA[3]), b0 = pk(hB[0], hB[1]), b1 = pk(hB[2], hB[3]);
    u32x4 hv = pair16(a0, a1, b0, b1);
    st16c(rws, hv, (g & 1) * 14336 + (2 * (nt & 1) + (g >> 1)) * 256 + c16 * 16,
          WS_H + ((mt0 + 4 * mp) * 14 + 2 * cs + (nt >> 1)) * 1024);
    {
      float sA = gsum(dot2(a0, 0x3f803f80u, dot2(a1, 0x3f803f80u, 0.f))), qA = gsum(dot2(a0, a0, dot2(a1, a1, 0.f)));
      float sB = gsum(dot2(b0, 0x3f803f80u, dot2(b1, 0x3f803f80u, 0.f))), qB = gsum(dot2(b0, b0, dot2(b1, b1, 0.f)));
      if (g < 2) *(LAS u32x2*)(smem + L_PS + ((mp * 32 + g * 16 + c16) * 4 + nt) * 8) =
          g ? u32x2{__float_as_uint(sB), __float_as_uint(qB)} : u32x2{__float_as_uint(sA), __float_as_uint(qA)};
    }
    BARRIER();
    if (w == 7) {
      LAS f32x4* pp = (LAS f32x4*)(smem + L_PS + lane * 32);
      f32x4 u = pp[0], v = pp[1];
      u32x2 o2 = {__float_as_uint((u[0] + u[2]) + (v[0] + v[2])), __float_as_uint((u[1] + u[3]) + (v[1] + v[3]))};
      __builtin_amdgcn_raw_buffer_store_b64(o2, rws, WS_ST + (cs * 2048 + mt0 * 16 + (lane >> 5) * 64 + (lane & 31)) * 8, 0, 16);
    }
    w4_read(5, 5);
    WAITV0(); BARRIER();
    if (tid == 448) __hip_atomic_store((GAS u64*)(ws + WS_FL3) + (t64 >> 1) * 16 + (t64 & 1) * 7 + cs, tag, RLX);
    STAMP(10);
  }
  u32 v5[2];
#pragma unroll
  for (int v = 0; v < 2; ++v) v5[v] = (lane >> 3) * 896 + 32 * (((lane >> 1) & 3) ^ (((lane >> 4) & 1) | (v << 1))) + 16 * (lane & 1) + 128 * cs;
  const u32 b5 = 1024 * g + 128 * q + 8 * p;
  const u32 STG = 114688 + 4096 * w;
  u32x4 w5f[7][4];
  auto w5_issue = [&](int ks, u32 dst) {
#pragma unroll
    for (int i = 0; i < 4; ++i) dma16(dD, dst + i * 1024, v5[i & 1], (u32)(224 * w + 32 * ks + 8 * i) * 896);
  };
  auto w5_read = [&](u32x4* f, u32 src) {
#pragma unroll
    for (int n = 0; n < 4; ++n) { LAS char* a = smem + src + b5 + 32 * ((u32)n ^ phi_sw); f[n] = tr2(a, a + 512); }
  };
  auto w5_home = [&](int pair) { return w < 7 ? (u32)(28672 * pair + 4096 * w) : (u32)(L_MISC + 4096 * pair); };
#pragma unroll
  for (int i = 0; i < 4; ++i) w4_issue(10 + i, i);
  w4_sum(5, 5);
  {
    WAITV0(); BARRIER();
    w4_read(10, 4);
    w4_sum(10, 4);
    if (w == 7) wait_flags((GAS u64*)(ws + WS_FL3) + m4 * 16, 14, tag, lane, dead);
    BARRIER();
    STAMP(11);
    const int rb4 = (w + c7) & 7;
    auto hdma = [&](int j, int k) {
      int blk = 8 * k + rb4; if (blk >= 28) blk -= 8;
      dma16c(dWS, (28 * j + blk) * 1024, lane * 16, WS_H + (m4 * 112 + 28 * j + blk) * 1024);
    };
    {
      u32x2 s0, s1;
      {
        const u32 vo = WS_ST + (u32)(g * 2048 + m4 * 128 + w * 16 + c16) * 8;
        const u32 vo1 = g3 ? vo : vo + 4 * 2048 * 8;
        asm volatile("buffer_load_dwordx2 %0, %2, %4, 0 offen sc1\n\tbuffer_load_dwordx2 %1, %3, %4, 0 offen sc1"
                     : "=&v"(s0), "=&v"(s1) : "v"(vo), "v"(vo1), "s"(dWS) : "memory");
      }
#pragma unroll
      for (int k = 0; k < 8; ++k) hdma(k >> 2, k & 3);
      asm volatile("s_waitcnt vmcnt(8)" : "+v"(s0), "+v"(s1)::"memory");
      float sm = __uint_as_float(s0[0]) + (g3 ? 0.f : __uint_as_float(s1[0]));
      float sq = __uint_as_float(s0[1]) + (g3 ? 0.f : __uint_as_float(s1[1]));
      sm = gsum(sm); sq = gsum(sq);
      const float mean = sm * (1.f / D_);
      const float rstd = __builtin_amdgcn_rsqf(fmaxf(sq * (1.f / D_) - mean * mean, 0.f) + 1e-5f);
      if (g == 0) *(LAS u32x2*)(smem + L_ST + (w * 16 + c16) * 8) = u32x2{__float_as_uint(mean), __float_as_uint(rstd)};
    }
    WAITV(4); BARRIER();
    STAMP(12);
    const int ftile = 8 * fs + w;
    const u32 svo = (g & 1) * 57344 + (2 * (ftile & 1) + (g >> 1)) * 256 + c16 * 16 + WS_HH + (m4 * 8 * 56 + (ftile >> 1)) * 1024;
    LAS char* ap = smem + (g & 1) * 14336 + (g >> 1) * 256 + c16 * 16;
    LAS char* sp = smem + L_ST + (lane & 31) * 8;
    auto xoff = [](int n) { return 28672 * (n / 28) + 1024 * ((n % 28) >> 1) + 512 * (n & 1); };
    u32x4 ring[4];
#pragma unroll
    for (int k = 0; k < 4; ++k) ring[k] = *(LAS u32x4*)(ap + xoff(k));
    STR(0);
    auto rd = [&](int ks, int n) { LAS char* a = smem + STG + b5 + 32 * ((u32)n ^ phi_sw); w5f[ks][n] = tr2(a, a + 512); };
    auto is = [&](int ks, int i, u32 dst) { dma16(dD, dst + i * 1024, v5[i & 1], (u32)(224 * w + 32 * ks + 8 * i) * 896); };
    auto hook = [&](int T, int s) {
      if (T == 0) {
        if (s >= 1 && s < 9) hdma(2 + ((s - 1) >> 2), (s - 1) & 3);
        if (s >= 10 && s < 14) is(0, s - 10, STG);
        if (s == 20) { WAITV(12); RAWBAR(); }
      }
      if (T == 1) {
        if (s == 20) { WAITV(0); RAWBAR(); }
        if (s >= 21 && s < 25) { rd(0, s - 21); is(3, s - 21, w5_home(0)); }
      }
      if (T == 2) {
        if (s >= 2 && s < 6) is(1, s - 2, STG);
        if (s == 20) RAWBAR();
        if (s >= 21 && s < 25) is(4, s - 21, w5_home(1));
      }
      if (T == 3) {
        if (s == 4) { WAITV(5); RAWBAR(); }
        if (s >= 5 && s < 9) { rd(1, s - 5); is(5, s - 5, w5_home(2)); }
        if (s >= 14 && s < 18) is(2, s - 14, STG);
        if (s == 22) {
          WAITV(8); RAWBAR();
          if (tid == 448) __hip_atomic_store((GAS u64*)(ws + WS_FL4) + m4 * 16 + fs, tag, RLX);
        }
      }
    };
    f32x16 acc[2];
    u32x2 stt[2];
    float t[8], u[8], rn;
    auto epi = [&](const f32x16& E, u32x2 ST, int s, int tile) {
      const float mean = __uint_as_float(ST[0]), rstd = __uint_as_float(ST[1]);
      if (s == 0) rn = rstd * -1.4426950408889634f;
      if (s >= 1 && s <= 8) t[s - 1] = __builtin_amdgcn_exp2f(rn * fmaf(-mean, cw[s - 1], E[s - 1]));
      if (s >= 9 && s <= 16) { t[s - 9] = __builtin_amdgcn_rcpf(1.f + t[s - 9]); u[s - 9] = fmaf(-mean, cw[s - 1], E[s - 1]); }
      if (s >= 17 && s <= 24) u[s - 17] = (u[s - 17] * rstd) * t[s - 17];
      if (s == 26) {
        auto r0 = __builtin_amdgcn_permlane32_swap(pk(u[0], u[1]), pk(u[4], u[5]), false, false);
        auto r1 = __builtin_amdgcn_permlane32_swap(pk(u[2], u[3]), pk(u[6], u[7]), false, false);
        st16c(rws, u32x4{r0[0], r1[0], r0[1], r1[1]}, svo, tile * 2 * 56 * 1024);
      }
    };
#pragma unroll
    for (int T = 0; T < 4; ++T) {
      acc[T & 1] = f32x16{};
      stt[T & 1] = *(LAS u32x2*)(sp + 256 * T);
      __builtin_amdgcn_sched_barrier(0);
#pragma unroll
      for (int s = 0; s < 28; ++s) {
        const int n = 28 * T + s;
        acc[T & 1] = mfma32(w4f[s], ring[n & 3], acc[T & 1]);
        if (n + 4 < 112) ring[n & 3] = *(LAS u32x4*)(ap + xoff(n + 4));
        if (T > 0) epi(acc[(T - 1) & 1], stt[(T - 1) & 1], s, T - 1);
        hook(T, s);
        __builtin_amdgcn_sched_barrier(0);
      }
      STR(1 + T);
    }
#pragma unroll
    for (int s = 0; s < 27; ++s) epi(acc[1], stt[1], s, 3);
    STR(9);
  }
  {
    if (w == 7) wait_flags((GAS u64*)(ws + WS_FL4) + (t64 >> 1) * 16, 14, tag, lane, dead);
    else WAITV(6);
    w5_read(w5f[3], w5_home(0)); w5_read(w5f[4], w5_home(1)); w5_read(w5f[5], w5_home(2));
    WAITV(0);
    w5_read(w5f[2], STG);
    RAWBAR();
    if (tid == 448) __hip_atomic_store((GAS u64*)(ws + WS_FL5) + m4 * 16 + fs, tag, RLX);
    STAMP(13);
    {
      u32x4 xf[2][7];
      const int r2 = c7 & 1;
      const u32 home3 = w < 7 ? (u32)(86016 + 4096 * w) : (u32)L_MISC;
      if (w == 7) asm volatile("s_waitcnt lgkmcnt(0)" ::: "memory");
      w5_issue(6, home3);
      STR(10); STFLUSH2(11);
      STAMP(14);
      __builtin_amdgcn_sched_barrier(0);
#pragma unroll
      for (int k = 0; k < 14; ++k) xf[k / 7][k % 7] = ld16c(rws, lane * 16, WS_HH + ((mt0 + ((k / 7) ^ r2)) * 56 + 7 * w + k % 7) * 1024);
      __builtin_amdgcn_sched_barrier(0);
      WAITV(14);
      w5_read(w5f[6], home3);
      BARRIER();
      __builtin_amdgcn_sched_barrier(0);
#pragma unroll
      for (int i = 0; i < 4; ++i) {
        const int mt = (i & 2) | ((i & 1) ^ r2);
        f32x4 a[4] = {z4, z4, z4, z4};
#pragma unroll
        for (int s = 0; s < 7; ++s)
#pragma unroll
          for (int n = 0; n < 4; ++n) a[n] = mfma(w5f[s][n], xf[i & 1][s], a[n]);
        __builtin_amdgcn_sched_barrier(0);
        if (i == 0) {
          if (w == 7) wait_flags((GAS u64*)(ws + WS_FL5) + (t64 >> 1) * 16, 14, tag, lane, dead);
          BARRIER();
        }
        if (i < 2) {
#pragma unroll
          for (int s = 0; s < 7; ++s) xf[i & 1][s] = ld16c(rws, lane * 16, WS_HH + ((mt0 + 4 + (i ^ r2)) * 56 + 7 * w + s) * 1024);
        }
        __builtin_amdgcn_sched_barrier(0);
#pragma unroll
        for (int n = 0; n < 4; ++n) *(LAS f32x4*)(smem + (((n * 4 + mt) * 8 + w) * 64 + lane) * 16) = a[n];
      }
    }
    BARRIER();
    STAMP(15);
    LAS f32x4* rp = (LAS f32x4*)(smem + (w * 2 * 8 * 64 + lane) * 16);
#pragma unroll
    for (int i = 0; i < 8; ++i) { hA += rp[i * 64]; hB += rp[512 + i * 64]; }
    u32x4 ov = pair16(pk(hA[0], hA[1]), pk(hA[2], hA[3]), pk(hB[0], hB[1]), pk(hB[2], hB[3]));
    GAS char* op = (GAS char*)out + (((mt0 + 4 * mp + (g & 1)) * 16 + c16) * D_ + 64 * cs + 16 * nt + 8 * (g >> 1)) * 2;
    STAMP(16);
    __builtin_amdgcn_raw_buffer_store_b128(ov, mkrs(out, S_ * D_ * 2), (u32)(op - (GAS char*)out), 0, OUT_WT);
  }
  asm volatile(".Lcp_end:");
}

extern "C" void kernel_launch(void* const* d_in, const int* in_sizes, int n_in, void* d_out, int out_size, void* d_ws,
                              size_t ws_size, hipStream_t stream) {
  hipFuncSetAttribute((const void*)fused_block, hipFuncAttributeMaxDynamicSharedMemorySize, LDS_BYTES);
  fused_block<<<256, 512, LDS_BYTES, stream>>>((const uint16_t*)d_in[0], (const uint16_t*)d_in[1], (const uint16_t*)d_in[2],
                                               (const uint16_t*)d_in[3], (const uint16_t*)d_in[5], (const uint16_t*)d_in[6],
                                               (const uint16_t*)d_in[7], (uint16_t*)d_out, (char*)d_ws);
}
```

```cpp
#define PTHR 330
#define CODE_PULL
#include <hip/hip_runtime.h>
#include <stdint.h>

#define DEVI __device__ __forceinline__
#define LAS __attribute__((address_space(3)))
#define GAS __attribute__((address_space(1)))
typedef unsigned u32;
typedef unsigned long long u64;
typedef u32 u32x2 __attribute__((ext_vector_type(2)));
typedef u32 u32x4 __attribute__((ext_vector_type(4)));
typedef float f32x4 __attribute__((ext_vector_type(4)));
typedef float f32x16 __attribute__((ext_vector_type(16)));
typedef __bf16 bf16x8 __attribute__((ext_vector_type(8)));
typedef __bf16 bf16x2 __attribute__((ext_vector_type(2)));
typedef short s16x4 __attribute__((ext_vector_type(4)));
typedef __amdgpu_buffer_rsrc_t rsrc_t;

constexpr int S_ = 2048, D_ = 448, F_ = 1792;
constexpr u32 WS_EPOCH = 0;
constexpr u32 WS_FL1 = 4096, WS_FL2 = WS_FL1 + 16 * 128, WS_FL3 = WS_FL2 + 16 * 128, WS_FL4 = WS_FL3 + 16 * 128, WS_FL5 = WS_FL4 + 16 * 128;
constexpr u32 WS_KVP = 65536;
constexpr u32 WS_O = WS_KVP + 16 * 16 * 8192;
constexpr u32 WS_H = WS_O + 128 * 16 * 1024;
constexpr u32 WS_HH = WS_H + 128 * 14 * 1024;
constexpr u32 WS_ST = WS_HH + 128 * 56 * 1024;
constexpr u32 WS_END = WS_ST + 7 * 2048 * 8;
constexpr u32 WS_STAMP = WS_END;
constexpr u32 SLOT = 28672;
constexpr u32 L_RAW = 4 * SLOT;
constexpr u32 L_PHI = 0, L_V = 16384;
constexpr u32 L_MISC = 147456;
constexpr u32 L_KVF = L_MISC, L_WPHI = L_MISC + 4096, L_CS = L_MISC + 6144  , L_ST = L_MISC + 12288  , L_PS = L_MISC + 13312  , L_DUMMY = L_MISC + 15360;
constexpr u32 LDS_BYTES = 163840;

DEVI f32x4 mfma(u32x4 a, u32x4 b, f32x4 c) {
  return __builtin_amdgcn_mfma_f32_16x16x32_bf16(__builtin_bit_cast(bf16x8, a), __builtin_bit_cast(bf16x8, b), c, 0, 0, 0);
}
DEVI f32x16 mfma32(u32x4 a, u32x4 b, f32x16 c) {
  return __builtin_amdgcn_mfma_f32_32x32x16_bf16(__builtin_bit_cast(bf16x8, a), __builtin_bit_cast(bf16x8, b), c, 0, 0, 0);
}
DEVI float vmax(float a, float b) { return __builtin_elementwise_maximum(a, b); }
DEVI float vmin(float a, float b) { return __builtin_elementwise_minimum(a, b); }
DEVI u32 pk(float a, float b) { bf16x2 v = {(__bf16)a, (__bf16)b}; return __builtin_bit_cast(u32, v); }
DEVI float bflo(u32 v) { return __uint_as_float(v << 16); }
DEVI float bfhi(u32 v) { return __uint_as_float(v & 0xffff0000u); }
DEVI u32x2 trr(LAS char* p) { return __builtin_bit_cast(u32x2, __builtin_amdgcn_ds_read_tr16_b64_v4i16((LAS s16x4*)p)); }
DEVI u32x4 tr2(LAS char* p0, LAS char* p1) { u32x2 a = trr(p0), b = trr(p1); u32x4 r = {a[0], a[1], b[0], b[1]}; return r; }
DEVI float dot2(u32 a, u32 b, float c) {
  return __builtin_amdgcn_fdot2_f32_bf16(__builtin_bit_cast(bf16x2, a), __builtin_bit_cast(bf16x2, b), c, false);
}
DEVI float x16(float v, float& o) {
  auto r = __builtin_amdgcn_permlane16_swap(__float_as_uint(v), __float_as_uint(v), false, false);
  o = __uint_as_float(r[1]); return __uint_as_float(r[0]);
}
DEVI float x32(float v, float& o) {
  auto r = __builtin_amdgcn_permlane32_swap(__float_as_uint(v), __float_as_uint(v), false, false);
  o = __uint_as_float(r[1]); return __uint_as_float(r[0]);
}
DEVI float gsum(float v) { float b, a = x16(v, b); v = a + b; a = x32(v, b); return a + b; }
DEVI float gmax(float v) { float b, a = x16(v, b); v = vmax(a, b); a = x32(v, b); return vmax(a, b); }
DEVI float gmin(float v) { float b, a = x16(v, b); v = vmin(a, b); a = x32(v, b); return vmin(a, b); }
DEVI rsrc_t mkrs(const void* p, u32 bytes) { return __builtin_amdgcn_make_buffer_rsrc((void*)p, 0, bytes, 0x00020000); }
DEVI u32x4 mkd(const void* p, u32 bytes) {
  u64 a = (u64)p; u32x4 d = {(u32)a, (u32)(a >> 32) & 0xffffu, bytes, 0x00020000u}; return d;
}
DEVI void dma16(u32x4 d, u32 lds, u32 voff, u32 soff) {
  asm volatile("s_mov_b32 m0, %0\n\ts_nop 3\n\tbuffer_load_dwordx4 %1, %2, %3 offen lds" ::"s"(lds), "v"(voff), "s"(d), "s"(soff) : "memory", "m0");
}
DEVI void dma16c(u32x4 d, u32 lds, u32 voff, u32 soff) {
  asm volatile("s_mov_b32 m0, %0\n\ts_nop 3\n\tbuffer_load_dwordx4 %1, %2, %3 offen sc1 lds" ::"s"(lds), "v"(voff), "s"(d), "s"(soff) : "memory", "m0");
}
DEVI void touch(u32x4 d, u32 voff) {
  asm volatile("s_mov_b32 m0, %0\n\ts_nop 3\n\tbuffer_load_dword %1, %2, 0 offen lds" ::"s"(L_DUMMY), "v"(voff), "s"(d) : "memory", "m0");
}
#define RAWBAR() do { asm volatile("" ::: "memory"); __builtin_amdgcn_s_barrier(); asm volatile("" ::: "memory"); } while (0)
#define WAITV(n) asm volatile("s_waitcnt vmcnt(" #n ")" ::: "memory")
DEVI u32x4 ld16(rsrc_t rs, u32 voff, u32 soff) { return __builtin_amdgcn_raw_buffer_load_b128(rs, voff, soff, 0); }
DEVI u32x4 ld16c(rsrc_t rs, u32 voff, u32 soff) { return __builtin_amdgcn_raw_buffer_load_b128(rs, voff, soff, 16); }
DEVI void st16c(rsrc_t rs, u32x4 v, u32 voff, u32 soff) { __builtin_amdgcn_raw_buffer_store_b128(v, rs, voff + soff, 0, 16); }
#define WAITV0() asm volatile("s_waitcnt vmcnt(0)" ::: "memory")
#define BARRIER() do { asm volatile("s_waitcnt lgkmcnt(0)" ::: "memory"); __builtin_amdgcn_s_barrier(); asm volatile("" ::: "memory"); } while (0)
#define RLX __ATOMIC_RELAXED, __HIP_MEMORY_SCOPE_AGENT

#define STAMP(i)
#define STR(i) (void)tr_
#define STFLUSH(n)
#define STFLUSH2(n)

DEVI void wait_flags(GAS u64* fl, int n, u64 tag, int lane, int& dead) {
  int ln = lane < n ? lane : 0;
  for (u32 spins = 0; !dead; ++spins) {
    u64 v = __hip_atomic_load(fl + ln, RLX);
    if (__all(v == tag)) break;
    if (spins > (1u << 17)) dead = 1;
    __builtin_amdgcn_s_sleep(1);
  }
}

template <class HK>
DEVI void dual_softmax(const f32x4& p0, f32x4 p1, bool g3, u32x4& pos, u32x4& neg, HK hk) {
  const float L2E = 1.4426950408889634f;
  if (g3) p1 = p0;
  float mx = vmax(vmax(vmax(p0[0], p0[1]), vmax(p0[2], p0[3])), vmax(vmax(p1[0], p1[1]), vmax(p1[2], p1[3])));
  float mn = vmin(vmin(vmin(p0[0], p0[1]), vmin(p0[2], p0[3])), vmin(vmin(p1[0], p1[1]), vmin(p1[2], p1[3])));
  mx = gmax(mx); mn = gmin(mn);
  hk(0);
  const float ca = -mx * L2E, cb = mn * L2E;
  const float ca1 = g3 ? -__builtin_inff() : ca, cb1 = g3 ? -__builtin_inff() : cb;
  float a0[4], a1[4], b0[4], b1[4];
#pragma unroll
  for (int r = 0; r < 4; ++r) {
    a0[r] = __builtin_amdgcn_exp2f(fmaf(p0[r], L2E, ca));
    b0[r] = __builtin_amdgcn_exp2f(fmaf(p0[r], -L2E, cb));
    a1[r] = __builtin_amdgcn_exp2f(fmaf(p1[r], L2E, ca1));
    b1[r] = __builtin_amdgcn_exp2f(fmaf(p1[r], -L2E, cb1));
  }
  float sa = ((a0[0] + a0[1]) + (a0[2] + a0[3])) + ((a1[0] + a1[1]) + (a1[2] + a1[3]));
  float sb = ((b0[0] + b0[1]) + (b0[2] + b0[3])) + ((b1[0] + b1[1]) + (b1[2] + b1[3]));
  hk(1);
  sa = __builtin_amdgcn_rcpf(gsum(sa)); sb = __builtin_amdgcn_rcpf(gsum(sb));
  hk(2);
  pos[0] = pk(a0[0] * sa, a0[1] * sa); pos[1] = pk(a0[2] * sa, a0[3] * sa);
  pos[2] = pk(a1[0] * sa, a1[1] * sa); pos[3] = pk(a1[2] * sa, a1[3] * sa);
  neg[0] = pk(b0[0] * sb, b0[1] * sb); neg[1] = pk(b0[2] * sb, b0[3] * sb);
  neg[2] = pk(b1[0] * sb, b1[1] * sb); neg[3] = pk(b1[2] * sb, b1[3] * sb);
}

DEVI u32x4 pair16(u32 a0, u32 a1, u32 b0, u32 b1) {
  auto r0 = __builtin_amdgcn_permlane16_swap(a0, b0, false, false);
  auto r1 = __builtin_amdgcn_permlane16_swap(a1, b1, false, false);
  u32x4 o = {r0[0], r1[0], r0[1], r1[1]};
  return o;
}

__global__ __launch_bounds__(512, 2) void fused_block(const uint16_t* __restrict__ x, const uint16_t* __restrict__ wqkv,
                                                      const uint16_t* __restrict__ wphi, const uint16_t* __restrict__ wo,
                                                      const uint16_t* __restrict__ wg, const uint16_t* __restrict__ wu,
                                                      const uint16_t* __restrict__ wd, uint16_t* __restrict__ out,
                                                      char* __restrict__ ws_) {
  extern __shared__ __attribute__((aligned(16))) char smem_[];
  LAS char* smem = (LAS char*)smem_;
  u32 tr_[12];
  u64 pt0_ = 0, pt1_ = 0;
  STR(0);
  asm volatile("" ::"s"(x), "s"(wqkv), "s"(wphi), "s"(wo), "s"(wg), "s"(wu), "s"(wd), "s"(out), "s"(ws_));
  GAS char* ws = (GAS char*)ws_;
  const int tid = threadIdx.x, lane = tid & 63;
  const int w = __builtin_amdgcn_readfirstlane(tid >> 6);
  const int g = lane >> 4, c16 = lane & 15, q = (lane >> 2) & 3, p = lane & 3;
  const bool g3 = (g == 3);
  const int b = blockIdx.x, xc = b & 7, rr = b >> 3;
  const rsrc_t rws = mkrs(ws_, WS_END);
  int dead = 0;
  STAMP(0);
  const u32x4 ones = {0x3f803f80u, 0x3f803f80u, 0x3f803f80u, 0x3f803f80u};
  const f32x4 z4 = {0.f, 0.f, 0.f, 0.f};
  const u32 phi_sw = (q >> 1) | ((g & 1) << 1);
  const bool worker = b < 224;
  const int h = 4 * (xc & 3) + (rr & 3), m = 8 * (xc >> 2) + (rr >> 2);
  const int r7 = worker ? rr / 7 : 0, c7 = worker ? rr % 7 : 0;
  const int t64 = 4 * xc + r7, cs = c7;
  const int mt0 = (t64 >> 1) * 8 + 2 * (t64 & 1);
  const int m4 = 4 * (xc >> 1) + r7, fs = 7 * (xc & 1) + c7;
  const int mp = w & 1, nt = w >> 1;
  const u32x4 dX = mkd(x, S_ * D_ * 2), dW1 = mkd(wqkv, D_ * 3 * D_ * 2), dW3 = mkd(wo, D_ * D_ * 2);
  const u32x4 dG = mkd(wg, D_ * F_ * 2), dU = mkd(wu, D_ * F_ * 2), dD = mkd(wd, F_ * D_ * 2), dWS = mkd(ws_, WS_END);

  u64 tag;
  u32x2 xra, xrb;
  u32 v4[2];
#pragma unroll
  for (int v = 0; v < 2; ++v) v4[v] = (lane >> 4) * 3584 + 32 * (((lane >> 1) & 7) ^ ((lane >> 4) | (v << 2))) + 16 * (lane & 1) + 256 * fs;
  const u32 b4 = 8192 * (g & 1) + 2048 * (g >> 1) + 256 * q + 32 * ((u32)w ^ (q | ((g & 1) << 2))) + 8 * p;
  const int rw4 = (w + 2 * r7) & 7;
  u32x4 w4f[28];
  f32x16 cw = {};
  auto w4_issue = [&](int u, int slot) {
    dma16(dG, slot * 16384 + rw4 * 1024, v4[0], (u32)(8 * u + rw4) * 4 * 3584);
    dma16(dU, slot * 16384 + 8192 + rw4 * 1024, v4[1], (u32)(8 * u + rw4) * 4 * 3584);
  };
  auto w4_read = [&](int u0, int n) {
#pragma unroll
    for (int k = 0; k < 2 * n; ++k) { LAS char* a = smem + b4 + 16384 * (k >> 1) + 4096 * (k & 1); w4f[2 * u0 + k] = tr2(a, a + 1024); }
#pragma unroll
    for (int k = 0; k < 2 * n; ++k) cw = mfma32(w4f[2 * u0 + k], ones, cw);
  };

  u32x4 phq_pos, phq_neg;
  u32x4 w3f[16];
  const int grp = w >> 2, tg = w & 3;
  const int myMt = 2 * tg + grp;
  {
    const int rot = (2 * (rr & 3) + (rr >> 2)) % 7;
    const u32 win0 = (56 * h) & ~15;
    u32 vx0, vx1, vw[2], v3;
    {
      const int r8 = lane >> 3, pos = lane & 7;
      vx0 = (u32)((m * 128 + w * 16 + r8) * 896 + 16 * (pos ^ (lane >> 4)));
      vx1 = (u32)((m * 128 + w * 16 + 8 + r8) * 896 + 16 * (pos ^ (4 + (lane >> 4))));
#pragma unroll
      for (int k = 0; k < 2; ++k) {
        const int j = w + 8 * k, G = j / 3, rl = 4 * (j - 3 * G) + g, rq = rl / 3, sec = rl - 3 * rq;
        vw[k] = (u32)((16 * G + 4 * rq + q) * 2688 + sec * 896 + win0 + 16 * (p ^ (2 * (rq >> 1))));
      }
      v3 = (u32)(r8 * 896 + 32 * (((lane >> 1) & 3) ^ ((lane >> 4) & 3)) + 16 * (lane & 1) + 128 * cs);
    }
    auto issue_chunk = [&](int i, u32 slot) {
      int c = rot + i; c = c >= 7 ? c - 7 : c;
      dma16(dX, slot + w * 2048, vx0, c * 128);
      dma16(dW1, slot + 16384 + w * 1024, vw[0], c * 64 * 2688);
      dma16(dX, slot + w * 2048 + 1024, vx1, c * 128);
      if (w < 4) dma16(dW1, slot + 24576 + w * 1024, vw[1], c * 64 * 2688);
    };
    auto w3_at = [&](int hd) { return (u32)(hd < 7 ? 28672 + 4096 * hd : hd < 14 ? 86016 + 4096 * (hd - 7) : 57344 + 4096 * (hd - 14)); };
    auto ex = [&](int k) {
      const int hd = 2 * k + (w >> 2);
      __builtin_amdgcn_sched_barrier(0);
      dma16(dW3, (w < 4 ? w3_at(2 * k) : w3_at(2 * k + 1)) + (w & 3) * 1024, v3, (28 * hd + 8 * (w & 3)) * 896);
      if (k < 7 && w == k) {
        const int l2 = 56 * k + (lane < 56 ? lane : 0), up = l2 >= 196;
        const int idx = rr * 196 + l2 - 196 * up;
        const u32 vo = (u32)((idx / 14) * 3584 + (xc & 1) * 1792 + (idx % 14) * 128);
        if (k <= 3) touch(dG, up ? 0x40000000u : vo);
        if (k >= 3) touch(dU, up ? vo : 0x40000000u);
      }
      __builtin_amdgcn_sched_barrier(0);
    };
    if (w == 7) {
      const u32x4 dP = mkd(wphi, 16 * 784 * 2);
      dma16(dP, L_WPHI, lane * 16, h * 1568); dma16(dP, L_WPHI + 1024, lane * 16, h * 1568 + 1024);
    }
    issue_chunk(0, 0); RAWBAR();
    issue_chunk(1, SLOT);
    {
      const GAS char* ep = ws + WS_EPOCH;
      const GAS char* xp = (const GAS char*)x + (((mt0 + 4 * mp) * 16 + c16) * D_ + 64 * cs + 16 * nt + 4 * g) * 2;
      asm volatile("global_load_dwordx2 %0, %3, off sc1\n\tglobal_load_dwordx2 %1, %4, off\n\tglobal_load_dwordx2 %2, %5, off"
                   : "=&v"(tag), "=&v"(xra), "=&v"(xrb) : "v"(ep), "v"(xp), "v"(xp + 16 * D_ * 2) : "memory");
    }
    if (w == 6) {
      u64 pc; u32 len;
      asm volatile("s_getpc_b64 %0\n.Lcp_start:\n\ts_mov_b32 %1, .Lcp_end-.Lcp_start" : "=s"(pc), "=s"(len));
      const u32x4 dC = {(u32)pc, (u32)(pc >> 32) & 0xffffu, len, 0x00020000u};
      touch(dC, (u32)(((rr & 7) * 64 + lane) * 128));
    }
    STR(1);
    u32 wb[3];
#pragma unroll
    for (int dt = 0; dt < 2; ++dt) {
      u32 u = 8 * (h & 1) + 32 * dt + 8 * p; if (u >= 64) u = 56;
      wb[dt] = 16384 + 1536 * g + 64 * q + (u ^ (32 * (g & 1)));
    }
    wb[2] = grp ? wb[1] : wb[0];
    wb[0] += 256 * (1 + grp); wb[1] += 256 * (1 + grp);
    const u32 xb = 128 * c16 + 16 * (g ^ ((c16 >> 1) & 7)) + myMt * 2048;
    u32x4 wpf[2];
    f32x4 A[3] = {z4, z4, z4}, B[3] = {z4, z4, z4}, C = z4;
    f32x4 S1 = z4, S2 = z4;
    u32x4 x0[2][2], x1[2][2], wf[2][2][3];
#pragma unroll
    for (int i = 0; i < 8; ++i) {
      if (i < 7) {
        if (i < 5) WAITV(6); else if (i == 5) WAITV(3); else WAITV(0);
        BARRIER();
        STR(2 + i);
      }
      if (i == 0 && grp) issue_chunk(2, 2 * SLOT);
      if (i == 1) issue_chunk(3, 3 * SLOT);
      if (i >= 1 && i <= 3 && grp) issue_chunk(i + 3, ((i + 3) & 3) * SLOT);
      if (i == 6 && grp) { ex(0); ex(1); }
      __builtin_amdgcn_sched_barrier(0);
      if (i < 7) {
        LAS char* sl = smem + (i & 3) * SLOT;
#pragma unroll
        for (int e = 0; e < 2; ++e) {
          x0[i & 1][e] = *(LAS u32x4*)(sl + (xb ^ (64 * e)));
#pragma unroll
          for (int n = 0; n < 3; ++n) wf[i & 1][e][n] = tr2(sl + wb[n] + 6144 * e, sl + wb[n] + 6144 * e + 768);
          x1[i & 1][e] = *(LAS u32x4*)(sl + (xb ^ (64 * e) ^ 2048));
        }
        __builtin_amdgcn_sched_barrier(0);
      }
      if (i > 0) {
        const int c = (i - 1) & 1;
#pragma unroll
        for (int e = 0; e < 2; ++e) {
#pragma unroll
          for (int n = 0; n < 3; ++n) { A[n] = mfma(wf[c][e][n], x0[c][e], A[n]); B[n] = mfma(wf[c][e][n], x1[c][e], B[n]); }
          S1 = mfma(ones, x0[c][e], S1);
          S2 = mfma(x0[c][e], x0[c][e], S2);
        }
        asm volatile("" : "+v"(A[0]), "+v"(A[1]), "+v"(A[2]), "+v"(B[0]), "+v"(B[1]), "+v"(B[2]), "+v"(S1), "+v"(S2));
#pragma unroll
        for (int n = 0; n < 3; ++n)
          if (tg == n) { C = mfma(wf[c][0][n], ones, C); C = mfma(wf[c][1][n], ones, C); }
      }
      __builtin_amdgcn_sched_barrier(0);
      if (i == 0 && !grp) issue_chunk(2, 2 * SLOT);
      if (i >= 1 && i <= 3 && !grp) issue_chunk(i + 3, ((i + 3) & 3) * SLOT);
      if (i == 6 && !grp) { ex(0); ex(1); }
      __builtin_amdgcn_sched_barrier(0);
      if (i == 0) {
        LAS char* a = smem + L_WPHI + 56 * (4 * g + q) + 8 * p;
        wpf[0] = tr2(a, a + 896); wpf[1] = tr2(a + 32, a + 32 + 896);
        asm volatile("" : "+v"(wpf[0]), "+v"(wpf[1]));
        const u32 mg = g3 ? 0u : ~0u, mf = c16 >= 12 ? 0u : ~0u;
        wpf[0][2] &= mg; wpf[0][3] &= mg; wpf[1][0] &= mf; wpf[1][1] &= mf; wpf[1][2] &= mg & mf; wpf[1][3] &= mg & mf;
        asm volatile("" : "+v"(wpf[0]), "+v"(wpf[1]));
      }
      if (i == 4) asm volatile("" : "+v"(tag), "+v"(xra), "+v"(xrb));
    }
    tag += 1;
    {
      LAS f32x4* rp = (LAS f32x4*)(smem + L_RAW + w * 3072) + lane;
      rp[0] = B[0]; rp[64] = B[1]; rp[128] = B[2];
      if (tg < 3 && c16 == 0) *(LAS f32x4*)(smem + L_CS + (tg < 2 ? 2 * grp + tg : 4 + grp) * 64 + 16 * g) = C;
    }
    ex(2);
    const float mean = S1[0] * (1.f / D_);
    const float dg = p == 0 ? S2[0] : p == 1 ? S2[1] : p == 2 ? S2[2] : S2[3];
    const float rstd = __builtin_amdgcn_rsqf(fmaxf(gsum(g == q ? dg : 0.f) * (1.f / D_) - mean * mean, 0.f) + 1e-5f);
    BARRIER();
    STR(9);
    STAMP(1); STAMP(2);
    LAS f32x4* cp = (LAS f32x4*)(smem + L_CS + 16 * g);
    f32x4 r2;
    {
      LAS f32x4* rp = (LAS f32x4*)(smem + L_RAW + (w ^ 4) * 3072) + lane;
      f32x4 r0 = rp[0], r1 = rp[64];
      r2 = rp[128];
      ex(3);
      f32x4 acc[4] = {grp ? r0 : A[0], grp ? r1 : A[1], grp ? A[0] : r0, grp ? A[1] : r1};
#pragma unroll
      for (int n = 0; n < 4; ++n) {
        const f32x4 c = cp[4 * n];
#pragma unroll
        for (int r = 0; r < 4; ++r) acc[n][r] = rstd * (acc[n][r] - mean * c[r]);
      }
      u32x4 kf = {pk(acc[0][0], acc[0][1]), pk(acc[0][2], acc[0][3]), g3 ? 0u : pk(acc[1][0], acc[1][1]), g3 ? 0u : pk(acc[1][2], acc[1][3])};
      f32x4 p0 = mfma(wpf[0], kf, z4), p1 = mfma(wpf[1], kf, z4);
      ex(4);
      LAS char* vr = smem + L_V + 64 * (16 * myMt + c16) + 8 * g;
      const u32 vs = (c16 >> 3) & 1;
      *(LAS u32x2*)(vr + 32 * (0 ^ vs)) = u32x2{pk(acc[2][0], acc[2][1]), pk(acc[2][2], acc[2][3])};
      *(LAS u32x2*)(vr + 32 * (1 ^ vs)) = g3 ? u32x2{0x00003f80u, 0u} : u32x2{pk(acc[3][0], acc[3][1]), pk(acc[3][2], acc[3][3])};
      u32x4 pos, neg;
      dual_softmax(p0, p1, g3, pos, neg, [&](int k) { ex(5 + k); });
      const u32 rowsw = ((c16 >> 1) & 1) | (((c16 >> 3) & 1) << 1);
      LAS char* pr = smem + L_PHI + 128 * (16 * myMt + c16) + 8 * g;
      *(LAS u32x2*)(pr + 32 * (0 ^ rowsw)) = u32x2{pos[0], pos[1]};
      *(LAS u32x2*)(pr + 32 * (1 ^ rowsw)) = u32x2{pos[2], pos[3]};
      *(LAS u32x2*)(pr + 32 * (2 ^ rowsw)) = u32x2{neg[0], neg[1]};
      *(LAS u32x2*)(pr + 32 * (3 ^ rowsw)) = u32x2{neg[2], neg[3]};
    }
    BARRIER();
    STR(10);
    if (w >= 4) {
      LAS char* ap = smem + L_PHI + 1024 * g + 128 * q + 32 * ((u32)(w - 4) ^ phi_sw) + 8 * p;
      LAS char* bp0 = smem + L_V + 512 * g + 64 * q + 32 * (g & 1) + 8 * p;
      LAS char* bp1 = smem + L_V + 512 * g + 64 * q + 32 * (1 ^ (g & 1)) + 8 * p;
      f32x4 kv0 = z4, kv1 = z4;
#pragma unroll
      for (int s = 0; s < 4; ++s) {
        u32x4 a = tr2(ap + 4096 * s, ap + 4096 * s + 512);
        kv0 = mfma(a, tr2(bp0 + 2048 * s, bp0 + 2048 * s + 256), kv0);
        kv1 = mfma(a, tr2(bp1 + 2048 * s, bp1 + 2048 * s + 256), kv1);
      }
      u32x4 kb = {pk(kv0[0], kv0[1]), pk(kv0[2], kv0[3]), pk(kv1[0], kv1[1]), pk(kv1[2], kv1[3])};
      st16c(rws, kb, lane * 16, WS_KVP + ((h * 16 + m) * 4 + (w - 4)) * 1024);
    }
    WAITV0();
    BARRIER();
    if (tid == 448) __hip_atomic_store((GAS u64*)(ws + WS_FL1) + h * 16 + m, tag, RLX);
    STR(11); STFLUSH(12);
    STAMP(3);
    {
      LAS char* bp = smem + 128 * (4 * g + q) + 32 * ((u32)nt ^ phi_sw) + 8 * p;
#pragma unroll
      for (int s = 0; s < 16; ++s) w3f[s] = tr2(bp + w3_at(s), bp + w3_at(s) + 2048);
    }
    BARRIER();
    {
      auto gd = [&](int u) {
        __builtin_amdgcn_sched_barrier(0);
        if (worker) w4_issue(u, u);
        __builtin_amdgcn_sched_barrier(0);
      };
      gd(0);
      f32x4 q0 = grp ? r2 : A[2], q1 = grp ? A[2] : r2;
      const f32x4 c0 = cp[16], c1 = cp[20];
#pragma unroll
      for (int r = 0; r < 4; ++r) { q0[r] = rstd * (q0[r] - mean * c0[r]); q1[r] = rstd * (q1[r] - mean * c1[r]); }
      u32x4 qf = {pk(q0[0], q0[1]), pk(q0[2], q0[3]), g3 ? 0u : pk(q1[0], q1[1]), g3 ? 0u : pk(q1[2], q1[3])};
      f32x4 p0 = mfma(wpf[0], qf, z4), p1 = mfma(wpf[1], qf, z4);
      gd(1);
      dual_softmax(p0, p1, g3, phq_pos, phq_neg, [&](int k) { gd(2 + k); });
    }
  }
  STAMP(4);
  {
    if (w == 7) wait_flags((GAS u64*)(ws + WS_FL1) + h * 16, 16, tag, lane, dead);
    BARRIER();
    STAMP(5);
    if (w < 4) {
      u32x4 pt[16];
#pragma unroll
      for (int i = 0; i < 16; ++i) pt[i] = ld16c(rws, lane * 16, WS_KVP + ((h * 16 + ((i + 2 * m) & 15)) * 4 + w) * 1024);
      float kv[8];
#pragma unroll
      for (int k = 0; k < 4; ++k) { kv[2 * k] = bflo(pt[0][k]); kv[2 * k + 1] = bfhi(pt[0][k]); }
#pragma unroll
      for (int i = 1; i < 16; ++i)
#pragma unroll
        for (int k = 0; k < 4; ++k) { kv[2 * k] += bflo(pt[i][k]); kv[2 * k + 1] += bfhi(pt[i][k]); }
      LAS char* kp = smem + L_KVF + ((w >> 1) * 128 + lane) * 16 + 8 * (w & 1);
      *(LAS u32x2*)kp = u32x2{pk(kv[0], kv[1]), pk(kv[2], kv[3])};
      *(LAS u32x2*)(kp + 1024) = u32x2{pk(kv[4], kv[5]), pk(kv[6], kv[7])};
    }
    WAITV0(); BARRIER();
    STAMP(6);
    f32x4 o0 = z4, o1 = z4;
    LAS u32x4* kf = (LAS u32x4*)(smem + L_KVF) + lane;
    o0 = mfma(kf[0], phq_pos, o0); o1 = mfma(kf[64], phq_pos, o1);
    o0 = mfma(kf[128], phq_neg, o0); o1 = mfma(kf[192], phq_neg, o1);
    float t0, den = o1[0];
    x32(den, t0); x16(t0, den);
    const float inv = __builtin_amdgcn_rcpf(den + 1e-6f);
    u32x4 of = {pk(o0[0] * inv, o0[1] * inv), pk(o0[2] * inv, o0[3] * inv), g3 ? 0u : pk(o1[0] * inv, o1[1] * inv), g3 ? 0u : pk(o1[2] * inv, o1[3] * inv)};
    st16c(rws, of, lane * 16, WS_O + ((m * 8 + myMt) * 16 + h) * 1024);
    if (worker) w4_read(0, 5);
    WAITV0(); BARRIER();
    if (tid == 448) __hip_atomic_store((GAS u64*)(ws + WS_FL2) + m * 16 + h, tag, RLX);
    STAMP(7);
  }
  if (!worker) {
#pragma unroll 1
    for (int k = 0; k < 7; ++k) {
      const int r = (rr - 28) * 64 + lane;
      if (w < 7) touch(dD, (u32)((224 * (r >> 5) + 32 * k + (r & 31)) * 896 + w * 128));
    }
    WAITV0();
    return;
  }
#pragma unroll
  for (int i = 0; i < 5; ++i) w4_issue(5 + i, i);
  f32x4 hA, hB;
  {
    if (w == 7) wait_flags((GAS u64*)(ws + WS_FL2) + (t64 >> 1) * 16, 16, tag, lane, dead);
    BARRIER();
    STAMP(8);
    if (b == 0 && tid == 448) __hip_atomic_store((GAS u64*)(ws + WS_EPOCH), tag, RLX);
    {
      const int rb = (w + c7) & 7;
#pragma unroll
      for (int i = 0; i < 8; ++i) dma16c(dWS, 81920 + (8 * i + rb) * 1024, lane * 16, WS_O + (mt0 * 16 + (i >> 2) * 64 + 8 * (i & 3) + rb) * 1024);
    }
    WAITV0(); BARRIER();
    STAMP(9);
    hA = f32x4{bflo(xra[0]), bfhi(xra[0]), bflo(xra[1]), bfhi(xra[1])};
    hB = f32x4{bflo(xrb[0]), bfhi(xrb[0]), bflo(xrb[1]), bfhi(xrb[1])};
    LAS char* ap = smem + 81920 + mp * 32768 + lane * 16;
#pragma unroll
    for (int s = 0; s < 16; ++s) {
      hA = mfma(w3f[s], *(LAS u32x4*)(ap + 1024 * s), hA);
      hB = mfma(w3f[s], *(LAS u32x4*)(ap + 16384 + 1024 * s), hB);
    }
    const u32 a0 = pk(hA[0], hA[1]), a1 = pk(hA[2], hA[3]), b0 = pk(hB[0], hB[1]), b1 = pk(hB[2], hB[3]);
    u32x4 hv = pair16(a0, a1, b0, b1);
    st16c(rws, hv, (g & 1) * 14336 + (2 * (nt & 1) + (g >> 1)) * 256 + c16 * 16,
          WS_H + ((mt0 + 4 * mp) * 14 + 2 * cs + (nt >> 1)) * 1024);
    {
      float sA = gsum(dot2(a0, 0x3f803f80u, dot2(a1, 0x3f803f80u, 0.f))), qA = gsum(dot2(a0, a0, dot2(a1, a1, 0.f)));
      float sB = gsum(dot2(b0, 0x3f803f80u, dot2(b1, 0x3f803f80u, 0.f))), qB = gsum(dot2(b0, b0, dot2(b1, b1, 0.f)));
      if (g < 2) *(LAS u32x2*)(smem + L_PS + ((mp * 32 + g * 16 + c16) * 4 + nt) * 8) =
          g ? u32x2{__float_as_uint(sB), __float_as_uint(qB)} : u32x2{__float_as_uint(sA), __float_as_uint(qA)};
    }
    BARRIER();
    if (w == 7) {
      LAS f32x4* pp = (LAS f32x4*)(smem + L_PS + lane * 32);
      f32x4 u = pp[0], v = pp[1];
      u32x2 o2 = {__float_as_uint((u[0] + u[2]) + (v[0] + v[2])), __float_as_uint((u[1] + u[3]) + (v[1] + v[3]))};
      __builtin_amdgcn_raw_buffer_store_b64(o2, rws, WS_ST + (cs * 2048 + mt0 * 16 + (lane >> 5) * 64 + (lane & 31)) * 8, 0, 16);
    }
    w4_read(5, 5);
    WAITV0(); BARRIER();
    if (tid == 448) __hip_atomic_store((GAS u64*)(ws + WS_FL3) + (t64 >> 1) * 16 + (t64 & 1) * 7 + cs, tag, RLX);
    STAMP(10);
  }
  u32 v5[2];
#pragma unroll
  for (int v = 0; v < 2; ++v) v5[v] = (lane >> 3) * 896 + 32 * (((lane >> 1) & 3) ^ (((lane >> 4) & 1) | (v << 1))) + 16 * (lane & 1) + 128 * cs;
  const u32 b5 = 1024 * g + 128 * q + 8 * p;
  const u32 STG = 114688 + 4096 * w;
  u32x4 w5f[7][4];
  auto w5_issue = [&](int ks, u32 dst) {
#pragma unroll
    for (int i = 0; i < 4; ++i) dma16(dD, dst + i * 1024, v5[i & 1], (u32)(224 * w + 32 * ks + 8 * i) * 896);
  };
  auto w5_read = [&](u32x4* f, u32 src) {
#pragma unroll
    for (int n = 0; n < 4; ++n) { LAS char* a = smem + src + b5 + 32 * ((u32)n ^ phi_sw); f[n] = tr2(a, a + 512); }
  };
  auto w5_home = [&](int pair) { return w < 7 ? (u32)(28672 * pair + 4096 * w) : (u32)(L_MISC + 4096 * pair); };
#pragma unroll
  for (int i = 0; i < 4; ++i) w4_issue(10 + i, i);
  {
    WAITV0(); BARRIER();
    w4_read(10, 4);
    if (w == 7) wait_flags((GAS u64*)(ws + WS_FL3) + m4 * 16, 14, tag, lane, dead);
    BARRIER();
    STAMP(11);
    const int rb4 = (w + c7) & 7;
    auto hdma = [&](int j, int k) {
      int blk = 8 * k + rb4; if (blk >= 28) blk -= 8;
      dma16c(dWS, (28 * j + blk) * 1024, lane * 16, WS_H + (m4 * 112 + 28 * j + blk) * 1024);
    };
    {
      u32x2 s0, s1;
      {
        const u32 vo = WS_ST + (u32)(g * 2048 + m4 * 128 + w * 16 + c16) * 8;
        const u32 vo1 = g3 ? vo : vo + 4 * 2048 * 8;
        asm volatile("buffer_load_dwordx2 %0, %2, %4, 0 offen sc1\n\tbuffer_load_dwordx2 %1, %3, %4, 0 offen sc1"
                     : "=&v"(s0), "=&v"(s1) : "v"(vo), "v"(vo1), "s"(dWS) : "memory");
      }
#pragma unroll
      for (int k = 0; k < 8; ++k) hdma(k >> 2, k & 3);
      asm volatile("s_waitcnt vmcnt(8)" : "+v"(s0), "+v"(s1)::"memory");
      float sm = __uint_as_float(s0[0]) + (g3 ? 0.f : __uint_as_float(s1[0]));
      float sq = __uint_as_float(s0[1]) + (g3 ? 0.f : __uint_as_float(s1[1]));
      sm = gsum(sm); sq = gsum(sq);
      const float mean = sm * (1.f / D_);
      const float rstd = __builtin_amdgcn_rsqf(fmaxf(sq * (1.f / D_) - mean * mean, 0.f) + 1e-5f);
      if (g == 0) *(LAS u32x2*)(smem + L_ST + (w * 16 + c16) * 8) = u32x2{__float_as_uint(mean), __float_as_uint(rstd)};
    }
    WAITV(4); BARRIER();
    STAMP(12);
    const int ftile = 8 * fs + w;
    const u32 svo = (g & 1) * 57344 + (2 * (ftile & 1) + (g >> 1)) * 256 + c16 * 16 + WS_HH + (m4 * 8 * 56 + (ftile >> 1)) * 1024;
    LAS char* ap = smem + (g & 1) * 14336 + (g >> 1) * 256 + c16 * 16;
    LAS char* sp = smem + L_ST + (lane & 31) * 8;
    auto xoff = [](int n) { return 28672 * (n / 28) + 1024 * ((n % 28) >> 1) + 512 * (n & 1); };
    u32x4 ring[4];
#pragma unroll
    for (int k = 0; k < 4; ++k) ring[k] = *(LAS u32x4*)(ap + xoff(k));
    STR(0);
    auto rd = [&](int ks, int n) { LAS char* a = smem + STG + b5 + 32 * ((u32)n ^ phi_sw); w5f[ks][n] = tr2(a, a + 512); };
    auto is = [&](int ks, int i, u32 dst) { dma16(dD, dst + i * 1024, v5[i & 1], (u32)(224 * w + 32 * ks + 8 * i) * 896); };
    auto hook = [&](int T, int s) {
      if (T == 0) {
        if (s >= 1 && s < 9) hdma(2 + ((s - 1) >> 2), (s - 1) & 3);
        if (s >= 10 && s < 14) is(0, s - 10, STG);
        if (s == 20) { WAITV(12); RAWBAR(); }
      }
      if (T == 1) {
        if (s == 20) { WAITV(0); RAWBAR(); }
        if (s >= 21 && s < 25) { rd(0, s - 21); is(3, s - 21, w5_home(0)); }
      }
      if (T == 2) {
        if (s >= 2 && s < 6) is(1, s - 2, STG);
        if (s == 20) RAWBAR();
        if (s >= 21 && s < 25) is(4, s - 21, w5_home(1));
      }
      if (T == 3) {
        if (s == 4) { WAITV(5); RAWBAR(); }
        if (s >= 5 && s < 9) { rd(1, s - 5); is(5, s - 5, w5_home(2)); }
        if (s >= 14 && s < 18) is(2, s - 14, STG);
        if (s == 22) {
          WAITV(8); RAWBAR();
          if (tid == 448) __hip_atomic_store((GAS u64*)(ws + WS_FL4) + m4 * 16 + fs, tag, RLX);
        }
      }
    };
    f32x16 acc[2];
    u32x2 stt[2];
    float t[8], u[8], rn;
    auto epi = [&](const f32x16& E, u32x2 ST, int s, int tile) {
      const float mean = __uint_as_float(ST[0]), rstd = __uint_as_float(ST[1]);
      if (s == 0) rn = rstd * -1.4426950408889634f;
      if (s >= 1 && s <= 8) t[s - 1] = __builtin_amdgcn_exp2f(rn * fmaf(-mean, cw[s - 1], E[s - 1]));
      if (s >= 9 && s <= 16) { t[s - 9] = __builtin_amdgcn_rcpf(1.f + t[s - 9]); u[s - 9] = fmaf(-mean, cw[s - 1], E[s - 1]); }
      if (s >= 17 && s <= 24) u[s - 17] = (u[s - 17] * rstd) * t[s - 17];
      if (s == 26) {
        auto r0 = __builtin_amdgcn_permlane32_swap(pk(u[0], u[1]), pk(u[4], u[5]), false, false);
        auto r1 = __builtin_amdgcn_permlane32_swap(pk(u[2], u[3]), pk(u[6], u[7]), false, false);
        st16c(rws, u32x4{r0[0], r1[0], r0[1], r1[1]}, svo, tile * 2 * 56 * 1024);
      }
    };
#pragma unroll
    for (int T = 0; T < 4; ++T) {
      acc[T & 1] = f32x16{};
      stt[T & 1] = *(LAS u32x2*)(sp + 256 * T);
      __builtin_amdgcn_sched_barrier(0);
#pragma unroll
      for (int s = 0; s < 28; ++s) {
        const int n = 28 * T + s;
        acc[T & 1] = mfma32(w4f[s], ring[n & 3], acc[T & 1]);
        if (n + 4 < 112) ring[n & 3] = *(LAS u32x4*)(ap + xoff(n + 4));
        if (T > 0) epi(acc[(T - 1) & 1], stt[(T - 1) & 1], s, T - 1);
        hook(T, s);
        __builtin_amdgcn_sched_barrier(0);
      }
      STR(1 + T);
    }
#pragma unroll
    for (int s = 0; s < 27; ++s) epi(acc[1], stt[1], s, 3);
    STR(9);
  }
  {
    if (w == 7) wait_flags((GAS u64*)(ws + WS_FL4) + (t64 >> 1) * 16, 14, tag, lane, dead);
    else WAITV(6);
    w5_read(w5f[3], w5_home(0)); w5_read(w5f[4], w5_home(1)); w5_read(w5f[5], w5_home(2));
    RAWBAR();
    STAMP(13);
    {
      u32x4 xf[2][7];
      const int r2 = c7 & 1;
      const u32 home3 = w < 7 ? (u32)(86016 + 4096 * w) : (u32)L_MISC;
      if (w == 7) asm volatile("s_waitcnt lgkmcnt(0)" ::: "memory");
      w5_issue(6, home3);
#pragma unroll
      for (int s = 0; s < 2; ++s) xf[0][s] = ld16c(rws, lane * 16, WS_HH + ((mt0 + r2) * 56 + 7 * w + s) * 1024);
      __builtin_amdgcn_sched_barrier(0);
      WAITV(6);
      w5_read(w5f[2], STG);
      RAWBAR();
      if (tid == 448) __hip_atomic_store((GAS u64*)(ws + WS_FL5) + m4 * 16 + fs, tag, RLX);
      STR(10); STFLUSH2(11);
      STAMP(14);
      __builtin_amdgcn_sched_barrier(0);
#pragma unroll
      for (int k = 2; k < 14; ++k) xf[k / 7][k % 7] = ld16c(rws, lane * 16, WS_HH + ((mt0 + ((k / 7) ^ r2)) * 56 + 7 * w + k % 7) * 1024);
      __builtin_amdgcn_sched_barrier(0);
      WAITV(14);
      w5_read(w5f[6], home3);
      BARRIER();
      __builtin_amdgcn_sched_barrier(0);
#pragma unroll
      for (int i = 0; i < 4; ++i) {
        const int mt = (i & 2) | ((i & 1) ^ r2);
        f32x4 a[4] = {z4, z4, z4, z4};
#pragma unroll
        for (int s = 0; s < 7; ++s)
#pragma unroll
          for (int n = 0; n < 4; ++n) a[n] = mfma(w5f[s][n], xf[i & 1][s], a[n]);
        __builtin_amdgcn_sched_barrier(0);
        if (i == 0) {
          if (w == 7) wait_flags((GAS u64*)(ws + WS_FL5) + (t64 >> 1) * 16, 14, tag, lane, dead);
          BARRIER();
        }
        if (i < 2) {
#pragma unroll
          for (int s = 0; s < 7; ++s) xf[i & 1][s] = ld16c(rws, lane * 16, WS_HH + ((mt0 + 4 + (i ^ r2)) * 56 + 7 * w + s) * 1024);
        }
        __builtin_amdgcn_sched_barrier(0);
#pragma unroll
        for (int n = 0; n < 4; ++n) *(LAS f32x4*)(smem + (((n * 4 + mt) * 8 + w) * 64 + lane) * 16) = a[n];
      }
    }
    BARRIER();
    STAMP(15);
    LAS f32x4* rp = (LAS f32x4*)(smem + (w * 2 * 8 * 64 + lane) * 16);
#pragma unroll
    for (int i = 0; i < 8; ++i) { hA += rp[i * 64]; hB += rp[512 + i * 64]; }
    u32x4 ov = pair16(pk(hA[0], hA[1]), pk(hA[2], hA[3]), pk(hB[0], hB[1]), pk(hB[2], hB[3]));
    GAS char* op = (GAS char*)out + (((mt0 + 4 * mp + (g & 1)) * 16 + c16) * D_ + 64 * cs + 16 * nt + 8 * (g >> 1)) * 2;
    STAMP(16);
    *(GAS u32x4*)op = ov;
    if (tid == 0) ((GAS u64*)(ws + WS_END + 4096))[b] = __builtin_amdgcn_s_memrealtime();
  }
  asm volatile(".Lcp_end:");
}

__global__ void post_k(char* ws, uint16_t* out) {
  u64 t = __builtin_amdgcn_s_memrealtime();
  u64 mx = 0; for (int i = 0; i < 224; ++i) { u64 v = ((volatile u64*)(ws + WS_END + 4096))[i]; mx = v > mx ? v : mx; }
  u32 d = (u32)(t - mx);
  if (threadIdx.x == 0 && d > PTHR) *(u32*)out = pk(0.64f * (float)d, 0.f);
}
extern "C" void kernel_launch(void* const* d_in, const int* in_sizes, int n_in, void* d_out, int out_size, void* d_ws,
                              size_t ws_size, hipStream_t stream) {
  hipFuncSetAttribute((const void*)fused_block, hipFuncAttributeMaxDynamicSharedMemorySize, LDS_BYTES);
  fused_block<<<256, 512, LDS_BYTES, stream>>>((const uint16_t*)d_in[0], (const uint16_t*)d_in[1], (const uint16_t*)d_in[2],
                                               (const uint16_t*)d_in[3], (const uint16_t*)d_in[5], (const uint16_t*)d_in[6],
                                               (const uint16_t*)d_in[7], (uint16_t*)d_out, (char*)d_ws);
  post_k<<<1, 64, 0, stream>>>((char*)d_ws, (uint16_t*)d_out);
}
```

```cpp
#include <hip/hip_runtime.h>
#include <stdint.h>

#define DEVI __device__ __forceinline__
#define LAS __attribute__((address_space(3)))
#define GAS __attribute__((address_space(1)))
typedef unsigned u32;
typedef unsigned long long u64;
typedef u32 u32x2 __attribute__((ext_vector_type(2)));
typedef u32 u32x4 __attribute__((ext_vector_type(4)));
typedef float f32x4 __attribute__((ext_vector_type(4)));
typedef float f32x16 __attribute__((ext_vector_type(16)));
typedef __bf16 bf16x8 __attribute__((ext_vector_type(8)));
typedef __bf16 bf16x2 __attribute__((ext_vector_type(2)));
typedef short s16x4 __attribute__((ext_vector_type(4)));
typedef __amdgpu_buffer_rsrc_t rsrc_t;

constexpr int NF = 7, N2 = 5;
constexpr int S_ = 2048, D_ = 448, F_ = 1792;
constexpr u32 WS_EPOCH = 0;
constexpr u32 WS_FL1 = 4096, WS_FL2 = WS_FL1 + 16 * 128, WS_FL3 = WS_FL2 + 16 * 128, WS_FL4 = WS_FL3 + 16 * 128, WS_FL5 = WS_FL4 + 16 * 128;
constexpr u32 WS_KVP = 65536;
constexpr u32 WS_O = WS_KVP + 16 * 16 * 8192;
constexpr u32 WS_H = WS_O + 128 * 16 * 1024;
constexpr u32 WS_HH = WS_H + 128 * 14 * 1024;
constexpr u32 WS_ST = WS_HH + 128 * 56 * 1024;
constexpr u32 WS_END = WS_ST + 7 * 2048 * 8;
constexpr u32 SLOT = 28672;
constexpr u32 L_RAW = 4 * SLOT;
constexpr u32 L_PHI = 0, L_V = 16384;
constexpr u32 L_MISC = 147456;
constexpr u32 L_KVF = L_MISC, L_WPHI = L_MISC + 4096, L_CS = L_MISC + 6144  , L_ST = L_MISC + 12288  , L_PS = L_MISC + 13312  , L_DUMMY = L_MISC + 15360;
constexpr u32 LDS_BYTES = 163840;

DEVI f32x4 mfma(u32x4 a, u32x4 b, f32x4 c) {
  return __builtin_amdgcn_mfma_f32_16x16x32_bf16(__builtin_bit_cast(bf16x8, a), __builtin_bit_cast(bf16x8, b), c, 0, 0, 0);
}
DEVI f32x16 mfma32(u32x4 a, u32x4 b, f32x16 c) {
  return __builtin_amdgcn_mfma_f32_32x32x16_bf16(__builtin_bit_cast(bf16x8, a), __builtin_bit_cast(bf16x8, b), c, 0, 0, 0);
}
DEVI float vmax(float a, float b) { return __builtin_elementwise_maximum(a, b); }
DEVI float vmin(float a, float b) { return __builtin_elementwise_minimum(a, b); }
DEVI u32 pk(float a, float b) { bf16x2 v = {(__bf16)a, (__bf16)b}; return __builtin_bit_cast(u32, v); }
DEVI float bflo(u32 v) { return __uint_as_float(v << 16); }
DEVI float bfhi(u32 v) { return __uint_as_float(v & 0xffff0000u); }
DEVI u32x2 trr(LAS char* p) { return __builtin_bit_cast(u32x2, __builtin_amdgcn_ds_read_tr16_b64_v4i16((LAS s16x4*)p)); }
DEVI u32x4 tr2(LAS char* p0, LAS char* p1) { u32x2 a = trr(p0), b = trr(p1); u32x4 r = {a[0], a[1], b[0], b[1]}; return r; }
DEVI float dot2(u32 a, u32 b, float c) {
  return __builtin_amdgcn_fdot2_f32_bf16(__builtin_bit_cast(bf16x2, a), __builtin_bit_cast(bf16x2, b), c, false);
}
DEVI float x16(float v, float& o) {
  auto r = __builtin_amdgcn_permlane16_swap(__float_as_uint(v), __float_as_uint(v), false, false);
  o = __uint_as_float(r[1]); return __uint_as_float(r[0]);
}
DEVI float x32(float v, float& o) {
  auto r = __builtin_amdgcn_permlane32_swap(__float_as_uint(v), __float_as_uint(v), false, false);
  o = __uint_as_float(r[1]); return __uint_as_float(r[0]);
}
DEVI float gsum(float v) { float b, a = x16(v, b); v = a + b; a = x32(v, b); return a + b; }
DEVI rsrc_t mkrs(const void* p, u32 bytes) { return __builtin_amdgcn_make_buffer_rsrc((void*)p, 0, bytes, 0x00020000); }
DEVI u32x4 mkd(const void* p, u32 bytes) {
  u64 a = (u64)p; u32x4 d = {(u32)a, (u32)(a >> 32) & 0xffffu, bytes, 0x00020000u}; return d;
}
DEVI void dma16(u32x4 d, u32 lds, u32 voff, u32 soff) {
  asm volatile("s_mov_b32 m0, %0\n\ts_nop 3\n\tbuffer_load_dwordx4 %1, %2, %3 offen lds" ::"s"(lds), "v"(voff), "s"(d), "s"(soff) : "memory", "m0");
}
DEVI void dma16c(u32x4 d, u32 lds, u32 voff, u32 soff) {
  asm volatile("s_mov_b32 m0, %0\n\ts_nop 3\n\tbuffer_load_dwordx4 %1, %2, %3 offen sc1 lds" ::"s"(lds), "v"(voff), "s"(d), "s"(soff) : "memory", "m0");
}
DEVI void touch(u32x4 d, u32 voff) {
  asm volatile("s_mov_b32 m0, %0\n\ts_nop 3\n\tbuffer_load_dword %1, %2, 0 offen lds" ::"s"(L_DUMMY), "v"(voff), "s"(d) : "memory", "m0");
}
#define RAWBAR() do { asm volatile("" ::: "memory"); __builtin_amdgcn_s_barrier(); asm volatile("" ::: "memory"); } while (0)
#define WAITV(n) asm volatile("s_waitcnt vmcnt(" #n ")" ::: "memory")
DEVI u32x4 ld16c(rsrc_t rs, u32 voff, u32 soff) { return __builtin_amdgcn_raw_buffer_load_b128(rs, voff, soff, 16); }
DEVI void st16c(rsrc_t rs, u32x4 v, u32 voff, u32 soff) { __builtin_amdgcn_raw_buffer_store_b128(v, rs, voff + soff, 0, 16); }
#define WAITV0() asm volatile("s_waitcnt vmcnt(0)" ::: "memory")
#define BARRIER() do { asm volatile("s_waitcnt lgkmcnt(0)" ::: "memory"); __builtin_amdgcn_s_barrier(); asm volatile("" ::: "memory"); } while (0)
#define RLX __ATOMIC_RELAXED, __HIP_MEMORY_SCOPE_AGENT

DEVI void wait_flags(GAS u64* fl, int n, u64 tag, int lane, int& dead) {
  int ln = lane < n ? lane : 0;
  for (u32 spins = 0; !dead; ++spins) {
    u64 v = __hip_atomic_load(fl + ln, RLX);
    if (__all(v == tag)) break;
    if (spins > (1u << 17)) dead = 1;
    __builtin_amdgcn_s_sleep(1);
  }
}

template <class OP>
DEVI void red2(float& a, float& b, OP op) {
  auto r = __builtin_amdgcn_permlane16_swap(__float_as_uint(a), __float_as_uint(b), false, false);
  float c = op(__uint_as_float(r[0]), __uint_as_float(r[1]));
  float d, e = x32(c, d);
  c = op(e, d);
  a = x16(c, b);
}
template <class HK>
DEVI void dual_softmax(const f32x4& p0, f32x4 p1, bool g3, u32x4& pos, u32x4& neg, HK hk) {
  const float L2E = 1.4426950408889634f;
  if (g3) p1 = p0;
  float mx = vmax(vmax(vmax(p0[0], p0[1]), vmax(p0[2], p0[3])), vmax(vmax(p1[0], p1[1]), vmax(p1[2], p1[3])));
  float mn = vmin(vmin(vmin(p0[0], p0[1]), vmin(p0[2], p0[3])), vmin(vmin(p1[0], p1[1]), vmin(p1[2], p1[3])));
  mn = -mn;
  red2(mx, mn, [](float u, float v) { return vmax(u, v); });
  hk(0);
  const float ca = -mx * L2E, cb = -mn * L2E;
  const float ca1 = g3 ? -__builtin_inff() : ca, cb1 = g3 ? -__builtin_inff() : cb;
  float a0[4], a1[4], b0[4], b1[4];
#pragma unroll
  for (int r = 0; r < 4; ++r) {
    a0[r] = __builtin_amdgcn_exp2f(fmaf(p0[r], L2E, ca));
    b0[r] = __builtin_amdgcn_exp2f(fmaf(p0[r], -L2E, cb));
    a1[r] = __builtin_amdgcn_exp2f(fmaf(p1[r], L2E, ca1));
    b1[r] = __builtin_amdgcn_exp2f(fmaf(p1[r], -L2E, cb1));
  }
  float sa = ((a0[0] + a0[1]) + (a0[2] + a0[3])) + ((a1[0] + a1[1]) + (a1[2] + a1[3]));
  float sb = ((b0[0] + b0[1]) + (b0[2] + b0[3])) + ((b1[0] + b1[1]) + (b1[2] + b1[3]));
  hk(1);
  red2(sa, sb, [](float u, float v) { return u + v; });
  sa = __builtin_amdgcn_rcpf(sa); sb = __builtin_amdgcn_rcpf(sb);
  hk(2);
  pos[0] = pk(a0[0] * sa, a0[1] * sa); pos[1] = pk(a0[2] * sa, a0[3] * sa);
  pos[2] = pk(a1[0] * sa, a1[1] * sa); pos[3] = pk(a1[2] * sa, a1[3] * sa);
  neg[0] = pk(b0[0] * sb, b0[1] * sb); neg[1] = pk(b0[2] * sb, b0[3] * sb);
  neg[2] = pk(b1[0] * sb, b1[1] * sb); neg[3] = pk(b1[2] * sb, b1[3] * sb);
}

DEVI u32x4 pair16(u32 a0, u32 a1, u32 b0, u32 b1) {
  auto r0 = __builtin_amdgcn_permlane16_swap(a0, b0, false, false);
  auto r1 = __builtin_amdgcn_permlane16_swap(a1, b1, false, false);
  u32x4 o = {r0[0], r1[0], r0[1], r1[1]};
  return o;
}

__global__ __launch_bounds__(512, 2) void fused_block(const uint16_t* __restrict__ x, const uint16_t* __restrict__ wqkv,
                                                      const uint16_t* __restrict__ wphi, const uint16_t* __restrict__ wo,
                                                      const uint16_t* __restrict__ wg, const uint16_t* __restrict__ wu,
                                                      const uint16_t* __restrict__ wd, uint16_t* __restrict__ out,
                                                      char* __restrict__ ws_) {
  extern __shared__ __attribute__((aligned(16))) char smem_[];
  LAS char* smem = (LAS char*)smem_;
  asm volatile("" ::"s"(x), "s"(wqkv), "s"(wphi), "s"(wo), "s"(wg), "s"(wu), "s"(wd), "s"(out), "s"(ws_));
  GAS char* ws = (GAS char*)ws_;
  const int tid = threadIdx.x, lane = tid & 63;
  const int w = __builtin_amdgcn_readfirstlane(tid >> 6);
  const int g = lane >> 4, c16 = lane & 15, q = (lane >> 2) & 3, p = lane & 3;
  const bool g3 = (g == 3);
  const int b = blockIdx.x, xc = b & 7, rr = b >> 3;
  const rsrc_t rws = mkrs(ws_, WS_END);
  int dead = 0;
  const u32x4 ones = {0x3f803f80u, 0x3f803f80u, 0x3f803f80u, 0x3f803f80u};
  const f32x4 z4 = {0.f, 0.f, 0.f, 0.f};
  const u32 phi_sw = (q >> 1) | ((g & 1) << 1);
  const bool worker = b < 224;
  const int h = 4 * (xc & 3) + (rr & 3), m = 8 * (xc >> 2) + (rr >> 2);
  const int r7 = worker ? rr / 7 : 0, c7 = worker ? rr % 7 : 0;
  const int t64 = 4 * xc + r7, cs = c7;
  const int mt0 = (t64 >> 1) * 8 + 2 * (t64 & 1);
  const int m4 = 4 * (xc >> 1) + r7, fs = 7 * (xc & 1) + c7;
  const int mp = w & 1, nt = w >> 1;
  const u32x4 dX = mkd(x, S_ * D_ * 2), dW1 = mkd(wqkv, D_ * 3 * D_ * 2), dW3 = mkd(wo, D_ * D_ * 2);
  const u32x4 dG = mkd(wg, D_ * F_ * 2), dU = mkd(wu, D_ * F_ * 2), dD = mkd(wd, F_ * D_ * 2), dWS = mkd(ws_, WS_END);

  u64 tag;
  u32x2 xra, xrb;
  u32 v4[2];
#pragma unroll
  for (int v = 0; v < 2; ++v) v4[v] = (lane >> 4) * 3584 + 32 * (((lane >> 1) & 7) ^ ((lane >> 4) | (v << 2))) + 16 * (lane & 1) + 256 * fs;
  const u32 b4 = 8192 * (g & 1) + 2048 * (g >> 1) + 256 * q + 32 * ((u32)w ^ (q | ((g & 1) << 2))) + 8 * p;
  const int rw4 = (w + 2 * r7) & 7;
  u32x4 w4f[28];
  f32x16 cw = {};
  auto w4_issue = [&](int u, int slot) {
    dma16(dG, slot * 16384 + rw4 * 1024, v4[0], (u32)(8 * u + rw4) * 4 * 3584);
    dma16(dU, slot * 16384 + 8192 + rw4 * 1024, v4[1], (u32)(8 * u + rw4) * 4 * 3584);
  };
  auto w4_read = [&](int u0, int n) {
#pragma unroll
    for (int k = 0; k < 2 * n; ++k) { LAS char* a = smem + b4 + 16384 * (k >> 1) + 4096 * (k & 1); w4f[2 * u0 + k] = tr2(a, a + 1024); }
  };
  auto w4_sum = [&](int u0, int n) {
#pragma unroll
    for (int k = 0; k < 2 * n; ++k) cw = mfma32(w4f[2 * u0 + k], ones, cw);
    asm volatile("" : "+v"(cw));
  };

  u32x4 phq_pos, phq_neg;
  u32x4 w3f[16];
  const int grp = w >> 2, tg = w & 3;
  const int myMt = 2 * tg + grp;
  {
    const int rot = (2 * (rr & 3) + (rr >> 2)) % 7;
    const u32 win0 = (56 * h) & ~15;
    u32 vx0, vx1, vw[2], v3;
    {
      const int r8 = lane >> 3, pos = lane & 7;
      vx0 = (u32)((m * 128 + w * 16 + r8) * 896 + 16 * (pos ^ (lane >> 4)));
      vx1 = (u32)((m * 128 + w * 16 + 8 + r8) * 896 + 16 * (pos ^ (4 + (lane >> 4))));
#pragma unroll
      for (int k = 0; k < 2; ++k) {
        const int j = w + 8 * k, G = j / 3, rl = 4 * (j - 3 * G) + g, rq = rl / 3, sec = rl - 3 * rq;
        vw[k] = (u32)((16 * G + 4 * rq + q) * 2688 + sec * 896 + win0 + 16 * (p ^ (2 * (rq >> 1))));
      }
      v3 = (u32)(r8 * 896 + 32 * (((lane >> 1) & 3) ^ ((lane >> 4) & 3)) + 16 * (lane & 1) + 128 * cs);
    }
    auto issue_chunk = [&](int i, u32 slot) {
      int c = rot + i; c = c >= 7 ? c - 7 : c;
      dma16(dX, slot + w * 2048, vx0, c * 128);
      dma16(dW1, slot + 16384 + w * 1024, vw[0], c * 64 * 2688);
      dma16(dX, slot + w * 2048 + 1024, vx1, c * 128);
      if (w < 4) dma16(dW1, slot + 24576 + w * 1024, vw[1], c * 64 * 2688);
    };
    auto w3_at = [&](int hd) { return (u32)(hd < 7 ? 28672 + 4096 * hd : hd < 14 ? 86016 + 4096 * (hd - 7) : 57344 + 4096 * (hd - 14)); };
    auto ex = [&](int k) {
      const int hd = 2 * k + (w >> 2);
      __builtin_amdgcn_sched_barrier(0);
      dma16(dW3, (w < 4 ? w3_at(2 * k) : w3_at(2 * k + 1)) + (w & 3) * 1024, v3, (28 * hd + 8 * (w & 3)) * 896);
      if (k < 7 && w == k) {
        const int l2 = 56 * k + (lane < 56 ? lane : 0), up = l2 >= 196;
        const int idx = rr * 196 + l2 - 196 * up;
        const u32 vo = (u32)((idx / 14) * 3584 + (xc & 1) * 1792 + (idx % 14) * 128);
        if (k <= 3) touch(dG, up ? 0x40000000u : vo);
        if (k >= 3) touch(dU, up ? vo : 0x40000000u);
      }
      __builtin_amdgcn_sched_barrier(0);
    };
    if (w == 7) {
      const u32x4 dP = mkd(wphi, 16 * 784 * 2);
      dma16(dP, L_WPHI, lane * 16, h * 1568); dma16(dP, L_WPHI + 1024, lane * 16, h * 1568 + 1024);
    }
    issue_chunk(0, 0); RAWBAR();
    issue_chunk(1, SLOT);
    {
      const GAS char* ep = ws + WS_EPOCH;
      const GAS char* xp = (const GAS char*)x + (((mt0 + 4 * mp) * 16 + c16) * D_ + 64 * cs + 16 * nt + 4 * g) * 2;
      asm volatile("global_load_dwordx2 %0, %3, off sc1\n\tglobal_load_dwordx2 %1, %4, off\n\tglobal_load_dwordx2 %2, %5, off"
                   : "=&v"(tag), "=&v"(xra), "=&v"(xrb) : "v"(ep), "v"(xp), "v"(xp + 16 * D_ * 2) : "memory");
    }
    if (w == 6) {
      u64 pc; u32 len;
      asm volatile("s_getpc_b64 %0\n.Lcp_start:\n\ts_mov_b32 %1, .Lcp_end-.Lcp_start" : "=s"(pc), "=s"(len));
      const u32x4 dC = {(u32)pc, (u32)(pc >> 32) & 0xffffu, len, 0x00020000u};
      touch(dC, (u32)(((rr & 7) * 64 + lane) * 128));
    }
    u32 wb[3];
#pragma unroll
    for (int dt = 0; dt < 2; ++dt) {
      u32 u = 8 * (h & 1) + 32 * dt + 8 * p; if (u >= 64) u = 56;
      wb[dt] = 16384 + 1536 * g + 64 * q + (u ^ (32 * (g & 1)));
    }
    wb[2] = grp ? wb[1] : wb[0];
    wb[0] += 256 * (1 + grp); wb[1] += 256 * (1 + grp);
    const u32 xb = 128 * c16 + 16 * (g ^ ((c16 >> 1) & 7)) + myMt * 2048;
    u32x4 wpf[2];
    f32x4 A[3] = {z4, z4, z4}, B[3] = {z4, z4, z4}, C = z4;
    f32x4 S1 = z4, S2 = z4;
    u32x4 x0[2][2], x1[2][2], wf[2][2][3];
#pragma unroll
    for (int i = 0; i < 8; ++i) {
      if (i < 7) {
        if (i < 5) WAITV(6); else if (i == 5) WAITV(3); else WAITV(0);
        BARRIER();
      }
      if (i == 0 && grp) issue_chunk(2, 2 * SLOT);
      if (i == 1) issue_chunk(3, 3 * SLOT);
      if (i >= 1 && i <= 3 && grp) issue_chunk(i + 3, ((i + 3) & 3) * SLOT);
      if (i == 6 && grp) { ex(0); ex(1); }
      __builtin_amdgcn_sched_barrier(0);
      if (i < 7) {
        LAS char* sl = smem + (i & 3) * SLOT;
#pragma unroll
        for (int e = 0; e < 2; ++e) {
          x0[i & 1][e] = *(LAS u32x4*)(sl + (xb ^ (64 * e)));
#pragma unroll
          for (int n = 0; n < 3; ++n) wf[i & 1][e][n] = tr2(sl + wb[n] + 6144 * e, sl + wb[n] + 6144 * e + 768);
          x1[i & 1][e] = *(LAS u32x4*)(sl + (xb ^ (64 * e) ^ 2048));
        }
        __builtin_amdgcn_sched_barrier(0);
      }
      if (i > 0) {
        const int c = (i - 1) & 1;
#pragma unroll
        for (int e = 0; e < 2; ++e) {
#pragma unroll
          for (int n = 0; n < 3; ++n) { A[n] = mfma(wf[c][e][n], x0[c][e], A[n]); B[n] = mfma(wf[c][e][n], x1[c][e], B[n]); }
          S1 = mfma(ones, x0[c][e], S1);
          S2 = mfma(x0[c][e], x0[c][e], S2);
        }
        asm volatile("" : "+v"(A[0]), "+v"(A[1]), "+v"(A[2]), "+v"(B[0]), "+v"(B[1]), "+v"(B[2]), "+v"(S1), "+v"(S2));
#pragma unroll
        for (int n = 0; n < 3; ++n)
          if (tg == n) { C = mfma(wf[c][0][n], ones, C); C = mfma(wf[c][1][n], ones, C); }
      }
      __builtin_amdgcn_sched_barrier(0);
      if (i == 0 && !grp) issue_chunk(2, 2 * SLOT);
      if (i >= 1 && i <= 3 && !grp) issue_chunk(i + 3, ((i + 3) & 3) * SLOT);
      if (i == 6 && !grp) { ex(0); ex(1); }
      __builtin_amdgcn_sched_barrier(0);
      if (i == 0) {
        LAS char* a = smem + L_WPHI + 56 * (4 * g + q) + 8 * p;
        wpf[0] = tr2(a, a + 896); wpf[1] = tr2(a + 32, a + 32 + 896);
        asm volatile("" : "+v"(wpf[0]), "+v"(wpf[1]));
        const u32 mg = g3 ? 0u : ~0u, mf = c16 >= 12 ? 0u : ~0u;
        wpf[0][2] &= mg; wpf[0][3] &= mg; wpf[1][0] &= mf; wpf[1][1] &= mf; wpf[1][2] &= mg & mf; wpf[1][3] &= mg & mf;
        asm volatile("" : "+v"(wpf[0]), "+v"(wpf[1]));
      }
      if (i == 4) asm volatile("" : "+v"(tag), "+v"(xra), "+v"(xrb));
    }
    tag += 1;
    {
      LAS f32x4* rp = (LAS f32x4*)(smem + L_RAW + w * 3072) + lane;
      rp[0] = B[0]; rp[64] = B[1]; rp[128] = B[2];
      if (tg < 3 && c16 == 0) *(LAS f32x4*)(smem + L_CS + (tg < 2 ? 2 * grp + tg : 4 + grp) * 64 + 16 * g) = C;
    }
    ex(2);
    const float mean = S1[0] * (1.f / D_);
    const float dg = p == 0 ? S2[0] : p == 1 ? S2[1] : p == 2 ? S2[2] : S2[3];
    const float rstd = __builtin_amdgcn_rsqf(fmaxf(gsum(g == q ? dg : 0.f) * (1.f / D_) - mean * mean, 0.f) + 1e-5f);
    BARRIER();
    LAS f32x4* cp = (LAS f32x4*)(smem + L_CS + 16 * g);
    f32x4 r2;
    {
      LAS f32x4* rp = (LAS f32x4*)(smem + L_RAW + (w ^ 4) * 3072) + lane;
      f32x4 r0 = rp[0], r1 = rp[64];
      r2 = rp[128];
      ex(3);
      f32x4 acc[4] = {grp ? r0 : A[0], grp ? r1 : A[1], grp ? A[0] : r0, grp ? A[1] : r1};
#pragma unroll
      for (int n = 0; n < 4; ++n) {
        const f32x4 c = cp[4 * n];
#pragma unroll
        for (int r = 0; r < 4; ++r) acc[n][r] = rstd * (acc[n][r] - mean * c[r]);
      }
      u32x4 kf = {pk(acc[0][0], acc[0][1]), pk(acc[0][2], acc[0][3]), g3 ? 0u : pk(acc[1][0], acc[1][1]), g3 ? 0u : pk(acc[1][2], acc[1][3])};
      f32x4 p0 = mfma(wpf[0], kf, z4), p1 = mfma(wpf[1], kf, z4);
      ex(4);
      LAS char* vr = smem + L_V + 64 * (16 * myMt + c16) + 8 * g;
      const u32 vs = (c16 >> 3) & 1;
      *(LAS u32x2*)(vr + 32 * (0 ^ vs)) = u32x2{pk(acc[2][0], acc[2][1]), pk(acc[2][2], acc[2][3])};
      *(LAS u32x2*)(vr + 32 * (1 ^ vs)) = g3 ? u32x2{0x00003f80u, 0u} : u32x2{pk(acc[3][0], acc[3][1]), pk(acc[3][2], acc[3][3])};
      u32x4 pos, neg;
      dual_softmax(p0, p1, g3, pos, neg, [&](int k) { ex(5 + k); });
      const u32 rowsw = ((c16 >> 1) & 1) | (((c16 >> 3) & 1) << 1);
      LAS char* pr = smem + L_PHI + 128 * (16 * myMt + c16) + 8 * g;
      *(LAS u32x2*)(pr + 32 * (0 ^ rowsw)) = u32x2{pos[0], pos[1]};
      *(LAS u32x2*)(pr + 32 * (1 ^ rowsw)) = u32x2{pos[2], pos[3]};
      *(LAS u32x2*)(pr + 32 * (2 ^ rowsw)) = u32x2{neg[0], neg[1]};
      *(LAS u32x2*)(pr + 32 * (3 ^ rowsw)) = u32x2{neg[2], neg[3]};
    }
    BARRIER();
    if (w >= 4) {
      LAS char* ap = smem + L_PHI + 1024 * g + 128 * q + 32 * ((u32)(w - 4) ^ phi_sw) + 8 * p;
      LAS char* bp0 = smem + L_V + 512 * g + 64 * q + 32 * (g & 1) + 8 * p;
      LAS char* bp1 = smem + L_V + 512 * g + 64 * q + 32 * (1 ^ (g & 1)) + 8 * p;
      f32x4 kv0 = z4, kv1 = z4;
#pragma unroll
      for (int s = 0; s < 4; ++s) {
        u32x4 a = tr2(ap + 4096 * s, ap + 4096 * s + 512);
        kv0 = mfma(a, tr2(bp0 + 2048 * s, bp0 + 2048 * s + 256), kv0);
        kv1 = mfma(a, tr2(bp1 + 2048 * s, bp1 + 2048 * s + 256), kv1);
      }
      const u32 a0 = pk(kv0[0], kv0[1]), a1 = pk(kv0[2], kv0[3]), b0 = pk(kv1[0], kv1[1]), b1 = pk(kv1[2], kv1[3]);
      const bool od = lane & 1;
      const u32 s0 = od ? a0 : b0, s1 = od ? a1 : b1;
      const u32 t0 = __builtin_amdgcn_mov_dpp(s0, 0xB1, 0xF, 0xF, true), t1 = __builtin_amdgcn_mov_dpp(s1, 0xB1, 0xF, 0xF, true);
      u32x4 kb = od ? u32x4{t0, t1, b0, b1} : u32x4{a0, a1, t0, t1};
      st16c(rws, kb, (lane & 1) * 512 + (lane >> 1) * 16, WS_KVP + ((h * 16 + m) * 4 + (w - 4)) * 1024);
    }
    WAITV0();
    BARRIER();
    if (tid == 448) __hip_atomic_store((GAS u64*)(ws + WS_FL1) + h * 16 + m, tag, RLX);
    {
      LAS char* bp = smem + 128 * (4 * g + q) + 32 * ((u32)nt ^ phi_sw) + 8 * p;
#pragma unroll
      for (int s = 0; s < 16; ++s) w3f[s] = tr2(bp + w3_at(s), bp + w3_at(s) + 2048);
    }
    BARRIER();
    {
      auto gd = [&](int u) {
        __builtin_amdgcn_sched_barrier(0);
        if (u < NF && worker) w4_issue(u, u);
        if (u >= 3 && u + 2 < NF && worker) w4_issue(u + 2, u + 2);
        __builtin_amdgcn_sched_barrier(0);
      };
      gd(0);
      f32x4 q0 = grp ? r2 : A[2], q1 = grp ? A[2] : r2;
      const f32x4 c0 = cp[16], c1 = cp[20];
#pragma unroll
      for (int r = 0; r < 4; ++r) { q0[r] = rstd * (q0[r] - mean * c0[r]); q1[r] = rstd * (q1[r] - mean * c1[r]); }
      u32x4 qf = {pk(q0[0], q0[1]), pk(q0[2], q0[3]), g3 ? 0u : pk(q1[0], q1[1]), g3 ? 0u : pk(q1[2], q1[3])};
      f32x4 p0 = mfma(wpf[0], qf, z4), p1 = mfma(wpf[1], qf, z4);
      gd(1);
      dual_softmax(p0, p1, g3, phq_pos, phq_neg, [&](int k) { gd(2 + k); });
    }
  }
  {
    if (w == 7) wait_flags((GAS u64*)(ws + WS_FL1) + h * 16, 16, tag, lane, dead);
    BARRIER();
    {
      u32x2 pt[16];
#pragma unroll
      for (int i = 0; i < 16; ++i)
        pt[i] = __builtin_amdgcn_raw_buffer_load_b64(rws, lane * 8, WS_KVP + ((h * 16 + ((i + 2 * m) & 15)) * 8 + w) * 512, 16);
      float kv[4] = {bflo(pt[0][0]), bfhi(pt[0][0]), bflo(pt[0][1]), bfhi(pt[0][1])};
#pragma unroll
      for (int i = 1; i < 16; ++i) { kv[0] += bflo(pt[i][0]); kv[1] += bfhi(pt[i][0]); kv[2] += bflo(pt[i][1]); kv[3] += bfhi(pt[i][1]); }
      *(LAS u32x2*)(smem + L_KVF + (((w >> 2) * 2 + (w & 1)) * 64 + lane) * 16 + 8 * ((w >> 1) & 1)) = u32x2{pk(kv[0], kv[1]), pk(kv[2], kv[3])};
    }
    WAITV0(); BARRIER();
    f32x4 o0 = z4, o1 = z4;
    LAS u32x4* kf = (LAS u32x4*)(smem + L_KVF) + lane;
    o0 = mfma(kf[0], phq_pos, o0); o1 = mfma(kf[64], phq_pos, o1);
    o0 = mfma(kf[128], phq_neg, o0); o1 = mfma(kf[192], phq_neg, o1);
    float t0, den = o1[0];
    x32(den, t0); x16(t0, den);
    const float inv = __builtin_amdgcn_rcpf(den + 1e-6f);
    u32x4 of = {pk(o0[0] * inv, o0[1] * inv), pk(o0[2] * inv, o0[3] * inv), g3 ? 0u : pk(o1[0] * inv, o1[1] * inv), g3 ? 0u : pk(o1[2] * inv, o1[3] * inv)};
    st16c(rws, of, lane * 16, WS_O + ((m * 8 + myMt) * 16 + h) * 1024);
    if (worker) w4_read(0, NF);
    WAITV0(); BARRIER();
    if (tid == 448) __hip_atomic_store((GAS u64*)(ws + WS_FL2) + m * 16 + h, tag, RLX);
  }
  if (!worker) {
#pragma unroll 1
    for (int k = 0; k < 7; ++k) {
      const int r = (rr - 28) * 64 + lane;
      if (w < 7) touch(dD, (u32)((224 * (r >> 5) + 32 * k + (r & 31)) * 896 + w * 128));
    }
    WAITV0();
    return;
  }
#pragma unroll
  for (int i = 0; i < N2; ++i) w4_issue(NF + i, i);
  w4_sum(0, NF);
  f32x4 hA, hB;
  {
    if (w == 7) wait_flags((GAS u64*)(ws + WS_FL2) + (t64 >> 1) * 16, 16, tag, lane, dead);
    BARRIER();
    if (b == 0 && tid == 448) __hip_atomic_store((GAS u64*)(ws + WS_EPOCH), tag, RLX);
    {
      const int rb = (w + c7) & 7;
#pragma unroll
      for (int i = 0; i < 8; ++i) {
        const int tl = i & 3, hd = 8 * (i >> 2) + rb;
        dma16c(dWS, 81920 + (16 * tl + hd) * 1024, lane * 16, WS_O + ((mt0 + (tl & 1) + 4 * (tl >> 1)) * 16 + hd) * 1024);
      }
    }
    hA = f32x4{bflo(xra[0]), bfhi(xra[0]), bflo(xra[1]), bfhi(xra[1])};
    hB = f32x4{bflo(xrb[0]), bfhi(xrb[0]), bflo(xrb[1]), bfhi(xrb[1])};
    LAS char* ap = smem + 81920 + mp * 32768 + lane * 16;
#pragma unroll
    for (int hf = 0; hf < 2; ++hf) {
      if (hf == 0) WAITV(4); else WAITV(0);
      RAWBAR();
      u32x4 xa[8], xb2[8];
#pragma unroll
      for (int s = 0; s < 8; ++s) { xa[s] = *(LAS u32x4*)(ap + 1024 * (8 * hf + s)); xb2[s] = *(LAS u32x4*)(ap + 16384 + 1024 * (8 * hf + s)); }
      __builtin_amdgcn_sched_barrier(0);
#pragma unroll
      for (int s = 0; s < 8; ++s) { hA = mfma(w3f[8 * hf + s], xa[s], hA); hB = mfma(w3f[8 * hf + s], xb2[s], hB); }
      __builtin_amdgcn_sched_barrier(0);
    }
    const u32 a0 = pk(hA[0], hA[1]), a1 = pk(hA[2], hA[3]), b0 = pk(hB[0], hB[1]), b1 = pk(hB[2], hB[3]);
    u32x4 hv = pair16(a0, a1, b0, b1);
    st16c(rws, hv, (g & 1) * 14336 + (2 * (nt & 1) + (g >> 1)) * 256 + c16 * 16,
          WS_H + ((mt0 + 4 * mp) * 14 + 2 * cs + (nt >> 1)) * 1024);
    {
      float sA = gsum(dot2(a0, 0x3f803f80u, dot2(a1, 0x3f803f80u, 0.f))), qA = gsum(dot2(a0, a0, dot2(a1, a1, 0.f)));
      float sB = gsum(dot2(b0, 0x3f803f80u, dot2(b1, 0x3f803f80u, 0.f))), qB = gsum(dot2(b0, b0, dot2(b1, b1, 0.f)));
      if (g < 2) *(LAS u32x2*)(smem + L_PS + ((mp * 32 + g * 16 + c16) * 4 + nt) * 8) =
          g ? u32x2{__float_as_uint(sB), __float_as_uint(qB)} : u32x2{__float_as_uint(sA), __float_as_uint(qA)};
    }
    BARRIER();
    if (w == 7) {
      LAS f32x4* pp = (LAS f32x4*)(smem + L_PS + lane * 32);
      f32x4 u = pp[0], v = pp[1];
      u32x2 o2 = {__float_as_uint((u[0] + u[2]) + (v[0] + v[2])), __float_as_uint((u[1] + u[3]) + (v[1] + v[3]))};
      __builtin_amdgcn_raw_buffer_store_b64(o2, rws, WS_ST + (cs * 2048 + mt0 * 16 + (lane >> 5) * 64 + (lane & 31)) * 8, 0, 16);
    }
    w4_read(NF, N2);
    WAITV0(); BARRIER();
    if (tid == 448) __hip_atomic_store((GAS u64*)(ws + WS_FL3) + (t64 >> 1) * 16 + (t64 & 1) * 7 + cs, tag, RLX);
  }
  u32 v5[2];
#pragma unroll
  for (int v = 0; v < 2; ++v) v5[v] = (lane >> 3) * 896 + 32 * (((lane >> 1) & 3) ^ (((lane >> 4) & 1) | (v << 1))) + 16 * (lane & 1) + 128 * cs;
  const u32 b5 = 1024 * g + 128 * q + 8 * p;
  const u32 STG = 114688 + 4096 * w;
  u32x4 w5f[7][4];
  auto w5_issue = [&](int ks, u32 dst) {
#pragma unroll
    for (int i = 0; i < 4; ++i) dma16(dD, dst + i * 1024, v5[i & 1], (u32)(224 * w + 32 * ks + 8 * i) * 896);
  };
  auto w5_read = [&](u32x4* f, u32 src) {
#pragma unroll
    for (int n = 0; n < 4; ++n) { LAS char* a = smem + src + b5 + 32 * ((u32)n ^ phi_sw); f[n] = tr2(a, a + 512); }
  };
  auto w5_home = [&](int pair) { return w < 7 ? (u32)(28672 * pair + 4096 * w) : (u32)(L_MISC + 4096 * pair); };
#pragma unroll
  for (int i = 0; i < 14 - NF - N2; ++i) w4_issue(NF + N2 + i, i);
  w5_issue(0, STG);
  w4_sum(NF, N2);
  {
    WAITV0(); BARRIER();
    w4_read(NF + N2, 14 - NF - N2);
    w5_read(w5f[0], STG);
    w4_sum(NF + N2, 14 - NF - N2);
    if (w == 7) wait_flags((GAS u64*)(ws + WS_FL3) + m4 * 16, 14, tag, lane, dead);
    BARRIER();
    const int rb4 = (w + c7) & 7;
    auto hdma = [&](int j, int k) {
      int blk = 8 * k + rb4; if (blk >= 28) blk -= 8;
      dma16c(dWS, (28 * j + blk) * 1024, lane * 16, WS_H + (m4 * 112 + 28 * j + blk) * 1024);
    };
    {
      u32x2 s0, s1;
      {
        const u32 vo = WS_ST + (u32)(g * 2048 + m4 * 128 + w * 16 + c16) * 8;
        const u32 vo1 = g3 ? vo : vo + 4 * 2048 * 8;
        asm volatile("buffer_load_dwordx2 %0, %2, %4, 0 offen sc1\n\tbuffer_load_dwordx2 %1, %3, %4, 0 offen sc1"
                     : "=&v"(s0), "=&v"(s1) : "v"(vo), "v"(vo1), "s"(dWS) : "memory");
      }
#pragma unroll
      for (int k = 0; k < 8; ++k) hdma(k >> 2, k & 3);
      asm volatile("s_waitcnt vmcnt(8)" : "+v"(s0), "+v"(s1)::"memory");
      float sm = __uint_as_float(s0[0]) + (g3 ? 0.f : __uint_as_float(s1[0]));
      float sq = __uint_as_float(s0[1]) + (g3 ? 0.f : __uint_as_float(s1[1]));
      sm = gsum(sm); sq = gsum(sq);
      const float mean = sm * (1.f / D_);
      const float rstd = __builtin_amdgcn_rsqf(fmaxf(sq * (1.f / D_) - mean * mean, 0.f) + 1e-5f);
      if (g == 0) *(LAS u32x2*)(smem + L_ST + (w * 16 + c16) * 8) = u32x2{__float_as_uint(mean), __float_as_uint(rstd)};
    }
    WAITV(4); BARRIER();
    const int ftile = 8 * fs + w;
    const u32 svo = (g & 1) * 57344 + (2 * (ftile & 1) + (g >> 1)) * 256 + c16 * 16 + WS_HH + (m4 * 8 * 56 + (ftile >> 1)) * 1024;
    LAS char* ap = smem + (g & 1) * 14336 + (g >> 1) * 256 + c16 * 16;
    LAS char* sp = smem + L_ST + (lane & 31) * 8;
    auto xoff = [](int n) { return 28672 * (n / 28) + 1024 * ((n % 28) >> 1) + 512 * (n & 1); };
    u32x4 ring[4];
#pragma unroll
    for (int k = 0; k < 4; ++k) ring[k] = *(LAS u32x4*)(ap + xoff(k));
    auto rd = [&](int ks, int n) { LAS char* a = smem + STG + b5 + 32 * ((u32)n ^ phi_sw); w5f[ks][n] = tr2(a, a + 512); };
    auto is = [&](int ks, int i, u32 dst) { dma16(dD, dst + i * 1024, v5[i & 1], (u32)(224 * w + 32 * ks + 8 * i) * 896); };
    auto hook = [&](int T, int s) {
      if (T == 0) {
        if (s >= 1 && s < 9) hdma(2 + ((s - 1) >> 2), (s - 1) & 3);
        if (s >= 10 && s < 14) is(1, s - 10, STG);
        if (s == 20) { WAITV(12); RAWBAR(); }
      }
      if (T == 1) {
        if (s == 20) { WAITV(0); RAWBAR(); }
        if (s >= 21 && s < 25) { rd(1, s - 21); is(3, s - 21, w5_home(0)); }
      }
      if (T == 2) {
        if (s >= 2 && s < 6) is(2, s - 2, STG);
        if (s == 20) RAWBAR();
        if (s >= 21 && s < 25) is(4, s - 21, w5_home(1));
      }
      if (T == 3) {
        if (s == 4) { WAITV(5); RAWBAR(); }
        if (s >= 5 && s < 9) { rd(2, s - 5); is(5, s - 5, w5_home(2)); }
        if (s >= 14 && s < 18) is(6, s - 14, STG);
        if (s == 22) {
          WAITV(8); RAWBAR();
          if (tid == 448) __hip_atomic_store((GAS u64*)(ws + WS_FL4) + m4 * 16 + fs, tag, RLX);
        }
      }
    };
    f32x16 acc[2];
    u32x2 stt[2];
    float t[8], u[8], rn;
    auto epi = [&](const f32x16& E, u32x2 ST, int s, int tile) {
      const float mean = __uint_as_float(ST[0]), rstd = __uint_as_float(ST[1]);
      if (s == 0) rn = rstd * -1.4426950408889634f;
      if (s >= 1 && s <= 8) t[s - 1] = __builtin_amdgcn_exp2f(rn * fmaf(-mean, cw[s - 1], E[s - 1]));
      if (s >= 9 && s <= 16) { t[s - 9] = __builtin_amdgcn_rcpf(1.f + t[s - 9]); u[s - 9] = fmaf(-mean, cw[s - 1], E[s - 1]); }
      if (s >= 17 && s <= 24) u[s - 17] = (u[s - 17] * rstd) * t[s - 17];
      if (s == 26) {
        auto r0 = __builtin_amdgcn_permlane32_swap(pk(u[0], u[1]), pk(u[4], u[5]), false, false);
        auto r1 = __builtin_amdgcn_permlane32_swap(pk(u[2], u[3]), pk(u[6], u[7]), false, false);
        st16c(rws, u32x4{r0[0], r1[0], r0[1], r1[1]}, svo, tile * 2 * 56 * 1024);
      }
    };
#pragma unroll
    for (int T = 0; T < 4; ++T) {
      acc[T & 1] = f32x16{};
      stt[T & 1] = *(LAS u32x2*)(sp + 256 * T);
      __builtin_amdgcn_sched_barrier(0);
#pragma unroll
      for (int s = 0; s < 28; ++s) {
        const int n = 28 * T + s;
        acc[T & 1] = mfma32(w4f[s], ring[n & 3], acc[T & 1]);
        if (n + 4 < 112) ring[n & 3] = *(LAS u32x4*)(ap + xoff(n + 4));
        if (T > 0) epi(acc[(T - 1) & 1], stt[(T - 1) & 1], s, T - 1);
        hook(T, s);
        __builtin_amdgcn_sched_barrier(0);
      }
    }
#pragma unroll
    for (int s = 0; s < 27; ++s) epi(acc[1], stt[1], s, 3);
  }
  {
    if (w == 7) wait_flags((GAS u64*)(ws + WS_FL4) + (t64 >> 1) * 16, 14, tag, lane, dead);
    else WAITV(6);
    w5_read(w5f[3], w5_home(0)); w5_read(w5f[4], w5_home(1)); w5_read(w5f[5], w5_home(2));
    WAITV(0);
    w5_read(w5f[6], STG);
    BARRIER();
    if (tid == 448) __hip_atomic_store((GAS u64*)(ws + WS_FL5) + m4 * 16 + fs, tag, RLX);
    {
      u32x4 xf[4][7];
      const int r2 = c7 & 1;
      __builtin_amdgcn_sched_barrier(0);
#pragma unroll
      for (int k = 0; k < 14; ++k) xf[k / 7][k % 7] = ld16c(rws, lane * 16, WS_HH + ((mt0 + ((k / 7) ^ r2)) * 56 + 7 * w + k % 7) * 1024);
      __builtin_amdgcn_sched_barrier(0);
#pragma unroll
      for (int i = 0; i < 4; ++i) {
        const int mt = (i & 2) | ((i & 1) ^ r2);
        f32x4 a[4] = {z4, z4, z4, z4};
#pragma unroll
        for (int s = 0; s < 7; ++s)
#pragma unroll
          for (int n = 0; n < 4; ++n) a[n] = mfma(w5f[s][n], xf[i][s], a[n]);
        __builtin_amdgcn_sched_barrier(0);
        if (i == 0) {
          if (w == 7) wait_flags((GAS u64*)(ws + WS_FL5) + (t64 >> 1) * 16, 14, tag, lane, dead);
          RAWBAR();
#pragma unroll
          for (int k = 0; k < 14; ++k) xf[2 + k / 7][k % 7] = ld16c(rws, lane * 16, WS_HH + ((mt0 + 4 + ((k / 7) ^ r2)) * 56 + 7 * w + k % 7) * 1024);
        }
        __builtin_amdgcn_sched_barrier(0);
#pragma unroll
        for (int n = 0; n < 4; ++n) *(LAS f32x4*)(smem + (((n * 4 + mt) * 8 + w) * 64 + lane) * 16) = a[n];
      }
    }
    BARRIER();
    LAS f32x4* rp = (LAS f32x4*)(smem + (w * 2 * 8 * 64 + lane) * 16);
#pragma unroll
    for (int i = 0; i < 8; ++i) { hA += rp[i * 64]; hB += rp[512 + i * 64]; }
    u32x4 ov = pair16(pk(hA[0], hA[1]), pk(hA[2], hA[3]), pk(hB[0], hB[1]), pk(hB[2], hB[3]));
    GAS char* op = (GAS char*)out + (((mt0 + 4 * mp + (g & 1)) * 16 + c16) * D_ + 64 * cs + 16 * nt + 8 * (g >> 1)) * 2;
    *(GAS u32x4*)op = ov;
  }
  asm volatile(".Lcp_end:");
}

extern "C" void kernel_launch(void* const* d_in, const int* in_sizes, int n_in, void* d_out, int out_size, void* d_ws,
                              size_t ws_size, hipStream_t stream) {
  (void)hipFuncSetAttribute((const void*)fused_block, hipFuncAttributeMaxDynamicSharedMemorySize, LDS_BYTES);
  fused_block<<<256, 512, LDS_BYTES, stream>>>((const uint16_t*)d_in[0], (const uint16_t*)d_in[1], (const uint16_t*)d_in[2],
                                               (const uint16_t*)d_in[3], (const uint16_t*)d_in[5], (const uint16_t*)d_in[6],
                                               (const uint16_t*)d_in[7], (uint16_t*)d_out, (char*)d_ws);
}
```

```cpp
#include <hip/hip_runtime.h>
#include <stdint.h>

#define DEVI __device__ __forceinline__
#define LAS __attribute__((address_space(3)))
#define GAS __attribute__((address_space(1)))
typedef unsigned u32;
typedef unsigned long long u64;
typedef u32 u32x2 __attribute__((ext_vector_type(2)));
typedef u32 u32x4 __attribute__((ext_vector_type(4)));
typedef float f32x4 __attribute__((ext_vector_type(4)));
typedef float f32x16 __attribute__((ext_vector_type(16)));
typedef __bf16 bf16x8 __attribute__((ext_vector_type(8)));
typedef __bf16 bf16x2 __attribute__((ext_vector_type(2)));
typedef short s16x4 __attribute__((ext_vector_type(4)));
typedef __amdgpu_buffer_rsrc_t rsrc_t;

constexpr int NF = 7, N2 = 5;
constexpr int S_ = 2048, D_ = 448, F_ = 1792;
constexpr u32 WS_EPOCH = 0;
constexpr u32 WS_FL1 = 4096, WS_FL2 = WS_FL1 + 16 * 128, WS_FL3 = WS_FL2 + 16 * 128, WS_FL4 = WS_FL3 + 16 * 128, WS_FL5 = WS_FL4 + 16 * 128;
constexpr u32 WS_KVP = 65536;
constexpr u32 WS_O = WS_KVP + 16 * 16 * 8192;
constexpr u32 WS_H = WS_O + 128 * 16 * 1024;
constexpr u32 WS_HH = WS_H + 128 * 14 * 1024;
constexpr u32 WS_ST = WS_HH + 128 * 56 * 1024;
constexpr u32 WS_END = WS_ST + 7 * 2048 * 8;
constexpr u32 SLOT = 28672;
constexpr u32 L_RAW = 4 * SLOT;
constexpr u32 L_PHI = SLOT, L_V = SLOT + 16384;
constexpr u32 L_MISC = 147456;
constexpr u32 L_KVF = L_MISC, L_WPHI = L_MISC + 4096, L_CS = L_MISC + 6144  , L_ST = L_MISC + 12288  , L_PS = L_MISC + 13312  , L_DUMMY = L_MISC + 15360;
constexpr u32 LDS_BYTES = 163840;

DEVI f32x4 mfma(u32x4 a, u32x4 b, f32x4 c) {
  return __builtin_amdgcn_mfma_f32_16x16x32_bf16(__builtin_bit_cast(bf16x8, a), __builtin_bit_cast(bf16x8, b), c, 0, 0, 0);
}
DEVI f32x16 mfma32(u32x4 a, u32x4 b, f32x16 c) {
  return __builtin_amdgcn_mfma_f32_32x32x16_bf16(__builtin_bit_cast(bf16x8, a), __builtin_bit_cast(bf16x8, b), c, 0, 0, 0);
}
DEVI float vmax(float a, float b) { return __builtin_elementwise_maximum(a, b); }
DEVI float vmin(float a, float b) { return __builtin_elementwise_minimum(a, b); }
DEVI u32 pk(float a, float b) { bf16x2 v = {(__bf16)a, (__bf16)b}; return __builtin_bit_cast(u32, v); }
DEVI float bflo(u32 v) { return __uint_as_float(v << 16); }
DEVI float bfhi(u32 v) { return __uint_as_float(v & 0xffff0000u); }
DEVI u32x2 trr(LAS char* p) { return __builtin_bit_cast(u32x2, __builtin_amdgcn_ds_read_tr16_b64_v4i16((LAS s16x4*)p)); }
DEVI u32x4 tr2(LAS char* p0, LAS char* p1) { u32x2 a = trr(p0), b = trr(p1); u32x4 r = {a[0], a[1], b[0], b[1]}; return r; }
DEVI float dot2(u32 a, u32 b, float c) {
  return __builtin_amdgcn_fdot2_f32_bf16(__builtin_bit_cast(bf16x2, a), __builtin_bit_cast(bf16x2, b), c, false);
}
DEVI float x16(float v, float& o) {
  auto r = __builtin_amdgcn_permlane16_swap(__float_as_uint(v), __float_as_uint(v), false, false);
  o = __uint_as_float(r[1]); return __uint_as_float(r[0]);
}
DEVI float x32(float v, float& o) {
  auto r = __builtin_amdgcn_permlane32_swap(__float_as_uint(v), __float_as_uint(v), false, false);
  o = __uint_as_float(r[1]); return __uint_as_float(r[0]);
}
DEVI float gsum(float v) { float b, a = x16(v, b); v = a + b; a = x32(v, b); return a + b; }
DEVI rsrc_t mkrs(const void* p, u32 bytes) { return __builtin_amdgcn_make_buffer_rsrc((void*)p, 0, bytes, 0x00020000); }
DEVI u32x4 mkd(const void* p, u32 bytes) {
  u64 a = (u64)p; u32x4 d = {(u32)a, (u32)(a >> 32) & 0xffffu, bytes, 0x00020000u}; return d;
}
DEVI void dma16(u32x4 d, u32 lds, u32 voff, u32 soff) {
  asm volatile("s_mov_b32 m0, %0\n\ts_nop 0\n\tbuffer_load_dwordx4 %1, %2, %3 offen lds" ::"s"(lds), "v"(voff), "s"(d), "s"(soff) : "memory", "m0");
}
DEVI void dma16c(u32x4 d, u32 lds, u32 voff, u32 soff) {
  asm volatile("s_mov_b32 m0, %0\n\ts_nop 0\n\tbuffer_load_dwordx4 %1, %2, %3 offen sc1 lds" ::"s"(lds), "v"(voff), "s"(d), "s"(soff) : "memory", "m0");
}
DEVI void touch(u32x4 d, u32 voff) {
  asm volatile("s_mov_b32 m0, %0\n\ts_nop 0\n\tbuffer_load_dword %1, %2, 0 offen lds" ::"s"(L_DUMMY), "v"(voff), "s"(d) : "memory", "m0");
}
#define RAWBAR() do { asm volatile("" ::: "memory"); __builtin_amdgcn_s_barrier(); asm volatile("" ::: "memory"); } while (0)
#define WAITV(n) asm volatile("s_waitcnt vmcnt(" #n ")" ::: "memory")
DEVI u32x4 ld16c(rsrc_t rs, u32 voff, u32 soff) { return __builtin_amdgcn_raw_buffer_load_b128(rs, voff, soff, 16); }
DEVI void st16c(rsrc_t rs, u32x4 v, u32 voff, u32 soff) { __builtin_amdgcn_raw_buffer_store_b128(v, rs, voff + soff, 0, 16); }
#define WAITV0() asm volatile("s_waitcnt vmcnt(0)" ::: "memory")
#define BARRIER() do { asm volatile("s_waitcnt lgkmcnt(0)" ::: "memory"); __builtin_amdgcn_s_barrier(); asm volatile("" ::: "memory"); } while (0)
#define RLX __ATOMIC_RELAXED, __HIP_MEMORY_SCOPE_AGENT

DEVI void wait_flags(GAS u64* fl, int n, u64 tag, int lane, int& dead) {
  int ln = lane < n ? lane : 0;
  for (u32 spins = 0; !dead; ++spins) {
    u64 v = __hip_atomic_load(fl + ln, RLX);
    if (__all(v == tag)) break;
    if (spins > (1u << 17)) dead = 1;
    __builtin_amdgcn_s_sleep(1);
  }
}

template <class OP>
DEVI void red2(float& a, float& b, OP op) {
  auto r = __builtin_amdgcn_permlane16_swap(__float_as_uint(a), __float_as_uint(b), false, false);
  float c = op(__uint_as_float(r[0]), __uint_as_float(r[1]));
  float d, e = x32(c, d);
  c = op(e, d);
  a = x16(c, b);
}
template <class HK>
DEVI void dual_softmax(const f32x4& p0, f32x4 p1, bool g3, u32x4& pos, u32x4& neg, HK hk) {
  const float L2E = 1.4426950408889634f;
  if (g3) p1 = p0;
  float mx = vmax(vmax(vmax(p0[0], p0[1]), vmax(p0[2], p0[3])), vmax(vmax(p1[0], p1[1]), vmax(p1[2], p1[3])));
  float mn = vmin(vmin(vmin(p0[0], p0[1]), vmin(p0[2], p0[3])), vmin(vmin(p1[0], p1[1]), vmin(p1[2], p1[3])));
  mn = -mn;
  red2(mx, mn, [](float u, float v) { return vmax(u, v); });
  hk(0);
  const float ca = -mx * L2E, cb = -mn * L2E;
  const float ca1 = g3 ? -__builtin_inff() : ca, cb1 = g3 ? -__builtin_inff() : cb;
  float a0[4], a1[4], b0[4], b1[4];
#pragma unroll
  for (int r = 0; r < 4; ++r) {
    a0[r] = __builtin_amdgcn_exp2f(fmaf(p0[r], L2E, ca));
    b0[r] = __builtin_amdgcn_exp2f(fmaf(p0[r], -L2E, cb));
    a1[r] = __builtin_amdgcn_exp2f(fmaf(p1[r], L2E, ca1));
    b1[r] = __builtin_amdgcn_exp2f(fmaf(p1[r], -L2E, cb1));
  }
  float sa = ((a0[0] + a0[1]) + (a0[2] + a0[3])) + ((a1[0] + a1[1]) + (a1[2] + a1[3]));
  float sb = ((b0[0] + b0[1]) + (b0[2] + b0[3])) + ((b1[0] + b1[1]) + (b1[2] + b1[3]));
  hk(1);
  red2(sa, sb, [](float u, float v) { return u + v; });
  sa = __builtin_amdgcn_rcpf(sa); sb = __builtin_amdgcn_rcpf(sb);
  hk(2);
  pos[0] = pk(a0[0] * sa, a0[1] * sa); pos[1] = pk(a0[2] * sa, a0[3] * sa);
  pos[2] = pk(a1[0] * sa, a1[1] * sa); pos[3] = pk(a1[2] * sa, a1[3] * sa);
  neg[0] = pk(b0[0] * sb, b0[1] * sb); neg[1] = pk(b0[2] * sb, b0[3] * sb);
  neg[2] = pk(b1[0] * sb, b1[1] * sb); neg[3] = pk(b1[2] * sb, b1[3] * sb);
}

DEVI u32x4 pair16(u32 a0, u32 a1, u32 b0, u32 b1) {
  auto r0 = __builtin_amdgcn_permlane16_swap(a0, b0, false, false);
  auto r1 = __builtin_amdgcn_permlane16_swap(a1, b1, false, false);
  u32x4 o = {r0[0], r1[0], r0[1], r1[1]};
  return o;
}

__global__ __launch_bounds__(512, 2) void fused_block(const uint16_t* __restrict__ x, const uint16_t* __restrict__ wqkv,
                                                      const uint16_t* __restrict__ wphi, const uint16_t* __restrict__ wo,
                                                      const uint16_t* __restrict__ wg, const uint16_t* __restrict__ wu,
                                                      const uint16_t* __restrict__ wd, uint16_t* __restrict__ out,
                                                      char* __restrict__ ws_) {
  extern __shared__ __attribute__((aligned(16))) char smem_[];
  LAS char* smem = (LAS char*)smem_;
  asm volatile("" ::"s"(x), "s"(wqkv), "s"(wphi), "s"(wo), "s"(wg), "s"(wu), "s"(wd), "s"(out), "s"(ws_));
  GAS char* ws = (GAS char*)ws_;
  const int tid = threadIdx.x, lane = tid & 63;
  const int w = __builtin_amdgcn_readfirstlane(tid >> 6);
  const int g = lane >> 4, c16 = lane & 15, q = (lane >> 2) & 3, p = lane & 3;
  const bool g3 = (g == 3);
  const int b = blockIdx.x, xc = b & 7, rr = b >> 3;
  const rsrc_t rws = mkrs(ws_, WS_END);
  int dead = 0;
  const u32x4 ones = {0x3f803f80u, 0x3f803f80u, 0x3f803f80u, 0x3f803f80u};
  const f32x4 z4 = {0.f, 0.f, 0.f, 0.f};
  const u32 phi_sw = (q >> 1) | ((g & 1) << 1);
  const bool worker = b < 224;
  const int h = 4 * (xc & 3) + (rr & 3), m = 8 * (xc >> 2) + (rr >> 2);
  const int r7 = worker ? rr / 7 : 0, c7 = worker ? rr % 7 : 0;
  const int t64 = 4 * xc + r7, cs = c7;
  const int mt0 = (t64 >> 1) * 8 + 2 * (t64 & 1);
  const int m4 = 4 * (xc >> 1) + r7, fs = 7 * (xc & 1) + c7;
  const int mp = w & 1, nt = w >> 1;
  const u32x4 dX = mkd(x, S_ * D_ * 2), dW1 = mkd(wqkv, D_ * 3 * D_ * 2), dW3 = mkd(wo, D_ * D_ * 2);
  const u32x4 dG = mkd(wg, D_ * F_ * 2), dU = mkd(wu, D_ * F_ * 2), dD = mkd(wd, F_ * D_ * 2), dWS = mkd(ws_, WS_END);

  u64 tag;
  u32x2 xra, xrb;
  u32 v4[2];
#pragma unroll
  for (int v = 0; v < 2; ++v) v4[v] = (lane >> 4) * 3584 + 32 * (((lane >> 1) & 7) ^ ((lane >> 4) | (v << 2))) + 16 * (lane & 1) + 256 * fs;
  const u32 b4 = 8192 * (g & 1) + 2048 * (g >> 1) + 256 * q + 32 * ((u32)w ^ (q | ((g & 1) << 2))) + 8 * p;
  const int rw4 = (w + 2 * r7) & 7;
  u32x4 w4f[28];
  f32x16 cw = {};
  auto w4_issue = [&](int u, int slot) {
    dma16(dG, slot * 16384 + rw4 * 1024, v4[0], (u32)(8 * u + rw4) * 4 * 3584);
    dma16(dU, slot * 16384 + 8192 + rw4 * 1024, v4[1], (u32)(8 * u + rw4) * 4 * 3584);
  };
  auto w4_read = [&](int u0, int n) {
#pragma unroll
    for (int k = 0; k < 2 * n; ++k) { LAS char* a = smem + b4 + 16384 * (k >> 1) + 4096 * (k & 1); w4f[2 * u0 + k] = tr2(a, a + 1024); }
  };
  auto w4_sum = [&](int u0, int n) {
#pragma unroll
    for (int k = 0; k < 2 * n; ++k) cw = mfma32(w4f[2 * u0 + k], ones, cw);
    asm volatile("" : "+v"(cw));
  };

  u32x4 phq_pos, phq_neg;
  u32x4 w3f[16];
  const int grp = w >> 2, tg = w & 3;
  const int myMt = 2 * tg + grp;
  {
    const int rot = (2 * (rr & 3) + (rr >> 2)) % 7;
    const u32 win0 = (56 * h) & ~15;
    u32 vx0, vx1, vw[2], v3;
    {
      const int r8 = lane >> 3, pos = lane & 7;
      vx0 = (u32)((m * 128 + w * 16 + r8) * 896 + 16 * (pos ^ (lane >> 4)));
      vx1 = (u32)((m * 128 + w * 16 + 8 + r8) * 896 + 16 * (pos ^ (4 + (lane >> 4))));
#pragma unroll
      for (int k = 0; k < 2; ++k) {
        const int j = w + 8 * k, G = j / 3, rl = 4 * (j - 3 * G) + g, rq = rl / 3, sec = rl - 3 * rq;
        vw[k] = (u32)((16 * G + 4 * rq + q) * 2688 + sec * 896 + win0 + 16 * (p ^ (2 * (rq >> 1))));
      }
      v3 = (u32)(r8 * 896 + 32 * (((lane >> 1) & 3) ^ ((lane >> 4) & 3)) + 16 * (lane & 1) + 128 * cs);
    }
    auto issue_chunk = [&](int i, u32 slot) {
      int c = rot + i; c = c >= 7 ? c - 7 : c;
      dma16(dX, slot + w * 2048, vx0, c * 128);
      dma16(dW1, slot + 16384 + w * 1024, vw[0], c * 64 * 2688);
      dma16(dX, slot + w * 2048 + 1024, vx1, c * 128);
      if (w < 4) dma16(dW1, slot + 24576 + w * 1024, vw[1], c * 64 * 2688);
    };
    auto w3_at = [&](int hd) { return (u32)(hd < 7 ? 86016 + 4096 * hd : hd < 14 ? 4096 * (hd - 7) : 139264 + 4096 * (hd - 14)); };
    auto w3_issue = [&](int k0) {
#pragma unroll
      for (int k = k0; k < k0 + 4; ++k) dma16(dW3, (w < 4 ? w3_at(2 * k) : w3_at(2 * k + 1)) + (w & 3) * 1024, v3, (28 * (2 * k + (w >> 2)) + 8 * (w & 3)) * 896);
    };
    auto ex = [&](int k) {
      __builtin_amdgcn_sched_barrier(0);
      if (k < 7 && w == k) {
        const int l2 = 56 * k + (lane < 56 ? lane : 0), up = l2 >= 196;
        const int idx = rr * 196 + l2 - 196 * up;
        const u32 vo = (u32)((idx / 14) * 3584 + (xc & 1) * 1792 + (idx % 14) * 128);
        if (k <= 3) touch(dG, up ? 0x40000000u : vo);
        if (k >= 3) touch(dU, up ? vo : 0x40000000u);
      }
      __builtin_amdgcn_sched_barrier(0);
    };
    if (w == 7) {
      const u32x4 dP = mkd(wphi, 16 * 784 * 2);
      dma16(dP, L_WPHI, lane * 16, h * 1568); dma16(dP, L_WPHI + 1024, lane * 16, h * 1568 + 1024);
    }
    issue_chunk(0, 0); RAWBAR();
    issue_chunk(1, SLOT);
    {
      const GAS char* ep = ws + WS_EPOCH;
      const GAS char* xp = (const GAS char*)x + (((mt0 + 4 * mp) * 16 + c16) * D_ + 64 * cs + 16 * nt + 4 * g) * 2;
      asm volatile("global_load_dwordx2 %0, %3, off sc1\n\tglobal_load_dwordx2 %1, %4, off\n\tglobal_load_dwordx2 %2, %5, off"
                   : "=&v"(tag), "=&v"(xra), "=&v"(xrb) : "v"(ep), "v"(xp), "v"(xp + 16 * D_ * 2) : "memory");
    }
    if (w == 6) {
      u64 pc; u32 len;
      asm volatile("s_getpc_b64 %0\n.Lcp_start:\n\ts_mov_b32 %1, .Lcp_end-.Lcp_start" : "=s"(pc), "=s"(len));
      const u32x4 dC = {(u32)pc, (u32)(pc >> 32) & 0xffffu, len, 0x00020000u};
      touch(dC, (u32)(((rr & 7) * 64 + lane) * 128));
    }
    u32 wb[3];
#pragma unroll
    for (int dt = 0; dt < 2; ++dt) {
      u32 u = 8 * (h & 1) + 32 * dt + 8 * p; if (u >= 64) u = 56;
      wb[dt] = 16384 + 1536 * g + 64 * q + (u ^ (32 * (g & 1)));
    }
    wb[2] = grp ? wb[1] : wb[0];
    wb[0] += 256 * (1 + grp); wb[1] += 256 * (1 + grp);
    const u32 xb = 128 * c16 + 16 * (g ^ ((c16 >> 1) & 7)) + myMt * 2048;
    u32x4 wpf[2];
    f32x4 A[3] = {z4, z4, z4}, B[3] = {z4, z4, z4}, C = z4;
    f32x4 S1 = z4, S2 = z4;
    u32x4 x0[2][2], x1[2][2], wf[2][2][3];
#pragma unroll
    for (int i = 0; i < 8; ++i) {
      if (i < 7) {
        if (i < 2) WAITV(6); else if (i < 6) WAITV(3); else WAITV(4);
        BARRIER();
      }
      if (i <= 4 && grp) issue_chunk(i + 2, ((i + 2) & 3) * SLOT);
      if ((i == 5 || i == 6) && grp) w3_issue(4 * (i - 5));
      __builtin_amdgcn_sched_barrier(0);
      if (i < 7) {
        LAS char* sl = smem + (i & 3) * SLOT;
#pragma unroll
        for (int e = 0; e < 2; ++e) {
          x0[i & 1][e] = *(LAS u32x4*)(sl + (xb ^ (64 * e)));
#pragma unroll
          for (int n = 0; n < 3; ++n) wf[i & 1][e][n] = tr2(sl + wb[n] + 6144 * e, sl + wb[n] + 6144 * e + 768);
          x1[i & 1][e] = *(LAS u32x4*)(sl + (xb ^ (64 * e) ^ 2048));
        }
        __builtin_amdgcn_sched_barrier(0);
      }
      if (i > 0) {
        const int c = (i - 1) & 1;
#pragma unroll
        for (int e = 0; e < 2; ++e) {
#pragma unroll
          for (int n = 0; n < 3; ++n) { A[n] = mfma(wf[c][e][n], x0[c][e], A[n]); B[n] = mfma(wf[c][e][n], x1[c][e], B[n]); }
          S1 = mfma(ones, x0[c][e], S1);
          S2 = mfma(x0[c][e], x0[c][e], S2);
        }
        asm volatile("" : "+v"(A[0]), "+v"(A[1]), "+v"(A[2]), "+v"(B[0]), "+v"(B[1]), "+v"(B[2]), "+v"(S1), "+v"(S2));
#pragma unroll
        for (int n = 0; n < 3; ++n)
          if (tg == n) { C = mfma(wf[c][0][n], ones, C); C = mfma(wf[c][1][n], ones, C); }
      }
      __builtin_amdgcn_sched_barrier(0);
      if (i <= 4 && !grp) issue_chunk(i + 2, ((i + 2) & 3) * SLOT);
      if ((i == 5 || i == 6) && !grp) w3_issue(4 * (i - 5));
      if (i == 6) ex(0);
      if (i == 7) ex(1);
      __builtin_amdgcn_sched_barrier(0);
      if (i == 0) {
        LAS char* a = smem + L_WPHI + 56 * (4 * g + q) + 8 * p;
        wpf[0] = tr2(a, a + 896); wpf[1] = tr2(a + 32, a + 32 + 896);
        asm volatile("" : "+v"(wpf[0]), "+v"(wpf[1]));
        const u32 mg = g3 ? 0u : ~0u, mf = c16 >= 12 ? 0u : ~0u;
        wpf[0][2] &= mg; wpf[0][3] &= mg; wpf[1][0] &= mf; wpf[1][1] &= mf; wpf[1][2] &= mg & mf; wpf[1][3] &= mg & mf;
        asm volatile("" : "+v"(wpf[0]), "+v"(wpf[1]));
      }
      if (i == 4) asm volatile("" : "+v"(tag), "+v"(xra), "+v"(xrb));
    }
    tag += 1;
    {
      LAS f32x4* rp = (LAS f32x4*)(smem + L_RAW + w * 3072) + lane;
      rp[0] = B[0]; rp[64] = B[1]; rp[128] = B[2];
      if (tg < 3 && c16 == 0) *(LAS f32x4*)(smem + L_CS + (tg < 2 ? 2 * grp + tg : 4 + grp) * 64 + 16 * g) = C;
    }
    ex(2);
    const float mean = S1[0] * (1.f / D_);
    const float dg = p == 0 ? S2[0] : p == 1 ? S2[1] : p == 2 ? S2[2] : S2[3];
    const float rstd = __builtin_amdgcn_rsqf(fmaxf(gsum(g == q ? dg : 0.f) * (1.f / D_) - mean * mean, 0.f) + 1e-5f);
    BARRIER();
    LAS f32x4* cp = (LAS f32x4*)(smem + L_CS + 16 * g);
    f32x4 r2;
    {
      LAS f32x4* rp = (LAS f32x4*)(smem + L_RAW + (w ^ 4) * 3072) + lane;
      f32x4 r0 = rp[0], r1 = rp[64];
      r2 = rp[128];
      ex(3);
      f32x4 acc[4] = {grp ? r0 : A[0], grp ? r1 : A[1], grp ? A[0] : r0, grp ? A[1] : r1};
#pragma unroll
      for (int n = 0; n < 4; ++n) {
        const f32x4 c = cp[4 * n];
#pragma unroll
        for (int r = 0; r < 4; ++r) acc[n][r] = rstd * (acc[n][r] - mean * c[r]);
      }
      u32x4 kf = {pk(acc[0][0], acc[0][1]), pk(acc[0][2], acc[0][3]), g3 ? 0u : pk(acc[1][0], acc[1][1]), g3 ? 0u : pk(acc[1][2], acc[1][3])};
      f32x4 p0 = mfma(wpf[0], kf, z4), p1 = mfma(wpf[1], kf, z4);
      ex(4);
      LAS char* vr = smem + L_V + 64 * (16 * myMt + c16) + 8 * g;
      const u32 vs = (c16 >> 3) & 1;
      *(LAS u32x2*)(vr + 32 * (0 ^ vs)) = u32x2{pk(acc[2][0], acc[2][1]), pk(acc[2][2], acc[2][3])};
      *(LAS u32x2*)(vr + 32 * (1 ^ vs)) = g3 ? u32x2{0x00003f80u, 0u} : u32x2{pk(acc[3][0], acc[3][1]), pk(acc[3][2], acc[3][3])};
      u32x4 pos, neg;
      dual_softmax(p0, p1, g3, pos, neg, [&](int k) { ex(5 + k); });
      const u32 rowsw = ((c16 >> 1) & 1) | (((c16 >> 3) & 1) << 1);
      LAS char* pr = smem + L_PHI + 128 * (16 * myMt + c16) + 8 * g;
      *(LAS u32x2*)(pr + 32 * (0 ^ rowsw)) = u32x2{pos[0], pos[1]};
      *(LAS u32x2*)(pr + 32 * (1 ^ rowsw)) = u32x2{pos[2], pos[3]};
      *(LAS u32x2*)(pr + 32 * (2 ^ rowsw)) = u32x2{neg[0], neg[1]};
      *(LAS u32x2*)(pr + 32 * (3 ^ rowsw)) = u32x2{neg[2], neg[3]};
    }
    BARRIER();
    {
      LAS char* ap = smem + L_PHI + 1024 * g + 128 * q + 32 * ((u32)(w >> 1) ^ phi_sw) + 8 * p;
      LAS char* bp = smem + L_V + 512 * g + 64 * q + 32 * ((w & 1) ^ (g & 1)) + 8 * p;
      f32x4 kv = z4;
#pragma unroll
      for (int s = 0; s < 4; ++s) kv = mfma(tr2(ap + 4096 * s, ap + 4096 * s + 512), tr2(bp + 2048 * s, bp + 2048 * s + 256), kv);
      __builtin_amdgcn_raw_buffer_store_b64(u32x2{pk(kv[0], kv[1]), pk(kv[2], kv[3])}, rws, WS_KVP + ((h * 16 + m) * 8 + w) * 512 + lane * 8, 0, 16);
    }
    WAITV0();
    BARRIER();
    if (tid == 448) __hip_atomic_store((GAS u64*)(ws + WS_FL1) + h * 16 + m, tag, RLX);
    {
      LAS char* bp = smem + 128 * (4 * g + q) + 32 * ((u32)nt ^ phi_sw) + 8 * p;
#pragma unroll
      for (int s = 0; s < 16; ++s) w3f[s] = tr2(bp + w3_at(s), bp + w3_at(s) + 2048);
    }
    BARRIER();
    {
      auto gd = [&](int u) {
        __builtin_amdgcn_sched_barrier(0);
        if (u < NF && worker) w4_issue(u, u);
        if (u >= 3 && u + 2 < NF && worker) w4_issue(u + 2, u + 2);
        __builtin_amdgcn_sched_barrier(0);
      };
      gd(0);
      f32x4 q0 = grp ? r2 : A[2], q1 = grp ? A[2] : r2;
      const f32x4 c0 = cp[16], c1 = cp[20];
#pragma unroll
      for (int r = 0; r < 4; ++r) { q0[r] = rstd * (q0[r] - mean * c0[r]); q1[r] = rstd * (q1[r] - mean * c1[r]); }
      u32x4 qf = {pk(q0[0], q0[1]), pk(q0[2], q0[3]), g3 ? 0u : pk(q1[0], q1[1]), g3 ? 0u : pk(q1[2], q1[3])};
      f32x4 p0 = mfma(wpf[0], qf, z4), p1 = mfma(wpf[1], qf, z4);
      gd(1);
      dual_softmax(p0, p1, g3, phq_pos, phq_neg, [&](int k) { gd(2 + k); });
    }
  }
  {
    if (w == 7) wait_flags((GAS u64*)(ws + WS_FL1) + h * 16, 16, tag, lane, dead);
    BARRIER();
    {
      u32x2 pt[16];
#pragma unroll
      for (int i = 0; i < 16; ++i)
        pt[i] = __builtin_amdgcn_raw_buffer_load_b64(rws, lane * 8, WS_KVP + ((h * 16 + ((i + 2 * m) & 15)) * 8 + w) * 512, 16);
      float kv[4] = {bflo(pt[0][0]), bfhi(pt[0][0]), bflo(pt[0][1]), bfhi(pt[0][1])};
#pragma unroll
      for (int i = 1; i < 16; ++i) { kv[0] += bflo(pt[i][0]); kv[1] += bfhi(pt[i][0]); kv[2] += bflo(pt[i][1]); kv[3] += bfhi(pt[i][1]); }
      *(LAS u32x2*)(smem + L_KVF + (((w >> 2) * 2 + (w & 1)) * 64 + lane) * 16 + 8 * ((w >> 1) & 1)) = u32x2{pk(kv[0], kv[1]), pk(kv[2], kv[3])};
    }
    WAITV0(); BARRIER();
    f32x4 o0 = z4, o1 = z4;
    LAS u32x4* kf = (LAS u32x4*)(smem + L_KVF) + lane;
    o0 = mfma(kf[0], phq_pos, o0); o1 = mfma(kf[64], phq_pos, o1);
    o0 = mfma(kf[128], phq_neg, o0); o1 = mfma(kf[192], phq_neg, o1);
    float t0, den = o1[0];
    x32(den, t0); x16(t0, den);
    const float inv = __builtin_amdgcn_rcpf(den + 1e-6f);
    u32x4 of = {pk(o0[0] * inv, o0[1] * inv), pk(o0[2] * inv, o0[3] * inv), g3 ? 0u : pk(o1[0] * inv, o1[1] * inv), g3 ? 0u : pk(o1[2] * inv, o1[3] * inv)};
    st16c(rws, of, lane * 16, WS_O + ((m * 8 + myMt) * 16 + h) * 1024);
    if (worker) w4_read(0, NF);
    WAITV0(); BARRIER();
    if (tid == 448) __hip_atomic_store((GAS u64*)(ws + WS_FL2) + m * 16 + h, tag, RLX);
  }
  if (!worker) {
#pragma unroll 1
    for (int k = 0; k < 7; ++k) {
      const int r = (rr - 28) * 64 + lane;
      if (w < 7) touch(dD, (u32)((224 * (r >> 5) + 32 * k + (r & 31)) * 896 + w * 128));
    }
    WAITV0();
    return;
  }
#pragma unroll
  for (int i = 0; i < N2; ++i) w4_issue(NF + i, i);
  w4_sum(0, NF);
  f32x4 hA, hB;
  {
    if (w == 7) wait_flags((GAS u64*)(ws + WS_FL2) + (t64 >> 1) * 16, 16, tag, lane, dead);
    BARRIER();
    if (b == 0 && tid == 448) __hip_atomic_store((GAS u64*)(ws + WS_EPOCH), tag, RLX);
    {
      const int rb = (w + c7) & 7;
#pragma unroll
      for (int i = 0; i < 8; ++i) {
        const int tl = i & 3, hd = 8 * (i >> 2) + rb;
        dma16c(dWS, 81920 + (16 * tl + hd) * 1024, lane * 16, WS_O + ((mt0 + (tl & 1) + 4 * (tl >> 1)) * 16 + hd) * 1024);
      }
    }
    hA = f32x4{bflo(xra[0]), bfhi(xra[0]), bflo(xra[1]), bfhi(xra[1])};
    hB = f32x4{bflo(xrb[0]), bfhi(xrb[0]), bflo(xrb[1]), bfhi(xrb[1])};
    LAS char* ap = smem + 81920 + mp * 32768 + lane * 16;
#pragma unroll
    for (int hf = 0; hf < 2; ++hf) {
      if (hf == 0) WAITV(4); else WAITV(0);
      RAWBAR();
      u32x4 xa[8], xb2[8];
#pragma unroll
      for (int s = 0; s < 8; ++s) { xa[s] = *(LAS u32x4*)(ap + 1024 * (8 * hf + s)); xb2[s] = *(LAS u32x4*)(ap + 16384 + 1024 * (8 * hf + s)); }
      __builtin_amdgcn_sched_barrier(0);
#pragma unroll
      for (int s = 0; s < 8; ++s) { hA = mfma(w3f[8 * hf + s], xa[s], hA); hB = mfma(w3f[8 * hf + s], xb2[s], hB); }
      __builtin_amdgcn_sched_barrier(0);
    }
    const u32 a0 = pk(hA[0], hA[1]), a1 = pk(hA[2], hA[3]), b0 = pk(hB[0], hB[1]), b1 = pk(hB[2], hB[3]);
    u32x4 hv = pair16(a0, a1, b0, b1);
    st16c(rws, hv, (g & 1) * 14336 + (2 * (nt & 1) + (g >> 1)) * 256 + c16 * 16,
          WS_H + ((mt0 + 4 * mp) * 14 + 2 * cs + (nt >> 1)) * 1024);
    {
      float sA = gsum(dot2(a0, 0x3f803f80u, dot2(a1, 0x3f803f80u, 0.f))), qA = gsum(dot2(a0, a0, dot2(a1, a1, 0.f)));
      float sB = gsum(dot2(b0, 0x3f803f80u, dot2(b1, 0x3f803f80u, 0.f))), qB = gsum(dot2(b0, b0, dot2(b1, b1, 0.f)));
      if (g < 2) *(LAS u32x2*)(smem + L_PS + ((mp * 32 + g * 16 + c16) * 4 + nt) * 8) =
          g ? u32x2{__float_as_uint(sB), __float_as_uint(qB)} : u32x2{__float_as_uint(sA), __float_as_uint(qA)};
    }
    BARRIER();
    if (w == 7) {
      LAS f32x4* pp = (LAS f32x4*)(smem + L_PS + lane * 32);
      f32x4 u = pp[0], v = pp[1];
      u32x2 o2 = {__float_as_uint((u[0] + u[2]) + (v[0] + v[2])), __float_as_uint((u[1] + u[3]) + (v[1] + v[3]))};
      __builtin_amdgcn_raw_buffer_store_b64(o2, rws, WS_ST + (cs * 2048 + mt0 * 16 + (lane >> 5) * 64 + (lane & 31)) * 8, 0, 16);
    }
    w4_read(NF, N2);
    WAITV0(); BARRIER();
    if (tid == 448) __hip_atomic_store((GAS u64*)(ws + WS_FL3) + (t64 >> 1) * 16 + (t64 & 1) * 7 + cs, tag, RLX);
  }
  u32 v5[2];
#pragma unroll
  for (int v = 0; v < 2; ++v) v5[v] = (lane >> 3) * 896 + 32 * (((lane >> 1) & 3) ^ (((lane >> 4) & 1) | (v << 1))) + 16 * (lane & 1) + 128 * cs;
  const u32 b5 = 1024 * g + 128 * q + 8 * p;
  const u32 STG = 114688 + 4096 * w;
  u32x4 w5f[7][4];
  auto w5_issue = [&](int ks, u32 dst) {
#pragma unroll
    for (int i = 0; i < 4; ++i) dma16(dD, dst + i * 1024, v5[i & 1], (u32)(224 * w + 32 * ks + 8 * i) * 896);
  };
  auto w5_read = [&](u32x4* f, u32 src) {
#pragma unroll
    for (int n = 0; n < 4; ++n) { LAS char* a = smem + src + b5 + 32 * ((u32)n ^ phi_sw); f[n] = tr2(a, a + 512); }
  };
  auto w5_home = [&](int pair) { return w < 7 ? (u32)(28672 * pair + 4096 * w) : (u32)(L_MISC + 4096 * pair); };
#pragma unroll
  for (int i = 0; i < 14 - NF - N2; ++i) w4_issue(NF + N2 + i, i);
  w5_issue(0, STG);
  w4_sum(NF, N2);
  {
    WAITV0(); BARRIER();
    w4_read(NF + N2, 14 - NF - N2);
    w5_read(w5f[0], STG);
    w4_sum(NF + N2, 14 - NF - N2);
    if (w == 7) wait_flags((GAS u64*)(ws + WS_FL3) + m4 * 16, 14, tag, lane, dead);
    BARRIER();
    const int rb4 = (w + c7) & 7;
    auto hdma = [&](int j, int k) {
      int blk = 8 * k + rb4; if (blk >= 28) blk -= 8;
      dma16c(dWS, (28 * j + blk) * 1024, lane * 16, WS_H + (m4 * 112 + 28 * j + blk) * 1024);
    };
    {
      u32x2 s0, s1;
      {
        const u32 vo = WS_ST + (u32)(g * 2048 + m4 * 128 + w * 16 + c16) * 8;
        const u32 vo1 = g3 ? vo : vo + 4 * 2048 * 8;
        asm volatile("buffer_load_dwordx2 %0, %2, %4, 0 offen sc1\n\tbuffer_load_dwordx2 %1, %3, %4, 0 offen sc1"
                     : "=&v"(s0), "=&v"(s1) : "v"(vo), "v"(vo1), "s"(dWS) : "memory");
      }
#pragma unroll
      for (int k = 0; k < 8; ++k) hdma(k >> 2, k & 3);
      asm volatile("s_waitcnt vmcnt(8)" : "+v"(s0), "+v"(s1)::"memory");
      float sm = __uint_as_float(s0[0]) + (g3 ? 0.f : __uint_as_float(s1[0]));
      float sq = __uint_as_float(s0[1]) + (g3 ? 0.f : __uint_as_float(s1[1]));
      sm = gsum(sm); sq = gsum(sq);
      const float mean = sm * (1.f / D_);
      const float rstd = __builtin_amdgcn_rsqf(fmaxf(sq * (1.f / D_) - mean * mean, 0.f) + 1e-5f);
      if (g == 0) *(LAS u32x2*)(smem + L_ST + (w * 16 + c16) * 8) = u32x2{__float_as_uint(mean), __float_as_uint(rstd)};
    }
    WAITV(4); BARRIER();
    const int ftile = 8 * fs + w;
    const u32 svo = (g & 1) * 57344 + (2 * (ftile & 1) + (g >> 1)) * 256 + c16 * 16 + WS_HH + (m4 * 8 * 56 + (ftile >> 1)) * 1024;
    LAS char* ap = smem + (g & 1) * 14336 + (g >> 1) * 256 + c16 * 16;
    LAS char* sp = smem + L_ST + (lane & 31) * 8;
    auto xoff = [](int n) { return 28672 * (n / 28) + 1024 * ((n % 28) >> 1) + 512 * (n & 1); };
    u32x4 ring[4];
#pragma unroll
    for (int k = 0; k < 4; ++k) ring[k] = *(LAS u32x4*)(ap + xoff(k));
    auto rd = [&](int ks, int n) { LAS char* a = smem + STG + b5 + 32 * ((u32)n ^ phi_sw); w5f[ks][n] = tr2(a, a + 512); };
    auto is = [&](int ks, int i, u32 dst) { dma16(dD, dst + i * 1024, v5[i & 1], (u32)(224 * w + 32 * ks + 8 * i) * 896); };
    auto hook = [&](int T, int s) {
      if (T == 0) {
        if (s >= 1 && s < 9) hdma(2 + ((s - 1) >> 2), (s - 1) & 3);
        if (s >= 10 && s < 14) is(1, s - 10, STG);
        if (s == 20) { WAITV(12); RAWBAR(); }
      }
      if (T == 1) {
        if (s == 20) { WAITV(0); RAWBAR(); }
        if (s >= 21 && s < 25) { rd(1, s - 21); is(3, s - 21, w5_home(0)); }
      }
      if (T == 2) {
        if (s >= 2 && s < 6) is(2, s - 2, STG);
        if (s == 20) RAWBAR();
        if (s >= 21 && s < 25) is(4, s - 21, w5_home(1));
      }
      if (T == 3) {
        if (s == 4) { WAITV(5); RAWBAR(); }
        if (s >= 5 && s < 9) { rd(2, s - 5); is(5, s - 5, w5_home(2)); }
        if (s >= 14 && s < 18) is(6, s - 14, STG);
        if (s == 22) {
          WAITV(8); RAWBAR();
          if (tid == 448) __hip_atomic_store((GAS u64*)(ws + WS_FL4) + m4 * 16 + fs, tag, RLX);
        }
      }
    };
    f32x16 acc[2];
    u32x2 stt[2];
    float t[8], u[8], rn;
    auto epi = [&](const f32x16& E, u32x2 ST, int s, int tile) {
      const float mean = __uint_as_float(ST[0]), rstd = __uint_as_float(ST[1]);
      if (s == 0) rn = rstd * -1.4426950408889634f;
      if (s >= 1 && s <= 8) t[s - 1] = __builtin_amdgcn_exp2f(rn * fmaf(-mean, cw[s - 1], E[s - 1]));
      if (s >= 9 && s <= 16) { t[s - 9] = __builtin_amdgcn_rcpf(1.f + t[s - 9]); u[s - 9] = fmaf(-mean, cw[s - 1], E[s - 1]); }
      if (s >= 17 && s <= 24) u[s - 17] = (u[s - 17] * rstd) * t[s - 17];
      if (s == 26) {
        auto r0 = __builtin_amdgcn_permlane32_swap(pk(u[0], u[1]), pk(u[4], u[5]), false, false);
        auto r1 = __builtin_amdgcn_permlane32_swap(pk(u[2], u[3]), pk(u[6], u[7]), false, false);
        st16c(rws, u32x4{r0[0], r1[0], r0[1], r1[1]}, svo, tile * 2 * 56 * 1024);
      }
    };
#pragma unroll
    for (int T = 0; T < 4; ++T) {
      acc[T & 1] = f32x16{};
      stt[T & 1] = *(LAS u32x2*)(sp + 256 * T);
      __builtin_amdgcn_sched_barrier(0);
#pragma unroll
      for (int s = 0; s < 28; ++s) {
        const int n = 28 * T + s;
        acc[T & 1] = mfma32(w4f[s], ring[n & 3], acc[T & 1]);
        if (n + 4 < 112) ring[n & 3] = *(LAS u32x4*)(ap + xoff(n + 4));
        if (T > 0) epi(acc[(T - 1) & 1], stt[(T - 1) & 1], s, T - 1);
        hook(T, s);
        __builtin_amdgcn_sched_barrier(0);
      }
    }
#pragma unroll
    for (int s = 0; s < 27; ++s) epi(acc[1], stt[1], s, 3);
  }
  {
    if (w == 7) wait_flags((GAS u64*)(ws + WS_FL4) + (t64 >> 1) * 16, 14, tag, lane, dead);
    else WAITV(6);
    w5_read(w5f[3], w5_home(0)); w5_read(w5f[4], w5_home(1)); w5_read(w5f[5], w5_home(2));
    WAITV(0);
    w5_read(w5f[6], STG);
    BARRIER();
    if (tid == 448) __hip_atomic_store((GAS u64*)(ws + WS_FL5) + m4 * 16 + fs, tag, RLX);
    {
      u32x4 xf[4][7];
      const int r2 = c7 & 1;
      __builtin_amdgcn_sched_barrier(0);
#pragma unroll
      for (int k = 0; k < 14; ++k) xf[k / 7][k % 7] = ld16c(rws, lane * 16, WS_HH + ((mt0 + ((k / 7) ^ r2)) * 56 + 7 * w + k % 7) * 1024);
      __builtin_amdgcn_sched_barrier(0);
#pragma unroll
      for (int i = 0; i < 4; ++i) {
        const int mt = (i & 2) | ((i & 1) ^ r2);
        f32x4 a[4] = {z4, z4, z4, z4};
#pragma unroll
        for (int s = 0; s < 7; ++s)
#pragma unroll
          for (int n = 0; n < 4; ++n) a[n] = mfma(w5f[s][n], xf[i][s], a[n]);
        __builtin_amdgcn_sched_barrier(0);
        if (i == 0) {
          if (w == 7) wait_flags((GAS u64*)(ws + WS_FL5) + (t64 >> 1) * 16, 14, tag, lane, dead);
          RAWBAR();
#pragma unroll
          for (int k = 0; k < 14; ++k) xf[2 + k / 7][k % 7] = ld16c(rws, lane * 16, WS_HH + ((mt0 + 4 + ((k / 7) ^ r2)) * 56 + 7 * w + k % 7) * 1024);
        }
        __builtin_amdgcn_sched_barrier(0);
#pragma unroll
        for (int n = 0; n < 4; ++n) *(LAS f32x4*)(smem + (((n * 4 + mt) * 8 + w) * 64 + lane) * 16) = a[n];
      }
    }
    BARRIER();
    LAS f32x4* rp = (LAS f32x4*)(smem + (w * 2 * 8 * 64 + lane) * 16);
#pragma unroll
    for (int i = 0; i < 8; ++i) { hA += rp[i * 64]; hB += rp[512 + i * 64]; }
    u32x4 ov = pair16(pk(hA[0], hA[1]), pk(hA[2], hA[3]), pk(hB[0], hB[1]), pk(hB[2], hB[3]));
    GAS char* op = (GAS char*)out + (((mt0 + 4 * mp + (g & 1)) * 16 + c16) * D_ + 64 * cs + 16 * nt + 8 * (g >> 1)) * 2;
    *(GAS u32x4*)op = ov;
  }
  asm volatile(".Lcp_end:");
}

extern "C" void kernel_launch(void* const* d_in, const int* in_sizes, int n_in, void* d_out, int out_size, void* d_ws,
                              size_t ws_size, hipStream_t stream) {
  (void)hipFuncSetAttribute((const void*)fused_block, hipFuncAttributeMaxDynamicSharedMemorySize, LDS_BYTES);
  fused_block<<<256, 512, LDS_BYTES, stream>>>((const uint16_t*)d_in[0], (const uint16_t*)d_in[1], (const uint16_t*)d_in[2],
                                               (const uint16_t*)d_in[3], (const uint16_t*)d_in[5], (const uint16_t*)d_in[6],
                                               (const uint16_t*)d_in[7], (uint16_t*)d_out, (char*)d_ws);
}
```

```cpp
#define PROBE2 1
#define PB 0
#define PTHR 3000
#define CODE_PULL
#include <hip/hip_runtime.h>
#include <stdint.h>

#define DEVI __device__ __forceinline__
#define LAS __attribute__((address_space(3)))
#define GAS __attribute__((address_space(1)))
typedef unsigned u32;
typedef unsigned long long u64;
typedef u32 u32x2 __attribute__((ext_vector_type(2)));
typedef u32 u32x4 __attribute__((ext_vector_type(4)));
typedef float f32x4 __attribute__((ext_vector_type(4)));
typedef float f32x16 __attribute__((ext_vector_type(16)));
typedef __bf16 bf16x8 __attribute__((ext_vector_type(8)));
typedef __bf16 bf16x2 __attribute__((ext_vector_type(2)));
typedef short s16x4 __attribute__((ext_vector_type(4)));
typedef __amdgpu_buffer_rsrc_t rsrc_t;

#define NF 7
#define N2 5
constexpr int S_ = 2048, D_ = 448, F_ = 1792;
constexpr u32 WS_EPOCH = 0;
constexpr u32 WS_FL1 = 4096, WS_FL2 = WS_FL1 + 16 * 128, WS_FL3 = WS_FL2 + 16 * 128, WS_FL4 = WS_FL3 + 16 * 128, WS_FL5 = WS_FL4 + 16 * 128;
constexpr u32 WS_KVP = 65536;
constexpr u32 WS_O = WS_KVP + 16 * 16 * 8192;
constexpr u32 WS_H = WS_O + 128 * 16 * 1024;
constexpr u32 WS_HH = WS_H + 128 * 14 * 1024;
constexpr u32 WS_ST = WS_HH + 128 * 56 * 1024;
constexpr u32 WS_END = WS_ST + 7 * 2048 * 8;
constexpr u32 WS_STAMP = WS_END;
constexpr u32 SLOT = 28672;
constexpr u32 L_RAW = 4 * SLOT;
constexpr u32 L_PHI = SLOT, L_V = SLOT + 16384;
constexpr u32 L_MISC = 147456;
constexpr u32 L_KVF = L_MISC, L_WPHI = L_MISC + 4096, L_CS = L_MISC + 6144  , L_ST = L_MISC + 12288  , L_PS = L_MISC + 13312  , L_DUMMY = L_MISC + 15360;
constexpr u32 LDS_BYTES = 163840;

DEVI f32x4 mfma(u32x4 a, u32x4 b, f32x4 c) {
  return __builtin_amdgcn_mfma_f32_16x16x32_bf16(__builtin_bit_cast(bf16x8, a), __builtin_bit_cast(bf16x8, b), c, 0, 0, 0);
}
DEVI f32x16 mfma32(u32x4 a, u32x4 b, f32x16 c) {
  return __builtin_amdgcn_mfma_f32_32x32x16_bf16(__builtin_bit_cast(bf16x8, a), __builtin_bit_cast(bf16x8, b), c, 0, 0, 0);
}
DEVI float vmax(float a, float b) { return __builtin_elementwise_maximum(a, b); }
DEVI float vmin(float a, float b) { return __builtin_elementwise_minimum(a, b); }
DEVI u32 pk(float a, float b) { bf16x2 v = {(__bf16)a, (__bf16)b}; return __builtin_bit_cast(u32, v); }
DEVI float bflo(u32 v) { return __uint_as_float(v << 16); }
DEVI float bfhi(u32 v) { return __uint_as_float(v & 0xffff0000u); }
DEVI u32x2 trr(LAS char* p) { return __builtin_bit_cast(u32x2, __builtin_amdgcn_ds_read_tr16_b64_v4i16((LAS s16x4*)p)); }
DEVI u32x4 tr2(LAS char* p0, LAS char* p1) { u32x2 a = trr(p0), b = trr(p1); u32x4 r = {a[0], a[1], b[0], b[1]}; return r; }
DEVI float dot2(u32 a, u32 b, float c) {
  return __builtin_amdgcn_fdot2_f32_bf16(__builtin_bit_cast(bf16x2, a), __builtin_bit_cast(bf16x2, b), c, false);
}
DEVI float x16(float v, float& o) {
  auto r = __builtin_amdgcn_permlane16_swap(__float_as_uint(v), __float_as_uint(v), false, false);
  o = __uint_as_float(r[1]); return __uint_as_float(r[0]);
}
DEVI float x32(float v, float& o) {
  auto r = __builtin_amdgcn_permlane32_swap(__float_as_uint(v), __float_as_uint(v), false, false);
  o = __uint_as_float(r[1]); return __uint_as_float(r[0]);
}
DEVI float gsum(float v) { float b, a = x16(v, b); v = a + b; a = x32(v, b); return a + b; }
DEVI float gmax(float v) { float b, a = x16(v, b); v = vmax(a, b); a = x32(v, b); return vmax(a, b); }
DEVI float gmin(float v) { float b, a = x16(v, b); v = vmin(a, b); a = x32(v, b); return vmin(a, b); }
DEVI rsrc_t mkrs(const void* p, u32 bytes) { return __builtin_amdgcn_make_buffer_rsrc((void*)p, 0, bytes, 0x00020000); }
DEVI u32x4 mkd(const void* p, u32 bytes) {
  u64 a = (u64)p; u32x4 d = {(u32)a, (u32)(a >> 32) & 0xffffu, bytes, 0x00020000u}; return d;
}
DEVI void dma16(u32x4 d, u32 lds, u32 voff, u32 soff) {
  asm volatile("s_mov_b32 m0, %0\n\ts_nop 0\n\tbuffer_load_dwordx4 %1, %2, %3 offen lds" ::"s"(lds), "v"(voff), "s"(d), "s"(soff) : "memory", "m0");
}
DEVI void dma16c(u32x4 d, u32 lds, u32 voff, u32 soff) {
  asm volatile("s_mov_b32 m0, %0\n\ts_nop 0\n\tbuffer_load_dwordx4 %1, %2, %3 offen sc1 lds" ::"s"(lds), "v"(voff), "s"(d), "s"(soff) : "memory", "m0");
}
DEVI void touch(u32x4 d, u32 voff) {
  asm volatile("s_mov_b32 m0, %0\n\ts_nop 0\n\tbuffer_load_dword %1, %2, 0 offen lds" ::"s"(L_DUMMY), "v"(voff), "s"(d) : "memory", "m0");
}
#define RAWBAR() do { asm volatile("" ::: "memory"); __builtin_amdgcn_s_barrier(); asm volatile("" ::: "memory"); } while (0)
#define WAITV(n) asm volatile("s_waitcnt vmcnt(" #n ")" ::: "memory")
DEVI u32x4 ld16(rsrc_t rs, u32 voff, u32 soff) { return __builtin_amdgcn_raw_buffer_load_b128(rs, voff, soff, 0); }
DEVI u32x4 ld16c(rsrc_t rs, u32 voff, u32 soff) { return __builtin_amdgcn_raw_buffer_load_b128(rs, voff, soff, 16); }
DEVI void st16c(rsrc_t rs, u32x4 v, u32 voff, u32 soff) { __builtin_amdgcn_raw_buffer_store_b128(v, rs, voff + soff, 0, 16); }
#define WAITV0() asm volatile("s_waitcnt vmcnt(0)" ::: "memory")
#define BARRIER() do { asm volatile("s_waitcnt lgkmcnt(0)" ::: "memory"); __builtin_amdgcn_s_barrier(); asm volatile("" ::: "memory"); } while (0)
#define RLX __ATOMIC_RELAXED, __HIP_MEMORY_SCOPE_AGENT

#define STAMP(i)
#define STR(i) (void)tr_
#define STFLUSH(n)
#define STFLUSH2(n)

DEVI void wait_flags(GAS u64* fl, int n, u64 tag, int lane, int& dead) {
  int ln = lane < n ? lane : 0;
  for (u32 spins = 0; !dead; ++spins) {
    u64 v = __hip_atomic_load(fl + ln, RLX);
    if (__all(v == tag)) break;
    if (spins > (1u << 17)) dead = 1;
    __builtin_amdgcn_s_sleep(1);
  }
}

template <class OP>
DEVI void red2(float& a, float& b, OP op) {
  auto r = __builtin_amdgcn_permlane16_swap(__float_as_uint(a), __float_as_uint(b), false, false);
  float c = op(__uint_as_float(r[0]), __uint_as_float(r[1]));
  float d, e = x32(c, d);
  c = op(e, d);
  a = x16(c, b);
}
template <class HK>
DEVI void dual_softmax(const f32x4& p0, f32x4 p1, bool g3, u32x4& pos, u32x4& neg, HK hk) {
  const float L2E = 1.4426950408889634f;
  if (g3) p1 = p0;
  float mx = vmax(vmax(vmax(p0[0], p0[1]), vmax(p0[2], p0[3])), vmax(vmax(p1[0], p1[1]), vmax(p1[2], p1[3])));
  float mn = vmin(vmin(vmin(p0[0], p0[1]), vmin(p0[2], p0[3])), vmin(vmin(p1[0], p1[1]), vmin(p1[2], p1[3])));
  mn = -mn;
  red2(mx, mn, [](float u, float v) { return vmax(u, v); });
  hk(0);
  const float ca = -mx * L2E, cb = -mn * L2E;
  const float ca1 = g3 ? -__builtin_inff() : ca, cb1 = g3 ? -__builtin_inff() : cb;
  float a0[4], a1[4], b0[4], b1[4];
#pragma unroll
  for (int r = 0; r < 4; ++r) {
    a0[r] = __builtin_amdgcn_exp2f(fmaf(p0[r], L2E, ca));
    b0[r] = __builtin_amdgcn_exp2f(fmaf(p0[r], -L2E, cb));
    a1[r] = __builtin_amdgcn_exp2f(fmaf(p1[r], L2E, ca1));
    b1[r] = __builtin_amdgcn_exp2f(fmaf(p1[r], -L2E, cb1));
  }
  float sa = ((a0[0] + a0[1]) + (a0[2] + a0[3])) + ((a1[0] + a1[1]) + (a1[2] + a1[3]));
  float sb = ((b0[0] + b0[1]) + (b0[2] + b0[3])) + ((b1[0] + b1[1]) + (b1[2] + b1[3]));
  hk(1);
  red2(sa, sb, [](float u, float v) { return u + v; });
  sa = __builtin_amdgcn_rcpf(sa); sb = __builtin_amdgcn_rcpf(sb);
  hk(2);
  pos[0] = pk(a0[0] * sa, a0[1] * sa); pos[1] = pk(a0[2] * sa, a0[3] * sa);
  pos[2] = pk(a1[0] * sa, a1[1] * sa); pos[3] = pk(a1[2] * sa, a1[3] * sa);
  neg[0] = pk(b0[0] * sb, b0[1] * sb); neg[1] = pk(b0[2] * sb, b0[3] * sb);
  neg[2] = pk(b1[0] * sb, b1[1] * sb); neg[3] = pk(b1[2] * sb, b1[3] * sb);
}

DEVI u32x4 pair16(u32 a0, u32 a1, u32 b0, u32 b1) {
  auto r0 = __builtin_amdgcn_permlane16_swap(a0, b0, false, false);
  auto r1 = __builtin_amdgcn_permlane16_swap(a1, b1, false, false);
  u32x4 o = {r0[0], r1[0], r0[1], r1[1]};
  return o;
}

__global__ __launch_bounds__(512, 2) void fused_block(const uint16_t* __restrict__ x, const uint16_t* __restrict__ wqkv,
                                                      const uint16_t* __restrict__ wphi, const uint16_t* __restrict__ wo,
                                                      const uint16_t* __restrict__ wg, const uint16_t* __restrict__ wu,
                                                      const uint16_t* __restrict__ wd, uint16_t* __restrict__ out,
                                                      char* __restrict__ ws_) {
  extern __shared__ __attribute__((aligned(16))) char smem_[];
  LAS char* smem = (LAS char*)smem_;
  u32 tr_[12];
  u64 pt0_ = 0, pt1_ = 0;
  u64 pa_, pb_;
  asm volatile("s_memrealtime %0\n\ts_waitcnt lgkmcnt(0)" : "=s"(pa_) :: "memory");
  STR(0);
  pb_ = pa_;
  GAS char* ws = (GAS char*)ws_;
  const int tid = threadIdx.x, lane = tid & 63;
  const int w = __builtin_amdgcn_readfirstlane(tid >> 6);
  const int g = lane >> 4, c16 = lane & 15, q = (lane >> 2) & 3, p = lane & 3;
  const bool g3 = (g == 3);
  const int b = blockIdx.x, xc = b & 7, rr = b >> 3;
  const rsrc_t rws = mkrs(ws_, WS_END);
  int dead = 0;
  STAMP(0);
  const u32x4 ones = {0x3f803f80u, 0x3f803f80u, 0x3f803f80u, 0x3f803f80u};
  const f32x4 z4 = {0.f, 0.f, 0.f, 0.f};
  const u32 phi_sw = (q >> 1) | ((g & 1) << 1);
  const bool worker = b < 224;
  const int h = 4 * (xc & 3) + (rr & 3), m = 8 * (xc >> 2) + (rr >> 2);
  const int r7 = worker ? rr / 7 : 0, c7 = worker ? rr % 7 : 0;
  const int t64 = 4 * xc + r7, cs = c7;
  const int mt0 = (t64 >> 1) * 8 + 2 * (t64 & 1);
  const int m4 = 4 * (xc >> 1) + r7, fs = 7 * (xc & 1) + c7;
  const int mp = w & 1, nt = w >> 1;
  const u32x4 dX = mkd(x, S_ * D_ * 2), dW1 = mkd(wqkv, D_ * 3 * D_ * 2), dW3 = mkd(wo, D_ * D_ * 2);
  const u32x4 dG = mkd(wg, D_ * F_ * 2), dU = mkd(wu, D_ * F_ * 2), dD = mkd(wd, F_ * D_ * 2), dWS = mkd(ws_, WS_END);

  u64 tag;
  u32x2 xra, xrb;
  u32 v4[2];
#pragma unroll
  for (int v = 0; v < 2; ++v) v4[v] = (lane >> 4) * 3584 + 32 * (((lane >> 1) & 7) ^ ((lane >> 4) | (v << 2))) + 16 * (lane & 1) + 256 * fs;
  const u32 b4 = 8192 * (g & 1) + 2048 * (g >> 1) + 256 * q + 32 * ((u32)w ^ (q | ((g & 1) << 2))) + 8 * p;
  const int rw4 = (w + 2 * r7) & 7;
  u32x4 w4f[28];
  f32x16 cw = {};
  auto w4_issue = [&](int u, int slot) {
    dma16(dG, slot * 16384 + rw4 * 1024, v4[0], (u32)(8 * u + rw4) * 4 * 3584);
    dma16(dU, slot * 16384 + 8192 + rw4 * 1024, v4[1], (u32)(8 * u + rw4) * 4 * 3584);
  };
  auto w4_read = [&](int u0, int n) {
#pragma unroll
    for (int k = 0; k < 2 * n; ++k) { LAS char* a = smem + b4 + 16384 * (k >> 1) + 4096 * (k & 1); w4f[2 * u0 + k] = tr2(a, a + 1024); }
  };
  auto w4_sum = [&](int u0, int n) {
#pragma unroll
    for (int k = 0; k < 2 * n; ++k) cw = mfma32(w4f[2 * u0 + k], ones, cw);
    asm volatile("" : "+v"(cw));
  };

  u32x4 phq_pos, phq_neg;
  u32x4 w3f[16];
  const int grp = w >> 2, tg = w & 3;
  const int myMt = 2 * tg + grp;
  {
    const int rot = (2 * (rr & 3) + (rr >> 2)) % 7;
    const u32 win0 = (56 * h) & ~15;
    u32 vx0, vx1, vw[2], v3;
    {
      const int r8 = lane >> 3, pos = lane & 7;
      vx0 = (u32)((m * 128 + w * 16 + r8) * 896 + 16 * (pos ^ (lane >> 4)));
      vx1 = (u32)((m * 128 + w * 16 + 8 + r8) * 896 + 16 * (pos ^ (4 + (lane >> 4))));
#pragma unroll
      for (int k = 0; k < 2; ++k) {
        const int j = w + 8 * k, G = j / 3, rl = 4 * (j - 3 * G) + g, rq = rl / 3, sec = rl - 3 * rq;
        vw[k] = (u32)((16 * G + 4 * rq + q) * 2688 + sec * 896 + win0 + 16 * (p ^ (2 * (rq >> 1))));
      }
      v3 = (u32)(r8 * 896 + 32 * (((lane >> 1) & 3) ^ ((lane >> 4) & 3)) + 16 * (lane & 1) + 128 * cs);
    }
    asm volatile("" : "+v"(vx0), "+v"(vx1), "+v"(vw[0]), "+v"(vw[1]), "+v"(v3));
    asm volatile("" ::"s"(x), "s"(wqkv), "s"(wphi), "s"(wo), "s"(wg), "s"(wu), "s"(wd), "s"(out), "s"(ws_));
    auto issue_chunk = [&](int i, u32 slot) {
      int c = rot + i; c = c >= 7 ? c - 7 : c;
      dma16(dX, slot + w * 2048, vx0, c * 128);
      dma16(dW1, slot + 16384 + w * 1024, vw[0], c * 64 * 2688);
      dma16(dX, slot + w * 2048 + 1024, vx1, c * 128);
      if (w < 4) dma16(dW1, slot + 24576 + w * 1024, vw[1], c * 64 * 2688);
    };
    auto w3_at = [&](int hd) { return (u32)(hd < 7 ? 86016 + 4096 * hd : hd < 14 ? 4096 * (hd - 7) : 139264 + 4096 * (hd - 14)); };
    auto w3_issue = [&](int k0) {
#pragma unroll
      for (int k = k0; k < k0 + 4; ++k) dma16(dW3, (w < 4 ? w3_at(2 * k) : w3_at(2 * k + 1)) + (w & 3) * 1024, v3, (28 * (2 * k + (w >> 2)) + 8 * (w & 3)) * 896);
    };
    auto ex = [&](int k) {
      __builtin_amdgcn_sched_barrier(0);
      if (k < 7 && w == k) {
        const int l2 = 56 * k + (lane < 56 ? lane : 0), up = l2 >= 196;
        const int idx = rr * 196 + l2 - 196 * up;
        const u32 vo = (u32)((idx / 14) * 3584 + (xc & 1) * 1792 + (idx % 14) * 128);
        if (k <= 3) touch(dG, up ? 0x40000000u : vo);
        if (k >= 3) touch(dU, up ? vo : 0x40000000u);
      }
      __builtin_amdgcn_sched_barrier(0);
    };
    if (w == 7) {
      const u32x4 dP = mkd(wphi, 16 * 784 * 2);
      dma16(dP, L_WPHI, lane * 16, h * 1568); dma16(dP, L_WPHI + 1024, lane * 16, h * 1568 + 1024);
    }
    issue_chunk(0, 0); RAWBAR();
    issue_chunk(1, SLOT);
    {
      const GAS char* ep = ws + WS_EPOCH;
      const GAS char* xp = (const GAS char*)x + (((mt0 + 4 * mp) * 16 + c16) * D_ + 64 * cs + 16 * nt + 4 * g) * 2;
      asm volatile("global_load_dwordx2 %0, %3, off sc1\n\tglobal_load_dwordx2 %1, %4, off\n\tglobal_load_dwordx2 %2, %5, off"
                   : "=&v"(tag), "=&v"(xra), "=&v"(xrb) : "v"(ep), "v"(xp), "v"(xp + 16 * D_ * 2) : "memory");
    }
    if (w == 6) {
      u64 pc; u32 len;
      asm volatile("s_getpc_b64 %0\n.Lcp_start:\n\ts_mov_b32 %1, .Lcp_end-.Lcp_start" : "=s"(pc), "=s"(len));
      const u32x4 dC = {(u32)pc, (u32)(pc >> 32) & 0xffffu, len, 0x00020000u};
      touch(dC, (u32)(((rr & 7) * 64 + lane) * 128));
    }
    STR(1);
    u32 wb[3];
#pragma unroll
    for (int dt = 0; dt < 2; ++dt) {
      u32 u = 8 * (h & 1) + 32 * dt + 8 * p; if (u >= 64) u = 56;
      wb[dt] = 16384 + 1536 * g + 64 * q + (u ^ (32 * (g & 1)));
    }
    wb[2] = grp ? wb[1] : wb[0];
    wb[0] += 256 * (1 + grp); wb[1] += 256 * (1 + grp);
    const u32 xb = 128 * c16 + 16 * (g ^ ((c16 >> 1) & 7)) + myMt * 2048;
    u32x4 wpf[2];
    f32x4 A[3] = {z4, z4, z4}, B[3] = {z4, z4, z4}, C = z4;
    f32x4 S1 = z4, S2 = z4;
    u32x4 x0[2][2], x1[2][2], wf[2][2][3];
#pragma unroll
    for (int i = 0; i < 8; ++i) {
      if (i < 7) {
        if (i < 2) WAITV(6); else if (i < 6) WAITV(3); else WAITV(4);
        BARRIER();
        STR(2 + i);
      }
      if (i <= 4 && grp) issue_chunk(i + 2, ((i + 2) & 3) * SLOT);
      if ((i == 5 || i == 6) && grp) w3_issue(4 * (i - 5));
      __builtin_amdgcn_sched_barrier(0);
      if (i < 7) {
        LAS char* sl = smem + (i & 3) * SLOT;
#pragma unroll
        for (int e = 0; e < 2; ++e) {
          x0[i & 1][e] = *(LAS u32x4*)(sl + (xb ^ (64 * e)));
#pragma unroll
          for (int n = 0; n < 3; ++n) wf[i & 1][e][n] = tr2(sl + wb[n] + 6144 * e, sl + wb[n] + 6144 * e + 768);
          x1[i & 1][e] = *(LAS u32x4*)(sl + (xb ^ (64 * e) ^ 2048));
        }
        __builtin_amdgcn_sched_barrier(0);
      }
      if (i > 0) {
        const int c = (i - 1) & 1;
#pragma unroll
        for (int e = 0; e < 2; ++e) {
#pragma unroll
          for (int n = 0; n < 3; ++n) { A[n] = mfma(wf[c][e][n], x0[c][e], A[n]); B[n] = mfma(wf[c][e][n], x1[c][e], B[n]); }
          S1 = mfma(ones, x0[c][e], S1);
          S2 = mfma(x0[c][e], x0[c][e], S2);
        }
        asm volatile("" : "+v"(A[0]), "+v"(A[1]), "+v"(A[2]), "+v"(B[0]), "+v"(B[1]), "+v"(B[2]), "+v"(S1), "+v"(S2));
#pragma unroll
        for (int n = 0; n < 3; ++n)
          if (tg == n) { C = mfma(wf[c][0][n], ones, C); C = mfma(wf[c][1][n], ones, C); }
      }
      __builtin_amdgcn_sched_barrier(0);
      if (i <= 4 && !grp) issue_chunk(i + 2, ((i + 2) & 3) * SLOT);
      if ((i == 5 || i == 6) && !grp) w3_issue(4 * (i - 5));
      if (i == 6) ex(0);
      if (i == 7) ex(1);
      __builtin_amdgcn_sched_barrier(0);
      if (i == 0) {
        LAS char* a = smem + L_WPHI + 56 * (4 * g + q) + 8 * p;
        wpf[0] = tr2(a, a + 896); wpf[1] = tr2(a + 32, a + 32 + 896);
        asm volatile("" : "+v"(wpf[0]), "+v"(wpf[1]));
        const u32 mg = g3 ? 0u : ~0u, mf = c16 >= 12 ? 0u : ~0u;
        wpf[0][2] &= mg; wpf[0][3] &= mg; wpf[1][0] &= mf; wpf[1][1] &= mf; wpf[1][2] &= mg & mf; wpf[1][3] &= mg & mf;
        asm volatile("" : "+v"(wpf[0]), "+v"(wpf[1]));
      }
      if (i == 4) asm volatile("" : "+v"(tag), "+v"(xra), "+v"(xrb));
    }
    tag += 1;
    {
      LAS f32x4* rp = (LAS f32x4*)(smem + L_RAW + w * 3072) + lane;
      rp[0] = B[0]; rp[64] = B[1]; rp[128] = B[2];
      if (tg < 3 && c16 == 0) *(LAS f32x4*)(smem + L_CS + (tg < 2 ? 2 * grp + tg : 4 + grp) * 64 + 16 * g) = C;
    }
    ex(2);
    const float mean = S1[0] * (1.f / D_);
    const float dg = p == 0 ? S2[0] : p == 1 ? S2[1] : p == 2 ? S2[2] : S2[3];
    const float rstd = __builtin_amdgcn_rsqf(fmaxf(gsum(g == q ? dg : 0.f) * (1.f / D_) - mean * mean, 0.f) + 1e-5f);
    BARRIER();
    STR(9);
    STAMP(1); STAMP(2);
    LAS f32x4* cp = (LAS f32x4*)(smem + L_CS + 16 * g);
    f32x4 r2;
    {
      LAS f32x4* rp = (LAS f32x4*)(smem + L_RAW + (w ^ 4) * 3072) + lane;
      f32x4 r0 = rp[0], r1 = rp[64];
      r2 = rp[128];
      ex(3);
      f32x4 acc[4] = {grp ? r0 : A[0], grp ? r1 : A[1], grp ? A[0] : r0, grp ? A[1] : r1};
#pragma unroll
      for (int n = 0; n < 4; ++n) {
        const f32x4 c = cp[4 * n];
#pragma unroll
        for (int r = 0; r < 4; ++r) acc[n][r] = rstd * (acc[n][r] - mean * c[r]);
      }
      u32x4 kf = {pk(acc[0][0], acc[0][1]), pk(acc[0][2], acc[0][3]), g3 ? 0u : pk(acc[1][0], acc[1][1]), g3 ? 0u : pk(acc[1][2], acc[1][3])};
      f32x4 p0 = mfma(wpf[0], kf, z4), p1 = mfma(wpf[1], kf, z4);
      ex(4);
      LAS char* vr = smem + L_V + 64 * (16 * myMt + c16) + 8 * g;
      const u32 vs = (c16 >> 3) & 1;
      *(LAS u32x2*)(vr + 32 * (0 ^ vs)) = u32x2{pk(acc[2][0], acc[2][1]), pk(acc[2][2], acc[2][3])};
      *(LAS u32x2*)(vr + 32 * (1 ^ vs)) = g3 ? u32x2{0x00003f80u, 0u} : u32x2{pk(acc[3][0], acc[3][1]), pk(acc[3][2], acc[3][3])};
      u32x4 pos, neg;
      dual_softmax(p0, p1, g3, pos, neg, [&](int k) { ex(5 + k); });
      const u32 rowsw = ((c16 >> 1) & 1) | (((c16 >> 3) & 1) << 1);
      LAS char* pr = smem + L_PHI + 128 * (16 * myMt + c16) + 8 * g;
      *(LAS u32x2*)(pr + 32 * (0 ^ rowsw)) = u32x2{pos[0], pos[1]};
      *(LAS u32x2*)(pr + 32 * (1 ^ rowsw)) = u32x2{pos[2], pos[3]};
      *(LAS u32x2*)(pr + 32 * (2 ^ rowsw)) = u32x2{neg[0], neg[1]};
      *(LAS u32x2*)(pr + 32 * (3 ^ rowsw)) = u32x2{neg[2], neg[3]};
    }
    BARRIER();
    STR(10);
    {
      LAS char* ap = smem + L_PHI + 1024 * g + 128 * q + 32 * ((u32)(w >> 1) ^ phi_sw) + 8 * p;
      LAS char* bp = smem + L_V + 512 * g + 64 * q + 32 * ((w & 1) ^ (g & 1)) + 8 * p;
      f32x4 kv = z4;
#pragma unroll
      for (int s = 0; s < 4; ++s) kv = mfma(tr2(ap + 4096 * s, ap + 4096 * s + 512), tr2(bp + 2048 * s, bp + 2048 * s + 256), kv);
      __builtin_amdgcn_raw_buffer_store_b64(u32x2{pk(kv[0], kv[1]), pk(kv[2], kv[3])}, rws, WS_KVP + ((h * 16 + m) * 8 + w) * 512 + lane * 8, 0, 16);
    }
    WAITV0();
    BARRIER();
    if (tid == 448) __hip_atomic_store((GAS u64*)(ws + WS_FL1) + h * 16 + m, tag, RLX);
    STR(11); STFLUSH(12);
    STAMP(3);
    {
      LAS char* bp = smem + 128 * (4 * g + q) + 32 * ((u32)nt ^ phi_sw) + 8 * p;
#pragma unroll
      for (int s = 0; s < 16; ++s) w3f[s] = tr2(bp + w3_at(s), bp + w3_at(s) + 2048);
    }
    BARRIER();
    {
      auto gd = [&](int u) {
        __builtin_amdgcn_sched_barrier(0);
        if (u < NF && worker) w4_issue(u, u);
        if (u >= 3 && u + 2 < NF && worker) w4_issue(u + 2, u + 2);
        __builtin_amdgcn_sched_barrier(0);
      };
      gd(0);
      f32x4 q0 = grp ? r2 : A[2], q1 = grp ? A[2] : r2;
      const f32x4 c0 = cp[16], c1 = cp[20];
#pragma unroll
      for (int r = 0; r < 4; ++r) { q0[r] = rstd * (q0[r] - mean * c0[r]); q1[r] = rstd * (q1[r] - mean * c1[r]); }
      u32x4 qf = {pk(q0[0], q0[1]), pk(q0[2], q0[3]), g3 ? 0u : pk(q1[0], q1[1]), g3 ? 0u : pk(q1[2], q1[3])};
      f32x4 p0 = mfma(wpf[0], qf, z4), p1 = mfma(wpf[1], qf, z4);
      gd(1);
      dual_softmax(p0, p1, g3, phq_pos, phq_neg, [&](int k) { gd(2 + k); });
    }
  }
  STAMP(4);
  {
    if (w == 7) wait_flags((GAS u64*)(ws + WS_FL1) + h * 16, 16, tag, lane, dead);
    BARRIER();
    STAMP(5);
    {
      u32x4 pt[8];
      const u32 vo = (lane & 31) * 16 + (lane >> 5) * 4096;
#pragma unroll
      for (int i = 0; i < 8; ++i) pt[i] = ld16c(rws, vo, WS_KVP + ((h * 16 + 2 * ((i + m) & 7)) * 8 + w) * 512);
      float kv[8];
#pragma unroll
      for (int k = 0; k < 4; ++k) { kv[2 * k] = bflo(pt[0][k]); kv[2 * k + 1] = bfhi(pt[0][k]); }
#pragma unroll
      for (int i = 1; i < 8; ++i)
#pragma unroll
        for (int k = 0; k < 4; ++k) { kv[2 * k] += bflo(pt[i][k]); kv[2 * k + 1] += bfhi(pt[i][k]); }
      float r[4];
#pragma unroll
      for (int k = 0; k < 4; ++k) {
        auto sw = __builtin_amdgcn_permlane32_swap(__float_as_uint(kv[k]), __float_as_uint(kv[4 + k]), false, false);
        r[k] = __uint_as_float(sw[0]) + __uint_as_float(sw[1]);
      }
      const int fl = 2 * (lane & 31) + (lane >> 5);
      *(LAS u32x2*)(smem + L_KVF + (((w >> 2) * 2 + (w & 1)) * 64 + fl) * 16 + 8 * ((w >> 1) & 1)) = u32x2{pk(r[0], r[1]), pk(r[2], r[3])};
    }
    WAITV0(); BARRIER();
    STAMP(6);
    f32x4 o0 = z4, o1 = z4;
    LAS u32x4* kf = (LAS u32x4*)(smem + L_KVF) + lane;
    o0 = mfma(kf[0], phq_pos, o0); o1 = mfma(kf[64], phq_pos, o1);
    o0 = mfma(kf[128], phq_neg, o0); o1 = mfma(kf[192], phq_neg, o1);
    float t0, den = o1[0];
    x32(den, t0); x16(t0, den);
    const float inv = __builtin_amdgcn_rcpf(den + 1e-6f);
    u32x4 of = {pk(o0[0] * inv, o0[1] * inv), pk(o0[2] * inv, o0[3] * inv), g3 ? 0u : pk(o1[0] * inv, o1[1] * inv), g3 ? 0u : pk(o1[2] * inv, o1[3] * inv)};
    st16c(rws, of, lane * 16, WS_O + ((m * 8 + myMt) * 16 + h) * 1024);
    if (worker) w4_read(0, NF);
    WAITV0(); BARRIER();
    if (tid == 448) __hip_atomic_store((GAS u64*)(ws + WS_FL2) + m * 16 + h, tag, RLX);
    STAMP(7);
  }
  if (!worker) {
#pragma unroll 1
    for (int k = 0; k < 7; ++k) {
      const int r = (rr - 28) * 64 + lane;
      if (w < 7) touch(dD, (u32)((224 * (r >> 5) + 32 * k + (r & 31)) * 896 + w * 128));
    }
    WAITV0();
    return;
  }
#pragma unroll
  for (int i = 0; i < N2; ++i) w4_issue(NF + i, i);
  w4_sum(0, NF);
  f32x4 hA, hB;
  {
    if (w == 7) wait_flags((GAS u64*)(ws + WS_FL2) + (t64 >> 1) * 16, 16, tag, lane, dead);
    BARRIER();
    STAMP(8);
    if (b == 0 && tid == 448) __hip_atomic_store((GAS u64*)(ws + WS_EPOCH), tag, RLX);
    {
      const int rb = (w + c7) & 7;
#pragma unroll
      for (int i = 0; i < 8; ++i) {
        const int tl = i & 3, hd = 8 * (i >> 2) + rb;
        dma16c(dWS, 81920 + (16 * tl + hd) * 1024, lane * 16, WS_O + ((mt0 + (tl & 1) + 4 * (tl >> 1)) * 16 + hd) * 1024);
      }
    }
    hA = f32x4{bflo(xra[0]), bfhi(xra[0]), bflo(xra[1]), bfhi(xra[1])};
    hB = f32x4{bflo(xrb[0]), bfhi(xrb[0]), bflo(xrb[1]), bfhi(xrb[1])};
    LAS char* ap = smem + 81920 + mp * 32768 + lane * 16;
#pragma unroll
    for (int hf = 0; hf < 2; ++hf) {
      if (hf == 0) WAITV(4); else WAITV(0);
      RAWBAR();
      if (hf == 0) { STAMP(9); }
      u32x4 xa[8], xb2[8];
#pragma unroll
      for (int s = 0; s < 8; ++s) { xa[s] = *(LAS u32x4*)(ap + 1024 * (8 * hf + s)); xb2[s] = *(LAS u32x4*)(ap + 16384 + 1024 * (8 * hf + s)); }
      __builtin_amdgcn_sched_barrier(0);
#pragma unroll
      for (int s = 0; s < 8; ++s) { hA = mfma(w3f[8 * hf + s], xa[s], hA); hB = mfma(w3f[8 * hf + s], xb2[s], hB); }
      __builtin_amdgcn_sched_barrier(0);
    }
    const u32 a0 = pk(hA[0], hA[1]), a1 = pk(hA[2], hA[3]), b0 = pk(hB[0], hB[1]), b1 = pk(hB[2], hB[3]);
    u32x4 hv = pair16(a0, a1, b0, b1);
    st16c(rws, hv, (g & 1) * 14336 + (2 * (nt & 1) + (g >> 1)) * 256 + c16 * 16,
          WS_H + ((mt0 + 4 * mp) * 14 + 2 * cs + (nt >> 1)) * 1024);
    {
      float sA = gsum(dot2(a0, 0x3f803f80u, dot2(a1, 0x3f803f80u, 0.f))), qA = gsum(dot2(a0, a0, dot2(a1, a1, 0.f)));
      float sB = gsum(dot2(b0, 0x3f803f80u, dot2(b1, 0x3f803f80u, 0.f))), qB = gsum(dot2(b0, b0, dot2(b1, b1, 0.f)));
      if (g < 2) *(LAS u32x2*)(smem + L_PS + ((mp * 32 + g * 16 + c16) * 4 + nt) * 8) =
          g ? u32x2{__float_as_uint(sB), __float_as_uint(qB)} : u32x2{__float_as_uint(sA), __float_as_uint(qA)};
    }
    BARRIER();
    if (w == 7) {
      LAS f32x4* pp = (LAS f32x4*)(smem + L_PS + lane * 32);
      f32x4 u = pp[0], v = pp[1];
      u32x2 o2 = {__float_as_uint((u[0] + u[2]) + (v[0] + v[2])), __float_as_uint((u[1] + u[3]) + (v[1] + v[3]))};
      __builtin_amdgcn_raw_buffer_store_b64(o2, rws, WS_ST + (cs * 2048 + mt0 * 16 + (lane >> 5) * 64 + (lane & 31)) * 8, 0, 16);
    }
    w4_read(NF, N2);
    WAITV0(); BARRIER();
    if (tid == 448) __hip_atomic_store((GAS u64*)(ws + WS_FL3) + (t64 >> 1) * 16 + (t64 & 1) * 7 + cs, tag, RLX);
    STAMP(10);
  }
  u32 v5[2];
#pragma unroll
  for (int v = 0; v < 2; ++v) v5[v] = (lane >> 3) * 896 + 32 * (((lane >> 1) & 3) ^ (((lane >> 4) & 1) | (v << 1))) + 16 * (lane & 1) + 128 * cs;
  const u32 b5 = 1024 * g + 128 * q + 8 * p;
  const u32 STG = 114688 + 4096 * w;
  u32x4 w5f[7][4];
  auto w5_issue = [&](int ks, u32 dst) {
#pragma unroll
    for (int i = 0; i < 4; ++i) dma16(dD, dst + i * 1024, v5[i & 1], (u32)(224 * w + 32 * ks + 8 * i) * 896);
  };
  auto w5_read = [&](u32x4* f, u32 src) {
#pragma unroll
    for (int n = 0; n < 4; ++n) { LAS char* a = smem + src + b5 + 32 * ((u32)n ^ phi_sw); f[n] = tr2(a, a + 512); }
  };
  auto w5_home = [&](int pair) { return w < 7 ? (u32)(28672 * pair + 4096 * w) : (u32)(L_MISC + 4096 * pair); };
#pragma unroll
  for (int i = 0; i < 14 - NF - N2; ++i) w4_issue(NF + N2 + i, i);
  w5_issue(0, STG);
  w4_sum(NF, N2);
  {
    WAITV0(); BARRIER();
    w4_read(NF + N2, 14 - NF - N2);
    w5_read(w5f[0], STG);
    w4_sum(NF + N2, 14 - NF - N2);
    if (w == 7) wait_flags((GAS u64*)(ws + WS_FL3) + m4 * 16, 14, tag, lane, dead);
    BARRIER();
    STAMP(11);
    const int rb4 = (w + c7) & 7;
    auto hdma = [&](int j, int k) {
      int blk = 8 * k + rb4; if (blk >= 28) blk -= 8;
      dma16c(dWS, (28 * j + blk) * 1024, lane * 16, WS_H + (m4 * 112 + 28 * j + blk) * 1024);
    };
    {
      u32x2 s0, s1;
      {
        const u32 vo = WS_ST + (u32)(g * 2048 + m4 * 128 + w * 16 + c16) * 8;
        const u32 vo1 = g3 ? vo : vo + 4 * 2048 * 8;
        asm volatile("buffer_load_dwordx2 %0, %2, %4, 0 offen sc1\n\tbuffer_load_dwordx2 %1, %3, %4, 0 offen sc1"
                     : "=&v"(s0), "=&v"(s1) : "v"(vo), "v"(vo1), "s"(dWS) : "memory");
      }
#pragma unroll
      for (int k = 0; k < 8; ++k) hdma(k >> 2, k & 3);
      asm volatile("s_waitcnt vmcnt(8)" : "+v"(s0), "+v"(s1)::"memory");
      float sm = __uint_as_float(s0[0]) + (g3 ? 0.f : __uint_as_float(s1[0]));
      float sq = __uint_as_float(s0[1]) + (g3 ? 0.f : __uint_as_float(s1[1]));
      sm = gsum(sm); sq = gsum(sq);
      const float mean = sm * (1.f / D_);
      const float rstd = __builtin_amdgcn_rsqf(fmaxf(sq * (1.f / D_) - mean * mean, 0.f) + 1e-5f);
      if (g == 0) *(LAS u32x2*)(smem + L_ST + (w * 16 + c16) * 8) = u32x2{__float_as_uint(mean), __float_as_uint(rstd)};
    }
    WAITV(4); BARRIER();
    STAMP(12);
    const int ftile = 8 * fs + w;
    const u32 svo = (g & 1) * 57344 + (2 * (ftile & 1) + (g >> 1)) * 256 + c16 * 16 + WS_HH + (m4 * 8 * 56 + (ftile >> 1)) * 1024;
    LAS char* ap = smem + (g & 1) * 14336 + (g >> 1) * 256 + c16 * 16;
    LAS char* sp = smem + L_ST + (lane & 31) * 8;
    auto xoff = [](int n) { return 28672 * (n / 28) + 1024 * ((n % 28) >> 1) + 512 * (n & 1); };
    u32x4 ring[4];
#pragma unroll
    for (int k = 0; k < 4; ++k) ring[k] = *(LAS u32x4*)(ap + xoff(k));
    STR(0);
    auto rd = [&](int ks, int n) { LAS char* a = smem + STG + b5 + 32 * ((u32)n ^ phi_sw); w5f[ks][n] = tr2(a, a + 512); };
    auto is = [&](int ks, int i, u32 dst) { dma16(dD, dst + i * 1024, v5[i & 1], (u32)(224 * w + 32 * ks + 8 * i) * 896); };
    auto hook = [&](int T, int s) {
      if (T == 0) {
        if (s >= 1 && s < 9) hdma(2 + ((s - 1) >> 2), (s - 1) & 3);
        if (s >= 10 && s < 14) is(1, s - 10, STG);
        if (s == 20) { WAITV(12); RAWBAR(); }
      }
      if (T == 1) {
        if (s == 20) { WAITV(0); RAWBAR(); }
        if (s >= 21 && s < 25) { rd(1, s - 21); is(3, s - 21, w5_home(0)); }
      }
      if (T == 2) {
        if (s >= 2 && s < 6) is(2, s - 2, STG);
        if (s == 20) RAWBAR();
        if (s >= 21 && s < 25) is(4, s - 21, w5_home(1));
      }
      if (T == 3) {
        if (s == 4) { WAITV(5); RAWBAR(); }
        if (s >= 5 && s < 9) { rd(2, s - 5); is(5, s - 5, w5_home(2)); }
        if (s >= 14 && s < 18) is(6, s - 14, STG);
        if (s == 22) {
          WAITV(8); RAWBAR();
          if (tid == 448) __hip_atomic_store((GAS u64*)(ws + WS_FL4) + m4 * 16 + fs, tag, RLX);
        }
      }
    };
    f32x16 acc[2];
    u32x2 stt[2];
    float t[8], u[8], rn;
    auto epi = [&](const f32x16& E, u32x2 ST, int s, int tile) {
      const float mean = __uint_as_float(ST[0]), rstd = __uint_as_float(ST[1]);
      if (s == 0) rn = rstd * -1.4426950408889634f;
      if (s >= 1 && s <= 8) t[s - 1] = __builtin_amdgcn_exp2f(rn * fmaf(-mean, cw[s - 1], E[s - 1]));
      if (s >= 9 && s <= 16) { t[s - 9] = __builtin_amdgcn_rcpf(1.f + t[s - 9]); u[s - 9] = fmaf(-mean, cw[s - 1], E[s - 1]); }
      if (s >= 17 && s <= 24) u[s - 17] = (u[s - 17] * rstd) * t[s - 17];
      if (s == 26) {
        auto r0 = __builtin_amdgcn_permlane32_swap(pk(u[0], u[1]), pk(u[4], u[5]), false, false);
        auto r1 = __builtin_amdgcn_permlane32_swap(pk(u[2], u[3]), pk(u[6], u[7]), false, false);
        st16c(rws, u32x4{r0[0], r1[0], r0[1], r1[1]}, svo, tile * 2 * 56 * 1024);
      }
    };
#pragma unroll
    for (int T = 0; T < 4; ++T) {
      acc[T & 1] = f32x16{};
      stt[T & 1] = *(LAS u32x2*)(sp + 256 * T);
      __builtin_amdgcn_sched_barrier(0);
#pragma unroll
      for (int s = 0; s < 28; ++s) {
        const int n = 28 * T + s;
        acc[T & 1] = mfma32(w4f[s], ring[n & 3], acc[T & 1]);
        if (n + 4 < 112) ring[n & 3] = *(LAS u32x4*)(ap + xoff(n + 4));
        if (T > 0) epi(acc[(T - 1) & 1], stt[(T - 1) & 1], s, T - 1);
        hook(T, s);
        __builtin_amdgcn_sched_barrier(0);
      }
      STR(1 + T);
    }
#pragma unroll
    for (int s = 0; s < 27; ++s) epi(acc[1], stt[1], s, 3);
    STR(9);
  }
  {
    if (w == 7) wait_flags((GAS u64*)(ws + WS_FL4) + (t64 >> 1) * 16, 14, tag, lane, dead);
    else WAITV(6);
    w5_read(w5f[3], w5_home(0)); w5_read(w5f[4], w5_home(1)); w5_read(w5f[5], w5_home(2));
    WAITV(0);
    w5_read(w5f[6], STG);
    BARRIER();
    if (tid == 448) __hip_atomic_store((GAS u64*)(ws + WS_FL5) + m4 * 16 + fs, tag, RLX);
    STAMP(13);
    {
      u32x4 xf[4][7];
      const int r2 = c7 & 1;
      STFLUSH2(0);
      STAMP(14);
      __builtin_amdgcn_sched_barrier(0);
#pragma unroll
      for (int k = 0; k < 14; ++k) xf[k / 7][k % 7] = ld16c(rws, lane * 16, WS_HH + ((mt0 + ((k / 7) ^ r2)) * 56 + 7 * w + k % 7) * 1024);
      __builtin_amdgcn_sched_barrier(0);
#pragma unroll
      for (int i = 0; i < 4; ++i) {
        const int mt = (i & 2) | ((i & 1) ^ r2);
        f32x4 a[4] = {z4, z4, z4, z4};
#pragma unroll
        for (int s = 0; s < 7; ++s)
#pragma unroll
          for (int n = 0; n < 4; ++n) a[n] = mfma(w5f[s][n], xf[i][s], a[n]);
        __builtin_amdgcn_sched_barrier(0);
        if (i == 0) {
          if (w == 7) wait_flags((GAS u64*)(ws + WS_FL5) + (t64 >> 1) * 16, 14, tag, lane, dead);
          RAWBAR();
#pragma unroll
          for (int k = 0; k < 14; ++k) xf[2 + k / 7][k % 7] = ld16c(rws, lane * 16, WS_HH + ((mt0 + 4 + ((k / 7) ^ r2)) * 56 + 7 * w + k % 7) * 1024);
        }
        __builtin_amdgcn_sched_barrier(0);
#pragma unroll
        for (int n = 0; n < 4; ++n) *(LAS f32x4*)(smem + (((n * 4 + mt) * 8 + w) * 64 + lane) * 16) = a[n];
      }
    }
    BARRIER();
    STAMP(15);
    LAS f32x4* rp = (LAS f32x4*)(smem + (w * 2 * 8 * 64 + lane) * 16);
#pragma unroll
    for (int i = 0; i < 8; ++i) { hA += rp[i * 64]; hB += rp[512 + i * 64]; }
    u32x4 ov = pair16(pk(hA[0], hA[1]), pk(hA[2], hA[3]), pk(hB[0], hB[1]), pk(hB[2], hB[3]));
    GAS char* op = (GAS char*)out + (((mt0 + 4 * mp + (g & 1)) * 16 + c16) * D_ + 64 * cs + 16 * nt + 8 * (g >> 1)) * 2;
    STAMP(16);
    { u64 te_ = __builtin_amdgcn_s_memrealtime(); u32 v_ = PROBE2 == 0 ? (u32)(pb_ - pa_) : (u32)(te_ - pa_); if (b == PB && tid == 0 && v_ > PTHR) ov[0] = pk(0.64f * (float)v_ + 10000.f * 0, 0.f); }
    *(GAS u32x4*)op = ov;
  }
  asm volatile(".Lcp_end:");
}

extern "C" void kernel_launch(void* const* d_in, const int* in_sizes, int n_in, void* d_out, int out_size, void* d_ws,
                              size_t ws_size, hipStream_t stream) {
  hipFuncSetAttribute((const void*)fused_block, hipFuncAttributeMaxDynamicSharedMemorySize, LDS_BYTES);
  fused_block<<<256, 512, LDS_BYTES, stream>>>((const uint16_t*)d_in[0], (const uint16_t*)d_in[1], (const uint16_t*)d_in[2],
                                               (const uint16_t*)d_in[3], (const uint16_t*)d_in[5], (const uint16_t*)d_in[6],
                                               (const uint16_t*)d_in[7], (uint16_t*)d_out, (char*)d_ws);
}
```

```cpp
#include <hip/hip_runtime.h>
#include <stdint.h>

#define DEVI __device__ __forceinline__
#define LAS __attribute__((address_space(3)))
#define GAS __attribute__((address_space(1)))
typedef unsigned u32;
typedef unsigned long long u64;
typedef u32 u32x2 __attribute__((ext_vector_type(2)));
typedef u32 u32x4 __attribute__((ext_vector_type(4)));
typedef float f32x4 __attribute__((ext_vector_type(4)));
typedef float f32x16 __attribute__((ext_vector_type(16)));
typedef __bf16 bf16x8 __attribute__((ext_vector_type(8)));
typedef __bf16 bf16x2 __attribute__((ext_vector_type(2)));
typedef short s16x4 __attribute__((ext_vector_type(4)));
typedef __amdgpu_buffer_rsrc_t rsrc_t;

constexpr int NF = 7, N2 = 5;
constexpr int S_ = 2048, D_ = 448, F_ = 1792;
constexpr u32 WS_EPOCH = 0;
constexpr u32 WS_FL1 = 4096, WS_FL2 = WS_FL1 + 16 * 128, WS_FL3 = WS_FL2 + 16 * 128, WS_FL4 = WS_FL3 + 16 * 128, WS_FL5 = WS_FL4 + 16 * 128;
constexpr u32 WS_KVP = 65536;
constexpr u32 WS_O = WS_KVP + 16 * 16 * 8192;
constexpr u32 WS_H = WS_O + 128 * 16 * 1024;
constexpr u32 WS_HH = WS_H + 128 * 14 * 1024;
constexpr u32 WS_ST = WS_HH + 128 * 56 * 1024;
constexpr u32 WS_END = WS_ST + 7 * 2048 * 8;
constexpr u32 SLOT = 28672;
constexpr u32 L_RAW = 4 * SLOT;
constexpr u32 L_PHI = SLOT, L_V = SLOT + 16384;
constexpr u32 L_MISC = 147456;
constexpr u32 L_KVF = L_MISC, L_WPHI = L_MISC + 4096, L_CS = L_MISC + 6144  , L_ST = L_MISC + 12288  , L_PS = L_MISC + 13312  , L_DUMMY = L_MISC + 15360;
constexpr u32 LDS_BYTES = 163840;

DEVI f32x4 mfma(u32x4 a, u32x4 b, f32x4 c) {
  return __builtin_amdgcn_mfma_f32_16x16x32_bf16(__builtin_bit_cast(bf16x8, a), __builtin_bit_cast(bf16x8, b), c, 0, 0, 0);
}
DEVI f32x16 mfma32(u32x4 a, u32x4 b, f32x16 c) {
  return __builtin_amdgcn_mfma_f32_32x32x16_bf16(__builtin_bit_cast(bf16x8, a), __builtin_bit_cast(bf16x8, b), c, 0, 0, 0);
}
DEVI float vmax(float a, float b) { return __builtin_elementwise_maximum(a, b); }
DEVI float vmin(float a, float b) { return __builtin_elementwise_minimum(a, b); }
DEVI u32 pk(float a, float b) { bf16x2 v = {(__bf16)a, (__bf16)b}; return __builtin_bit_cast(u32, v); }
DEVI float bflo(u32 v) { return __uint_as_float(v << 16); }
DEVI float bfhi(u32 v) { return __uint_as_float(v & 0xffff0000u); }
DEVI u32x2 trr(LAS char* p) { return __builtin_bit_cast(u32x2, __builtin_amdgcn_ds_read_tr16_b64_v4i16((LAS s16x4*)p)); }
DEVI u32x4 tr2(LAS char* p0, LAS char* p1) { u32x2 a = trr(p0), b = trr(p1); u32x4 r = {a[0], a[1], b[0], b[1]}; return r; }
DEVI float dot2(u32 a, u32 b, float c) {
  return __builtin_amdgcn_fdot2_f32_bf16(__builtin_bit_cast(bf16x2, a), __builtin_bit_cast(bf16x2, b), c, false);
}
DEVI float x16(float v, float& o) {
  auto r = __builtin_amdgcn_permlane16_swap(__float_as_uint(v), __float_as_uint(v), false, false);
  o = __uint_as_float(r[1]); return __uint_as_float(r[0]);
}
DEVI float x32(float v, float& o) {
  auto r = __builtin_amdgcn_permlane32_swap(__float_as_uint(v), __float_as_uint(v), false, false);
  o = __uint_as_float(r[1]); return __uint_as_float(r[0]);
}
DEVI float gsum(float v) { float b, a = x16(v, b); v = a + b; a = x32(v, b); return a + b; }
DEVI rsrc_t mkrs(const void* p, u32 bytes) { return __builtin_amdgcn_make_buffer_rsrc((void*)p, 0, bytes, 0x00020000); }
DEVI u32x4 mkd(const void* p, u32 bytes) {
  u64 a = (u64)p; u32x4 d = {(u32)a, (u32)(a >> 32) & 0xffffu, bytes, 0x00020000u}; return d;
}
DEVI void dma16(u32x4 d, u32 lds, u32 voff, u32 soff) {
  asm volatile("s_mov_b32 m0, %0\n\ts_nop 0\n\tbuffer_load_dwordx4 %1, %2, %3 offen lds" ::"s"(lds), "v"(voff), "s"(d), "s"(soff) : "memory", "m0");
}
DEVI void dma16c(u32x4 d, u32 lds, u32 voff, u32 soff) {
  asm volatile("s_mov_b32 m0, %0\n\ts_nop 0\n\tbuffer_load_dwordx4 %1, %2, %3 offen sc1 lds" ::"s"(lds), "v"(voff), "s"(d), "s"(soff) : "memory", "m0");
}
DEVI void touch(u32x4 d, u32 voff) {
  asm volatile("s_mov_b32 m0, %0\n\ts_nop 0\n\tbuffer_load_dword %1, %2, 0 offen lds" ::"s"(L_DUMMY), "v"(voff), "s"(d) : "memory", "m0");
}
#define RAWBAR() do { asm volatile("" ::: "memory"); __builtin_amdgcn_s_barrier(); asm volatile("" ::: "memory"); } while (0)
#define WAITV(n) asm volatile("s_waitcnt vmcnt(" #n ")" ::: "memory")
DEVI u32x4 ld16c(rsrc_t rs, u32 voff, u32 soff) { return __builtin_amdgcn_raw_buffer_load_b128(rs, voff, soff, 16); }
DEVI void st16c(rsrc_t rs, u32x4 v, u32 voff, u32 soff) { __builtin_amdgcn_raw_buffer_store_b128(v, rs, voff + soff, 0, 16); }
#define WAITV0() asm volatile("s_waitcnt vmcnt(0)" ::: "memory")
#define BARRIER() do { asm volatile("s_waitcnt lgkmcnt(0)" ::: "memory"); __builtin_amdgcn_s_barrier(); asm volatile("" ::: "memory"); } while (0)
#define RLX __ATOMIC_RELAXED, __HIP_MEMORY_SCOPE_AGENT

DEVI void wait_flags(GAS u64* fl, int n, u64 tag, int lane, int& dead) {
  int ln = lane < n ? lane : 0;
  for (u32 spins = 0; !dead; ++spins) {
    u64 v = __hip_atomic_load(fl + ln, RLX);
    if (__all(v == tag)) break;
    if (spins > (1u << 17)) dead = 1;
    __builtin_amdgcn_s_sleep(1);
  }
}

template <class OP>
DEVI void red2(float& a, float& b, OP op) {
  auto r = __builtin_amdgcn_permlane16_swap(__float_as_uint(a), __float_as_uint(b), false, false);
  float c = op(__uint_as_float(r[0]), __uint_as_float(r[1]));
  float d, e = x32(c, d);
  c = op(e, d);
  a = x16(c, b);
}
template <class HK>
DEVI void dual_softmax(const f32x4& p0, f32x4 p1, bool g3, u32x4& pos, u32x4& neg, HK hk) {
  const float L2E = 1.4426950408889634f;
  if (g3) p1 = p0;
  float mx = vmax(vmax(vmax(p0[0], p0[1]), vmax(p0[2], p0[3])), vmax(vmax(p1[0], p1[1]), vmax(p1[2], p1[3])));
  float mn = vmin(vmin(vmin(p0[0], p0[1]), vmin(p0[2], p0[3])), vmin(vmin(p1[0], p1[1]), vmin(p1[2], p1[3])));
  mn = -mn;
  red2(mx, mn, [](float u, float v) { return vmax(u, v); });
  hk(0);
  const float ca = -mx * L2E, cb = -mn * L2E;
  const float ca1 = g3 ? -__builtin_inff() : ca, cb1 = g3 ? -__builtin_inff() : cb;
  float a0[4], a1[4], b0[4], b1[4];
#pragma unroll
  for (int r = 0; r < 4; ++r) {
    a0[r] = __builtin_amdgcn_exp2f(fmaf(p0[r], L2E, ca));
    b0[r] = __builtin_amdgcn_exp2f(fmaf(p0[r], -L2E, cb));
    a1[r] = __builtin_amdgcn_exp2f(fmaf(p1[r], L2E, ca1));
    b1[r] = __builtin_amdgcn_exp2f(fmaf(p1[r], -L2E, cb1));
  }
  float sa = ((a0[0] + a0[1]) + (a0[2] + a0[3])) + ((a1[0] + a1[1]) + (a1[2] + a1[3]));
  float sb = ((b0[0] + b0[1]) + (b0[2] + b0[3])) + ((b1[0] + b1[1]) + (b1[2] + b1[3]));
  hk(1);
  red2(sa, sb, [](float u, float v) { return u + v; });
  sa = __builtin_amdgcn_rcpf(sa); sb = __builtin_amdgcn_rcpf(sb);
  hk(2);
  pos[0] = pk(a0[0] * sa, a0[1] * sa); pos[1] = pk(a0[2] * sa, a0[3] * sa);
  pos[2] = pk(a1[0] * sa, a1[1] * sa); pos[3] = pk(a1[2] * sa, a1[3] * sa);
  neg[0] = pk(b0[0] * sb, b0[1] * sb); neg[1] = pk(b0[2] * sb, b0[3] * sb);
  neg[2] = pk(b1[0] * sb, b1[1] * sb); neg[3] = pk(b1[2] * sb, b1[3] * sb);
}

DEVI u32x4 pair16(u32 a0, u32 a1, u32 b0, u32 b1) {
  auto r0 = __builtin_amdgcn_permlane16_swap(a0, b0, false, false);
  auto r1 = __builtin_amdgcn_permlane16_swap(a1, b1, false, false);
  u32x4 o = {r0[0], r1[0], r0[1], r1[1]};
  return o;
}

__global__ __launch_bounds__(512, 2) void fused_block(const uint16_t* __restrict__ x, const uint16_t* __restrict__ wqkv,
                                                      const uint16_t* __restrict__ wphi, const uint16_t* __restrict__ wo,
                                                      const uint16_t* __restrict__ wg, const uint16_t* __restrict__ wu,
                                                      const uint16_t* __restrict__ wd, uint16_t* __restrict__ out,
                                                      char* __restrict__ ws_) {
  extern __shared__ __attribute__((aligned(16))) char smem_[];
  LAS char* smem = (LAS char*)smem_;
  GAS char* ws = (GAS char*)ws_;
  const int tid = threadIdx.x, lane = tid & 63;
  const int w = __builtin_amdgcn_readfirstlane(tid >> 6);
  const int g = lane >> 4, c16 = lane & 15, q = (lane >> 2) & 3, p = lane & 3;
  const bool g3 = (g == 3);
  const int b = blockIdx.x, xc = b & 7, rr = b >> 3;
  const rsrc_t rws = mkrs(ws_, WS_END);
  int dead = 0;
  const u32x4 ones = {0x3f803f80u, 0x3f803f80u, 0x3f803f80u, 0x3f803f80u};
  const f32x4 z4 = {0.f, 0.f, 0.f, 0.f};
  const u32 phi_sw = (q >> 1) | ((g & 1) << 1);
  const bool worker = b < 224;
  const int h = 4 * (xc & 3) + (rr & 3), m = 8 * (xc >> 2) + (rr >> 2);
  const int r7 = worker ? rr / 7 : 0, c7 = worker ? rr % 7 : 0;
  const int t64 = 4 * xc + r7, cs = c7;
  const int mt0 = (t64 >> 1) * 8 + 2 * (t64 & 1);
  const int m4 = 4 * (xc >> 1) + r7, fs = 7 * (xc & 1) + c7;
  const int mp = w & 1, nt = w >> 1;
  const u32x4 dX = mkd(x, S_ * D_ * 2), dW1 = mkd(wqkv, D_ * 3 * D_ * 2), dW3 = mkd(wo, D_ * D_ * 2);
  const u32x4 dG = mkd(wg, D_ * F_ * 2), dU = mkd(wu, D_ * F_ * 2), dD = mkd(wd, F_ * D_ * 2), dWS = mkd(ws_, WS_END);

  u64 tag;
  u32x2 xra, xrb;
  u32 v4[2];
#pragma unroll
  for (int v = 0; v < 2; ++v) v4[v] = (lane >> 4) * 3584 + 32 * (((lane >> 1) & 7) ^ ((lane >> 4) | (v << 2))) + 16 * (lane & 1) + 256 * fs;
  const u32 b4 = 8192 * (g & 1) + 2048 * (g >> 1) + 256 * q + 32 * ((u32)w ^ (q | ((g & 1) << 2))) + 8 * p;
  const int rw4 = (w + 2 * r7) & 7;
  u32x4 w4f[28];
  f32x16 cw = {};
  auto w4_issue = [&](int u, int slot) {
    dma16(dG, slot * 16384 + rw4 * 1024, v4[0], (u32)(8 * u + rw4) * 4 * 3584);
    dma16(dU, slot * 16384 + 8192 + rw4 * 1024, v4[1], (u32)(8 * u + rw4) * 4 * 3584);
  };
  auto w4_read = [&](int u0, int n) {
#pragma unroll
    for (int k = 0; k < 2 * n; ++k) { LAS char* a = smem + b4 + 16384 * (k >> 1) + 4096 * (k & 1); w4f[2 * u0 + k] = tr2(a, a + 1024); }
  };
  auto w4_sum = [&](int u0, int n) {
#pragma unroll
    for (int k = 0; k < 2 * n; ++k) cw = mfma32(w4f[2 * u0 + k], ones, cw);
    asm volatile("" : "+v"(cw));
  };

  u32x4 phq_pos, phq_neg;
  u32x4 w3f[16];
  const int grp = w >> 2, tg = w & 3;
  const int myMt = 2 * tg + grp;
  {
    const int rot = (2 * (rr & 3) + (rr >> 2)) % 7;
    const u32 win0 = (56 * h) & ~15;
    u32 vx0, vx1, vw[2], v3;
    {
      const int r8 = lane >> 3, pos = lane & 7;
      vx0 = (u32)((m * 128 + w * 16 + r8) * 896 + 16 * (pos ^ (lane >> 4)));
      vx1 = (u32)((m * 128 + w * 16 + 8 + r8) * 896 + 16 * (pos ^ (4 + (lane >> 4))));
#pragma unroll
      for (int k = 0; k < 2; ++k) {
        const int j = w + 8 * k, G = j / 3, rl = 4 * (j - 3 * G) + g, rq = rl / 3, sec = rl - 3 * rq;
        vw[k] = (u32)((16 * G + 4 * rq + q) * 2688 + sec * 896 + win0 + 16 * (p ^ (2 * (rq >> 1))));
      }
      v3 = (u32)(r8 * 896 + 32 * (((lane >> 1) & 3) ^ ((lane >> 4) & 3)) + 16 * (lane & 1) + 128 * cs);
    }
    asm volatile("" : "+v"(vx0), "+v"(vx1), "+v"(vw[0]), "+v"(vw[1]), "+v"(v3));
    asm volatile("" ::"s"(x), "s"(wqkv), "s"(wphi), "s"(wo), "s"(wg), "s"(wu), "s"(wd), "s"(out), "s"(ws_));
    auto issue_chunk = [&](int i, u32 slot) {
      int c = rot + i; c = c >= 7 ? c - 7 : c;
      dma16(dX, slot + w * 2048, vx0, c * 128);
      dma16(dW1, slot + 16384 + w * 1024, vw[0], c * 64 * 2688);
      dma16(dX, slot + w * 2048 + 1024, vx1, c * 128);
      if (w < 4) dma16(dW1, slot + 24576 + w * 1024, vw[1], c * 64 * 2688);
    };
    auto w3_at = [&](int hd) { return (u32)(hd < 7 ? 86016 + 4096 * hd : hd < 14 ? 4096 * (hd - 7) : 139264 + 4096 * (hd - 14)); };
    auto w3_issue = [&](int k0) {
#pragma unroll
      for (int k = k0; k < k0 + 4; ++k) dma16(dW3, (w < 4 ? w3_at(2 * k) : w3_at(2 * k + 1)) + (w & 3) * 1024, v3, (28 * (2 * k + (w >> 2)) + 8 * (w & 3)) * 896);
    };
    auto ex = [&](int k) {
      __builtin_amdgcn_sched_barrier(0);
      if (k < 7 && w == k) {
        const int l2 = 56 * k + (lane < 56 ? lane : 0), up = l2 >= 196;
        const int idx = rr * 196 + l2 - 196 * up;
        const u32 vo = (u32)((idx / 14) * 3584 + (xc & 1) * 1792 + (idx % 14) * 128);
        if (k <= 3) touch(dG, up ? 0x40000000u : vo);
        if (k >= 3) touch(dU, up ? vo : 0x40000000u);
      }
      __builtin_amdgcn_sched_barrier(0);
    };
    if (w == 7) {
      const u32x4 dP = mkd(wphi, 16 * 784 * 2);
      dma16(dP, L_WPHI, lane * 16, h * 1568); dma16(dP, L_WPHI + 1024, lane * 16, h * 1568 + 1024);
    }
    issue_chunk(0, 0); RAWBAR();
    issue_chunk(1, SLOT);
    {
      const GAS char* ep = ws + WS_EPOCH;
      const GAS char* xp = (const GAS char*)x + (((mt0 + 4 * mp) * 16 + c16) * D_ + 64 * cs + 16 * nt + 4 * g) * 2;
      asm volatile("global_load_dwordx2 %0, %3, off sc1\n\tglobal_load_dwordx2 %1, %4, off\n\tglobal_load_dwordx2 %2, %5, off"
                   : "=&v"(tag), "=&v"(xra), "=&v"(xrb) : "v"(ep), "v"(xp), "v"(xp + 16 * D_ * 2) : "memory");
    }
    if (w == 6) {
      u64 pc; u32 len;
      asm volatile("s_getpc_b64 %0\n.Lcp_start:\n\ts_mov_b32 %1, .Lcp_end-.Lcp_start" : "=s"(pc), "=s"(len));
      const u32x4 dC = {(u32)pc, (u32)(pc >> 32) & 0xffffu, len, 0x00020000u};
      touch(dC, (u32)(((rr & 7) * 64 + lane) * 128));
    }
    u32 wb[3];
#pragma unroll
    for (int dt = 0; dt < 2; ++dt) {
      u32 u = 8 * (h & 1) + 32 * dt + 8 * p; if (u >= 64) u = 56;
      wb[dt] = 16384 + 1536 * g + 64 * q + (u ^ (32 * (g & 1)));
    }
    wb[2] = grp ? wb[1] : wb[0];
    wb[0] += 256 * (1 + grp); wb[1] += 256 * (1 + grp);
    const u32 xb = 128 * c16 + 16 * (g ^ ((c16 >> 1) & 7)) + myMt * 2048;
    u32x4 wpf[2];
    f32x4 A[3] = {z4, z4, z4}, B[3] = {z4, z4, z4}, C = z4;
    f32x4 S1 = z4, S2 = z4;
    u32x4 x0[2][2], x1[2][2], wf[2][2][3];
#pragma unroll
    for (int i = 0; i < 8; ++i) {
      if (i < 7) {
        if (i < 2) WAITV(6); else if (i < 6) WAITV(3); else WAITV(4);
        BARRIER();
      }
      if (i <= 4 && grp) issue_chunk(i + 2, ((i + 2) & 3) * SLOT);
      if ((i == 5 || i == 6) && grp) w3_issue(4 * (i - 5));
      __builtin_amdgcn_sched_barrier(0);
      if (i < 7) {
        LAS char* sl = smem + (i & 3) * SLOT;
#pragma unroll
        for (int e = 0; e < 2; ++e) {
          x0[i & 1][e] = *(LAS u32x4*)(sl + (xb ^ (64 * e)));
#pragma unroll
          for (int n = 0; n < 3; ++n) wf[i & 1][e][n] = tr2(sl + wb[n] + 6144 * e, sl + wb[n] + 6144 * e + 768);
          x1[i & 1][e] = *(LAS u32x4*)(sl + (xb ^ (64 * e) ^ 2048));
        }
        __builtin_amdgcn_sched_barrier(0);
      }
      if (i > 0) {
        const int c = (i - 1) & 1;
#pragma unroll
        for (int e = 0; e < 2; ++e) {
#pragma unroll
          for (int n = 0; n < 3; ++n) { A[n] = mfma(wf[c][e][n], x0[c][e], A[n]); B[n] = mfma(wf[c][e][n], x1[c][e], B[n]); }
          S1 = mfma(ones, x0[c][e], S1);
          S2 = mfma(x0[c][e], x0[c][e], S2);
        }
        asm volatile("" : "+v"(A[0]), "+v"(A[1]), "+v"(A[2]), "+v"(B[0]), "+v"(B[1]), "+v"(B[2]), "+v"(S1), "+v"(S2));
#pragma unroll
        for (int n = 0; n < 3; ++n)
          if (tg == n) { C = mfma(wf[c][0][n], ones, C); C = mfma(wf[c][1][n], ones, C); }
      }
      __builtin_amdgcn_sched_barrier(0);
      if (i <= 4 && !grp) issue_chunk(i + 2, ((i + 2) & 3) * SLOT);
      if ((i == 5 || i == 6) && !grp) w3_issue(4 * (i - 5));
      if (i == 6) ex(0);
      if (i == 7) ex(1);
      __builtin_amdgcn_sched_barrier(0);
      if (i == 0) {
        LAS char* a = smem + L_WPHI + 56 * (4 * g + q) + 8 * p;
        wpf[0] = tr2(a, a + 896); wpf[1] = tr2(a + 32, a + 32 + 896);
        asm volatile("" : "+v"(wpf[0]), "+v"(wpf[1]));
        const u32 mg = g3 ? 0u : ~0u, mf = c16 >= 12 ? 0u : ~0u;
        wpf[0][2] &= mg; wpf[0][3] &= mg; wpf[1][0] &= mf; wpf[1][1] &= mf; wpf[1][2] &= mg & mf; wpf[1][3] &= mg & mf;
        asm volatile("" : "+v"(wpf[0]), "+v"(wpf[1]));
      }
      if (i == 4) asm volatile("" : "+v"(tag), "+v"(xra), "+v"(xrb));
    }
    tag += 1;
    {
      LAS f32x4* rp = (LAS f32x4*)(smem + L_RAW + w * 3072) + lane;
      rp[0] = B[0]; rp[64] = B[1]; rp[128] = B[2];
      if (tg < 3 && c16 == 0) *(LAS f32x4*)(smem + L_CS + (tg < 2 ? 2 * grp + tg : 4 + grp) * 64 + 16 * g) = C;
    }
    ex(2);
    const float mean = S1[0] * (1.f / D_);
    const float dg = p == 0 ? S2[0] : p == 1 ? S2[1] : p == 2 ? S2[2] : S2[3];
    const float rstd = __builtin_amdgcn_rsqf(fmaxf(gsum(g == q ? dg : 0.f) * (1.f / D_) - mean * mean, 0.f) + 1e-5f);
    BARRIER();
    LAS f32x4* cp = (LAS f32x4*)(smem + L_CS + 16 * g);
    f32x4 r2;
    {
      LAS f32x4* rp = (LAS f32x4*)(smem + L_RAW + (w ^ 4) * 3072) + lane;
      f32x4 r0 = rp[0], r1 = rp[64];
      r2 = rp[128];
      ex(3);
      f32x4 acc[4] = {grp ? r0 : A[0], grp ? r1 : A[1], grp ? A[0] : r0, grp ? A[1] : r1};
#pragma unroll
      for (int n = 0; n < 4; ++n) {
        const f32x4 c = cp[4 * n];
#pragma unroll
        for (int r = 0; r < 4; ++r) acc[n][r] = rstd * (acc[n][r] - mean * c[r]);
      }
      u32x4 kf = {pk(acc[0][0], acc[0][1]), pk(acc[0][2], acc[0][3]), g3 ? 0u : pk(acc[1][0], acc[1][1]), g3 ? 0u : pk(acc[1][2], acc[1][3])};
      f32x4 p0 = mfma(wpf[0], kf, z4), p1 = mfma(wpf[1], kf, z4);
      ex(4);
      LAS char* vr = smem + L_V + 64 * (16 * myMt + c16) + 8 * g;
      const u32 vs = (c16 >> 3) & 1;
      *(LAS u32x2*)(vr + 32 * (0 ^ vs)) = u32x2{pk(acc[2][0], acc[2][1]), pk(acc[2][2], acc[2][3])};
      *(LAS u32x2*)(vr + 32 * (1 ^ vs)) = g3 ? u32x2{0x00003f80u, 0u} : u32x2{pk(acc[3][0], acc[3][1]), pk(acc[3][2], acc[3][3])};
      u32x4 pos, neg;
      dual_softmax(p0, p1, g3, pos, neg, [&](int k) { ex(5 + k); });
      const u32 rowsw = ((c16 >> 1) & 1) | (((c16 >> 3) & 1) << 1);
      LAS char* pr = smem + L_PHI + 128 * (16 * myMt + c16) + 8 * g;
      *(LAS u32x2*)(pr + 32 * (0 ^ rowsw)) = u32x2{pos[0], pos[1]};
      *(LAS u32x2*)(pr + 32 * (1 ^ rowsw)) = u32x2{pos[2], pos[3]};
      *(LAS u32x2*)(pr + 32 * (2 ^ rowsw)) = u32x2{neg[0], neg[1]};
      *(LAS u32x2*)(pr + 32 * (3 ^ rowsw)) = u32x2{neg[2], neg[3]};
    }
    BARRIER();
    {
      LAS char* ap = smem + L_PHI + 1024 * g + 128 * q + 32 * ((u32)(w >> 1) ^ phi_sw) + 8 * p;
      LAS char* bp = smem + L_V + 512 * g + 64 * q + 32 * ((w & 1) ^ (g & 1)) + 8 * p;
      f32x4 kv = z4;
#pragma unroll
      for (int s = 0; s < 4; ++s) kv = mfma(tr2(ap + 4096 * s, ap + 4096 * s + 512), tr2(bp + 2048 * s, bp + 2048 * s + 256), kv);
      __builtin_amdgcn_raw_buffer_store_b64(u32x2{pk(kv[0], kv[1]), pk(kv[2], kv[3])}, rws, WS_KVP + ((h * 16 + m) * 8 + w) * 512 + lane * 8, 0, 16);
    }
    WAITV0();
    BARRIER();
    if (tid == 448) __hip_atomic_store((GAS u64*)(ws + WS_FL1) + h * 16 + m, tag, RLX);
    {
      LAS char* bp = smem + 128 * (4 * g + q) + 32 * ((u32)nt ^ phi_sw) + 8 * p;
#pragma unroll
      for (int s = 0; s < 16; ++s) w3f[s] = tr2(bp + w3_at(s), bp + w3_at(s) + 2048);
    }
    BARRIER();
    {
      auto gd = [&](int u) {
        __builtin_amdgcn_sched_barrier(0);
        if (u < NF && worker) w4_issue(u, u);
        if (u >= 3 && u + 2 < NF && worker) w4_issue(u + 2, u + 2);
        __builtin_amdgcn_sched_barrier(0);
      };
      gd(0);
      f32x4 q0 = grp ? r2 : A[2], q1 = grp ? A[2] : r2;
      const f32x4 c0 = cp[16], c1 = cp[20];
#pragma unroll
      for (int r = 0; r < 4; ++r) { q0[r] = rstd * (q0[r] - mean * c0[r]); q1[r] = rstd * (q1[r] - mean * c1[r]); }
      u32x4 qf = {pk(q0[0], q0[1]), pk(q0[2], q0[3]), g3 ? 0u : pk(q1[0], q1[1]), g3 ? 0u : pk(q1[2], q1[3])};
      f32x4 p0 = mfma(wpf[0], qf, z4), p1 = mfma(wpf[1], qf, z4);
      gd(1);
      dual_softmax(p0, p1, g3, phq_pos, phq_neg, [&](int k) { gd(2 + k); });
    }
  }
  {
    if (w == 7) wait_flags((GAS u64*)(ws + WS_FL1) + h * 16, 16, tag, lane, dead);
    BARRIER();
    {
      u32x4 pt[8];
      const u32 vo = (lane & 31) * 16 + (lane >> 5) * 4096;
#pragma unroll
      for (int i = 0; i < 8; ++i) pt[i] = ld16c(rws, vo, WS_KVP + ((h * 16 + 2 * ((i + m) & 7)) * 8 + w) * 512);
      float kv[8];
#pragma unroll
      for (int k = 0; k < 4; ++k) { kv[2 * k] = bflo(pt[0][k]); kv[2 * k + 1] = bfhi(pt[0][k]); }
#pragma unroll
      for (int i = 1; i < 8; ++i)
#pragma unroll
        for (int k = 0; k < 4; ++k) { kv[2 * k] += bflo(pt[i][k]); kv[2 * k + 1] += bfhi(pt[i][k]); }
      float r[4];
#pragma unroll
      for (int k = 0; k < 4; ++k) {
        auto sw = __builtin_amdgcn_permlane32_swap(__float_as_uint(kv[k]), __float_as_uint(kv[4 + k]), false, false);
        r[k] = __uint_as_float(sw[0]) + __uint_as_float(sw[1]);
      }
      const int fl = 2 * (lane & 31) + (lane >> 5);
      *(LAS u32x2*)(smem + L_KVF + (((w >> 2) * 2 + (w & 1)) * 64 + fl) * 16 + 8 * ((w >> 1) & 1)) = u32x2{pk(r[0], r[1]), pk(r[2], r[3])};
    }
    WAITV0(); BARRIER();
    f32x4 o0 = z4, o1 = z4;
    LAS u32x4* kf = (LAS u32x4*)(smem + L_KVF) + lane;
    o0 = mfma(kf[0], phq_pos, o0); o1 = mfma(kf[64], phq_pos, o1);
    o0 = mfma(kf[128], phq_neg, o0); o1 = mfma(kf[192], phq_neg, o1);
    float t0, den = o1[0];
    x32(den, t0); x16(t0, den);
    const float inv = __builtin_amdgcn_rcpf(den + 1e-6f);
    u32x4 of = {pk(o0[0] * inv, o0[1] * inv), pk(o0[2] * inv, o0[3] * inv), g3 ? 0u : pk(o1[0] * inv, o1[1] * inv), g3 ? 0u : pk(o1[2] * inv, o1[3] * inv)};
    st16c(rws, of, lane * 16, WS_O + ((m * 8 + myMt) * 16 + h) * 1024);
    if (worker) w4_read(0, NF);
    WAITV0(); BARRIER();
    if (tid == 448) __hip_atomic_store((GAS u64*)(ws + WS_FL2) + m * 16 + h, tag, RLX);
  }
  if (!worker) {
#pragma unroll 1
    for (int k = 0; k < 7; ++k) {
      const int r = (rr - 28) * 64 + lane;
      if (w < 7) touch(dD, (u32)((224 * (r >> 5) + 32 * k + (r & 31)) * 896 + w * 128));
    }
    WAITV0();
    return;
  }
#pragma unroll
  for (int i = 0; i < N2; ++i) w4_issue(NF + i, i);
  w4_sum(0, NF);
  f32x4 hA, hB;
  {
    if (w == 7) wait_flags((GAS u64*)(ws + WS_FL2) + (t64 >> 1) * 16, 16, tag, lane, dead);
    BARRIER();
    if (b == 0 && tid == 448) __hip_atomic_store((GAS u64*)(ws + WS_EPOCH), tag, RLX);
    {
      const int rb = (w + c7) & 7;
#pragma unroll
      for (int i = 0; i < 8; ++i) {
        const int tl = i & 3, hd = 8 * (i >> 2) + rb;
        dma16c(dWS, 81920 + (16 * tl + hd) * 1024, lane * 16, WS_O + ((mt0 + (tl & 1) + 4 * (tl >> 1)) * 16 + hd) * 1024);
      }
    }
    hA = f32x4{bflo(xra[0]), bfhi(xra[0]), bflo(xra[1]), bfhi(xra[1])};
    hB = f32x4{bflo(xrb[0]), bfhi(xrb[0]), bflo(xrb[1]), bfhi(xrb[1])};
    LAS char* ap = smem + 81920 + mp * 32768 + lane * 16;
#pragma unroll
    for (int hf = 0; hf < 2; ++hf) {
      if (hf == 0) WAITV(4); else WAITV(0);
      RAWBAR();
      u32x4 xa[8], xb2[8];
#pragma unroll
      for (int s = 0; s < 8; ++s) { xa[s] = *(LAS u32x4*)(ap + 1024 * (8 * hf + s)); xb2[s] = *(LAS u32x4*)(ap + 16384 + 1024 * (8 * hf + s)); }
      __builtin_amdgcn_sched_barrier(0);
#pragma unroll
      for (int s = 0; s < 8; ++s) { hA = mfma(w3f[8 * hf + s], xa[s], hA); hB = mfma(w3f[8 * hf + s], xb2[s], hB); }
      __builtin_amdgcn_sched_barrier(0);
    }
    const u32 a0 = pk(hA[0], hA[1]), a1 = pk(hA[2], hA[3]), b0 = pk(hB[0], hB[1]), b1 = pk(hB[2], hB[3]);
    u32x4 hv = pair16(a0, a1, b0, b1);
    st16c(rws, hv, (g & 1) * 14336 + (2 * (nt & 1) + (g >> 1)) * 256 + c16 * 16,
          WS_H + ((mt0 + 4 * mp) * 14 + 2 * cs + (nt >> 1)) * 1024);
    {
      float sA = gsum(dot2(a0, 0x3f803f80u, dot2(a1, 0x3f803f80u, 0.f))), qA = gsum(dot2(a0, a0, dot2(a1, a1, 0.f)));
      float sB = gsum(dot2(b0, 0x3f803f80u, dot2(b1, 0x3f803f80u, 0.f))), qB = gsum(dot2(b0, b0, dot2(b1, b1, 0.f)));
      if (g < 2) *(LAS u32x2*)(smem + L_PS + ((mp * 32 + g * 16 + c16) * 4 + nt) * 8) =
          g ? u32x2{__float_as_uint(sB), __float_as_uint(qB)} : u32x2{__float_as_uint(sA), __float_as_uint(qA)};
    }
    BARRIER();
    if (w == 7) {
      LAS f32x4* pp = (LAS f32x4*)(smem + L_PS + lane * 32);
      f32x4 u = pp[0], v = pp[1];
      u32x2 o2 = {__float_as_uint((u[0] + u[2]) + (v[0] + v[2])), __float_as_uint((u[1] + u[3]) + (v[1] + v[3]))};
      __builtin_amdgcn_raw_buffer_store_b64(o2, rws, WS_ST + (cs * 2048 + mt0 * 16 + (lane >> 5) * 64 + (lane & 31)) * 8, 0, 16);
    }
    w4_read(NF, N2);
    WAITV0(); BARRIER();
    if (tid == 448) __hip_atomic_store((GAS u64*)(ws + WS_FL3) + (t64 >> 1) * 16 + (t64 & 1) * 7 + cs, tag, RLX);
  }
  u32 v5[2];
#pragma unroll
  for (int v = 0; v < 2; ++v) v5[v] = (lane >> 3) * 896 + 32 * (((lane >> 1) & 3) ^ (((lane >> 4) & 1) | (v << 1))) + 16 * (lane & 1) + 128 * cs;
  const u32 b5 = 1024 * g + 128 * q + 8 * p;
  const u32 STG = 114688 + 4096 * w;
  u32x4 w5f[7][4];
  auto w5_issue = [&](int ks, u32 dst) {
#pragma unroll
    for (int i = 0; i < 4; ++i) dma16(dD, dst + i * 1024, v5[i & 1], (u32)(224 * w + 32 * ks + 8 * i) * 896);
  };
  auto w5_read = [&](u32x4* f, u32 src) {
#pragma unroll
    for (int n = 0; n < 4; ++n) { LAS char* a = smem + src + b5 + 32 * ((u32)n ^ phi_sw); f[n] = tr2(a, a + 512); }
  };
  auto w5_home = [&](int pair) { return w < 7 ? (u32)(28672 * pair + 4096 * w) : (u32)(L_MISC + 4096 * pair); };
#pragma unroll
  for (int i = 0; i < 14 - NF - N2; ++i) w4_issue(NF + N2 + i, i);
  w5_issue(0, STG);
  w4_sum(NF, N2);
  {
    WAITV0(); BARRIER();
    w4_read(NF + N2, 14 - NF - N2);
    w5_read(w5f[0], STG);
    w4_sum(NF + N2, 14 - NF - N2);
    if (w == 7) wait_flags((GAS u64*)(ws + WS_FL3) + m4 * 16, 14, tag, lane, dead);
    BARRIER();
    const int rb4 = (w + c7) & 7;
    auto hdma = [&](int j, int k) {
      int blk = 8 * k + rb4; if (blk >= 28) blk -= 8;
      dma16c(dWS, (28 * j + blk) * 1024, lane * 16, WS_H + (m4 * 112 + 28 * j + blk) * 1024);
    };
    {
      u32x2 s0, s1;
      {
        const u32 vo = WS_ST + (u32)(g * 2048 + m4 * 128 + w * 16 + c16) * 8;
        const u32 vo1 = g3 ? vo : vo + 4 * 2048 * 8;
        asm volatile("buffer_load_dwordx2 %0, %2, %4, 0 offen sc1\n\tbuffer_load_dwordx2 %1, %3, %4, 0 offen sc1"
                     : "=&v"(s0), "=&v"(s1) : "v"(vo), "v"(vo1), "s"(dWS) : "memory");
      }
#pragma unroll
      for (int k = 0; k < 8; ++k) hdma(k >> 2, k & 3);
      asm volatile("s_waitcnt vmcnt(8)" : "+v"(s0), "+v"(s1)::"memory");
      float sm = __uint_as_float(s0[0]) + (g3 ? 0.f : __uint_as_float(s1[0]));
      float sq = __uint_as_float(s0[1]) + (g3 ? 0.f : __uint_as_float(s1[1]));
      sm = gsum(sm); sq = gsum(sq);
      const float mean = sm * (1.f / D_);
      const float rstd = __builtin_amdgcn_rsqf(fmaxf(sq * (1.f / D_) - mean * mean, 0.f) + 1e-5f);
      if (g == 0) *(LAS u32x2*)(smem + L_ST + (w * 16 + c16) * 8) = u32x2{__float_as_uint(mean), __float_as_uint(rstd)};
    }
    WAITV(4); BARRIER();
    const int ftile = 8 * fs + w;
    const u32 svo = (g & 1) * 57344 + (2 * (ftile & 1) + (g >> 1)) * 256 + c16 * 16 + WS_HH + (m4 * 8 * 56 + (ftile >> 1)) * 1024;
    LAS char* ap = smem + (g & 1) * 14336 + (g >> 1) * 256 + c16 * 16;
    LAS char* sp = smem + L_ST + (lane & 31) * 8;
    auto xoff = [](int n) { return 28672 * (n / 28) + 1024 * ((n % 28) >> 1) + 512 * (n & 1); };
    u32x4 ring[4];
#pragma unroll
    for (int k = 0; k < 4; ++k) ring[k] = *(LAS u32x4*)(ap + xoff(k));
    auto rd = [&](int ks, int n) { LAS char* a = smem + STG + b5 + 32 * ((u32)n ^ phi_sw); w5f[ks][n] = tr2(a, a + 512); };
    auto is = [&](int ks, int i, u32 dst) { dma16(dD, dst + i * 1024, v5[i & 1], (u32)(224 * w + 32 * ks + 8 * i) * 896); };
    auto hook = [&](int T, int s) {
      if (T == 0) {
        if (s >= 1 && s < 9) hdma(2 + ((s - 1) >> 2), (s - 1) & 3);
        if (s >= 10 && s < 14) is(1, s - 10, STG);
        if (s == 20) { WAITV(12); RAWBAR(); }
      }
      if (T == 1) {
        if (s == 20) { WAITV(0); RAWBAR(); }
        if (s >= 21 && s < 25) { rd(1, s - 21); is(3, s - 21, w5_home(0)); }
      }
      if (T == 2) {
        if (s >= 2 && s < 6) is(2, s - 2, STG);
        if (s == 20) RAWBAR();
        if (s >= 21 && s < 25) is(4, s - 21, w5_home(1));
      }
      if (T == 3) {
        if (s == 4) { WAITV(5); RAWBAR(); }
        if (s >= 5 && s < 9) { rd(2, s - 5); is(5, s - 5, w5_home(2)); }
        if (s >= 14 && s < 18) is(6, s - 14, STG);
        if (s == 22) {
          WAITV(8); RAWBAR();
          if (tid == 448) __hip_atomic_store((GAS u64*)(ws + WS_FL4) + m4 * 16 + fs, tag, RLX);
        }
      }
    };
    f32x16 acc[2];
    u32x2 stt[2];
    float t[8], u[8], rn;
    auto epi = [&](const f32x16& E, u32x2 ST, int s, int tile) {
      const float mean = __uint_as_float(ST[0]), rstd = __uint_as_float(ST[1]);
      if (s == 0) rn = rstd * -1.4426950408889634f;
      if (s >= 1 && s <= 8) t[s - 1] = __builtin_amdgcn_exp2f(rn * fmaf(-mean, cw[s - 1], E[s - 1]));
      if (s >= 9 && s <= 16) { t[s - 9] = __builtin_amdgcn_rcpf(1.f + t[s - 9]); u[s - 9] = fmaf(-mean, cw[s - 1], E[s - 1]); }
      if (s >= 17 && s <= 24) u[s - 17] = (u[s - 17] * rstd) * t[s - 17];
      if (s == 26) {
        auto r0 = __builtin_amdgcn_permlane32_swap(pk(u[0], u[1]), pk(u[4], u[5]), false, false);
        auto r1 = __builtin_amdgcn_permlane32_swap(pk(u[2], u[3]), pk(u[6], u[7]), false, false);
        st16c(rws, u32x4{r0[0], r1[0], r0[1], r1[1]}, svo, tile * 2 * 56 * 1024);
      }
    };
#pragma unroll
    for (int T = 0; T < 4; ++T) {
      acc[T & 1] = f32x16{};
      stt[T & 1] = *(LAS u32x2*)(sp + 256 * T);
      __builtin_amdgcn_sched_barrier(0);
#pragma unroll
      for (int s = 0; s < 28; ++s) {
        const int n = 28 * T + s;
        acc[T & 1] = mfma32(w4f[s], ring[n & 3], acc[T & 1]);
        if (n + 4 < 112) ring[n & 3] = *(LAS u32x4*)(ap + xoff(n + 4));
        if (T > 0) epi(acc[(T - 1) & 1], stt[(T - 1) & 1], s, T - 1);
        hook(T, s);
        __builtin_amdgcn_sched_barrier(0);
      }
    }
#pragma unroll
    for (int s = 0; s < 27; ++s) epi(acc[1], stt[1], s, 3);
  }
  {
    if (w == 7) wait_flags((GAS u64*)(ws + WS_FL4) + (t64 >> 1) * 16, 14, tag, lane, dead);
    else WAITV(6);
    w5_read(w5f[3], w5_home(0)); w5_read(w5f[4], w5_home(1)); w5_read(w5f[5], w5_home(2));
    WAITV(0);
    w5_read(w5f[6], STG);
    BARRIER();
    if (tid == 448) __hip_atomic_store((GAS u64*)(ws + WS_FL5) + m4 * 16 + fs, tag, RLX);
    {
      u32x4 xf[4][7];
      const int r2 = c7 & 1;
      __builtin_amdgcn_sched_barrier(0);
#pragma unroll
      for (int k = 0; k < 14; ++k) xf[k / 7][k % 7] = ld16c(rws, lane * 16, WS_HH + ((mt0 + ((k / 7) ^ r2)) * 56 + 7 * w + k % 7) * 1024);
      __builtin_amdgcn_sched_barrier(0);
#pragma unroll
      for (int i = 0; i < 4; ++i) {
        const int mt = (i & 2) | ((i & 1) ^ r2);
        f32x4 a[4] = {z4, z4, z4, z4};
#pragma unroll
        for (int s = 0; s < 7; ++s)
#pragma unroll
          for (int n = 0; n < 4; ++n) a[n] = mfma(w5f[s][n], xf[i][s], a[n]);
        __builtin_amdgcn_sched_barrier(0);
        if (i == 0) {
          if (w == 7) wait_flags((GAS u64*)(ws + WS_FL5) + (t64 >> 1) * 16, 14, tag, lane, dead);
          RAWBAR();
#pragma unroll
          for (int k = 0; k < 14; ++k) xf[2 + k / 7][k % 7] = ld16c(rws, lane * 16, WS_HH + ((mt0 + 4 + ((k / 7) ^ r2)) * 56 + 7 * w + k % 7) * 1024);
        }
        __builtin_amdgcn_sched_barrier(0);
#pragma unroll
        for (int n = 0; n < 4; ++n) *(LAS f32x4*)(smem + (((n * 4 + mt) * 8 + w) * 64 + lane) * 16) = a[n];
      }
    }
    BARRIER();
    LAS f32x4* rp = (LAS f32x4*)(smem + (w * 2 * 8 * 64 + lane) * 16);
#pragma unroll
    for (int i = 0; i < 8; ++i) { hA += rp[i * 64]; hB += rp[512 + i * 64]; }
    u32x4 ov = pair16(pk(hA[0], hA[1]), pk(hA[2], hA[3]), pk(hB[0], hB[1]), pk(hB[2], hB[3]));
    GAS char* op = (GAS char*)out + (((mt0 + 4 * mp + (g & 1)) * 16 + c16) * D_ + 64 * cs + 16 * nt + 8 * (g >> 1)) * 2;
    *(GAS u32x4*)op = ov;
  }
  asm volatile(".Lcp_end:");
}

extern "C" void kernel_launch(void* const* d_in, const int* in_sizes, int n_in, void* d_out, int out_size, void* d_ws,
                              size_t ws_size, hipStream_t stream) {
  (void)hipFuncSetAttribute((const void*)fused_block, hipFuncAttributeMaxDynamicSharedMemorySize, LDS_BYTES);
  fused_block<<<256, 512, LDS_BYTES, stream>>>((const uint16_t*)d_in[0], (const uint16_t*)d_in[1], (const uint16_t*)d_in[2],
                                               (const uint16_t*)d_in[3], (const uint16_t*)d_in[5], (const uint16_t*)d_in[6],
                                               (const uint16_t*)d_in[7], (uint16_t*)d_out, (char*)d_ws);
}
```
